```python
import torch

def _softmax(x, axis=-1):
    return torch.softmax(x, dim=axis)
def _gelu(x):
    return torch.nn.functional.gelu(x, approximate="tanh")
def _silu(x):
    return torch.nn.functional.silu(x)
def _sigmoid(x):
    return torch.sigmoid(x)
def _layernorm(x, axis=-1, eps=1e-5):
    m = x.mean(dim=axis, keepdim=True)
    v = x.var(dim=axis, keepdim=True, unbiased=False)
    return (x - m) / torch.sqrt(v + eps)
def _scatter_add(tgt, idx, src, axis=0):
    return tgt.clone().index_add_(axis, idx, src)
def _segment_sum(src, seg, n):
    out = torch.zeros((n,) + src.shape[1:], dtype=src.dtype)
    return out.index_add_(0, seg, src)
def _pad(x, pw):
    flat = tuple(v for pair in reversed(pw) for v in pair)
    return torch.nn.functional.pad(x, flat)

kernel_name = 'parallel_sum__grouped_kv_mqa__mixer_channel_mlp__prompt'

def setup_inputs():

    _w = (lambda a: (lambda t: t.to(torch.bfloat16) if t.is_floating_point() else t)(torch.as_tensor(a)))
    def _A_setup():
        B, S, D, H = 1, 2048, 1024, 32
        Dh = D // H
        return {
            "x": _w((lambda *s: torch.randn(*s, dtype=torch.bfloat16) * 0.05)(B, S, D)),
            "wq": _w((lambda *s: torch.randn(*s, dtype=torch.bfloat16) * 0.05)(D, H * Dh)),
            "wk": _w((lambda *s: torch.randn(*s, dtype=torch.bfloat16) * 0.05)(D, Dh)),
            "wv": _w((lambda *s: torch.randn(*s, dtype=torch.bfloat16) * 0.05)(D, Dh)),
            "wo": _w((lambda *s: torch.randn(*s, dtype=torch.bfloat16) * 0.05)(H * Dh, D)),
        }
    _A_in = _A_setup()

    def _B_setup():
        B, S, D, F = 1, 2048, 1024, 3584
        return {
            "x": _w((lambda *s: torch.randn(*s, dtype=torch.bfloat16) * 0.05)(B, S, D)),
            "w1": _w((lambda *s: torch.randn(*s, dtype=torch.bfloat16) * 3.2)(D, F) * (D ** -0.5)),
            "b1": _w((lambda *s: torch.randn(*s, dtype=torch.bfloat16) * 1.6)(F)),
            "w2": _w((lambda *s: torch.randn(*s, dtype=torch.bfloat16) * 0.4)(F, D) * (F ** -0.5)),
            "b2": _w((lambda *s: torch.randn(*s, dtype=torch.bfloat16) * 0.8)(D)),
        }
    _B_in = _B_setup()

    return {
        "A_x": _A_in["x"],
        "A_wq": _A_in["wq"],
        "A_wk": _A_in["wk"],
        "A_wv": _A_in["wv"],
        "A_wo": _A_in["wo"],
        "B_x": _B_in["x"],
        "B_w1": _B_in["w1"],
        "B_b1": _B_in["b1"],
        "B_w2": _B_in["w2"],
        "B_b2": _B_in["b2"],
    }

def reference(A_x, A_wq, A_wk, A_wv, A_wo, B_x, B_w1, B_b1, B_w2, B_b2):
    def _A_ref(x, wq, wk, wv, wo):
        B, S, D = x.shape
        H = 32
        Dh = D // H
        q = (lambda x, *d: x.permute(*d))((x @ wq).reshape(B, S, H, Dh), 0, 2, 1, 3)
        k = x @ wk
        v = x @ wv
        scores = torch.einsum("bhqd,bkd->bhqk", q, k) * (Dh ** -0.5)
        p = _softmax(scores, axis=-1)
        o = torch.einsum("bhqk,bkd->bhqd", p, v)
        return (lambda x, *d: x.permute(*d))(o, 0, 2, 1, 3).reshape(B, S, H * Dh) @ wo

    def _B_ref(x, w1, b1, w2, b2):
        y = _layernorm(x, axis=-1)
        h = _gelu(torch.einsum("bsd,df->bsf", y, w1) + b1)
        return torch.einsum("bsf,fd->bsd", h, w2) + b2

    x = A_x
    xn = _layernorm(x, axis=-1)
    return x + _A_ref(xn, A_wq, A_wk, A_wv, A_wo) + _B_ref(xn, B_w1, B_b1, B_w2, B_b2)
```

```cpp
#include <hip/hip_runtime.h>
#include <hip/hip_bf16.h>
#include <stdint.h>
typedef unsigned short u16;
__device__ __forceinline__ float bf2f(u16 b){ return __uint_as_float(((unsigned)b)<<16); }
__device__ __forceinline__ u16 f2bf(float f){ unsigned u=__float_as_uint(f); u+=0x7FFFu+((u>>16)&1u); return (u16)(u>>16); }
__device__ __forceinline__ float rb(float f){ return bf2f(f2bf(f)); }

constexpr int S=2048,D=1024,H=32,DH=32,F=3584;

__global__ void ln_k(const u16* x,u16* xn,u16* y){
  __shared__ float red[256];
  int r=blockIdx.x,t=threadIdx.x;
  float v[4]; for(int i=0;i<4;i++) v[i]=bf2f(x[r*D+t*4+i]);
  for(int pass=0;pass<2;pass++){
    float s=v[0]+v[1]+v[2]+v[3]; red[t]=s; __syncthreads();
    for(int o=128;o>0;o>>=1){ if(t<o) red[t]+=red[t+o]; __syncthreads(); }
    float mean=red[0]/D; __syncthreads();
    float q=0; for(int i=0;i<4;i++){ float d=v[i]-mean; q+=d*d; } red[t]=q; __syncthreads();
    for(int o=128;o>0;o>>=1){ if(t<o) red[t]+=red[t+o]; __syncthreads(); }
    float var=red[0]/D; __syncthreads();
    float mb=rb(mean), vb=rb(var); float sd=rb(sqrtf(rb(vb+1e-5f)));
    for(int i=0;i<4;i++) v[i]=rb(rb(v[i]-mb)/sd);
    u16* o=pass?y:xn; for(int i=0;i<4;i++) o[r*D+t*4+i]=f2bf(v[i]);
  }
}
__global__ void gemm_k(const u16* A,int lda,const u16* B,int ldb,int K,float* C,int ldc,int N){
  __shared__ float As[16][65], Bs[16][65];
  int tx=threadIdx.x&15, ty=threadIdx.x>>4; int m0=blockIdx.y*64,n0=blockIdx.x*64;
  float acc[4][4]={};
  for(int k0=0;k0<K;k0+=16){
    for(int i=threadIdx.x;i<64*16;i+=256){ int m=i>>4,k=i&15; As[k][m]=bf2f(A[(size_t)(m0+m)*lda+k0+k]); }
    for(int i=threadIdx.x;i<64*16;i+=256){ int k=i>>6,n=i&63; Bs[k][n]=(n0+n<N)?bf2f(B[(size_t)(k0+k)*ldb+n0+n]):0.f; }
    __syncthreads();
    for(int k=0;k<16;k++){ float a[4],b[4]; for(int i=0;i<4;i++){a[i]=As[k][ty*4+i]; b[i]=Bs[k][tx*4+i];}
      for(int i=0;i<4;i++) for(int j=0;j<4;j++) acc[i][j]+=a[i]*b[j]; }
    __syncthreads();
  }
  for(int i=0;i<4;i++) for(int j=0;j<4;j++){ int n=n0+tx*4+j; if(n<N) C[(size_t)(m0+ty*4+i)*ldc+n]=acc[i][j]; }
}
__global__ void tobf_k(const float* in,u16* out,int n){ int i=blockIdx.x*256+threadIdx.x; if(i<n) out[i]=f2bf(in[i]); }
__global__ void gelu_k(const float* in,const u16* b1,u16* out){ size_t i=(size_t)blockIdx.x*256+threadIdx.x; int f=i%F; float v=rb(rb(in[i])+bf2f(b1[f]));
  float u=0.7978845608f*(v+0.044715f*v*v*v); float t=tanhf(u); out[i]=f2bf(0.5f*v*(1.f+t)); }
__global__ void attn_k(const u16* q,const u16* k,const u16* v,u16* o){
  int row=blockIdx.x*4+(threadIdx.x>>6); int lane=threadIdx.x&63; int pos=row/H,h=row%H;
  float qv[DH]; for(int d=0;d<DH;d++) qv[d]=bf2f(q[(size_t)pos*D+h*DH+d]);
  float sc[32]; float m=-1e30f;
  for(int i=0;i<32;i++){ int key=i*64+lane; float s=0; for(int d=0;d<DH;d++) s+=qv[d]*bf2f(k[key*DH+d]); s=rb(rb(s)*0.17677669529663687f); sc[i]=s; m=fmaxf(m,s);}
  for(int off=32;off>0;off>>=1) m=fmaxf(m,__shfl_xor(m,off));
  float l=0; for(int i=0;i<32;i++){ sc[i]=__expf(sc[i]-m); l+=sc[i]; }
  for(int off=32;off>0;off>>=1) l+=__shfl_xor(l,off);
  float acc[DH]; for(int d=0;d<DH;d++) acc[d]=0;
  for(int i=0;i<32;i++){ int key=i*64+lane; float p=rb(sc[i]/l); for(int d=0;d<DH;d++) acc[d]+=p*bf2f(v[key*DH+d]); }
  for(int d=0;d<DH;d++){ float a=acc[d]; for(int off=32;off>0;off>>=1) a+=__shfl_xor(a,off); if(lane==0) o[(size_t)pos*D+h*DH+d]=f2bf(a);}
}
__global__ void final_k(const u16* x,const float* a,const float* b,const u16* b2,u16* out){ int i=blockIdx.x*256+threadIdx.x; int c=i%D;
  float r=rb(bf2f(x[i])+rb(a[i])); float bb=rb(rb(b[i])+bf2f(b2[c])); out[i]=f2bf(r+bb); }

extern "C" void kernel_launch(void* const* d_in,const int* in_sizes,int n_in,void* d_out,int out_size,void* d_ws,size_t ws_size,hipStream_t st){
  const u16* x=(const u16*)d_in[0]; const u16* wq=(const u16*)d_in[1]; const u16* wk=(const u16*)d_in[2]; const u16* wv=(const u16*)d_in[3]; const u16* wo=(const u16*)d_in[4];
  const u16* w1=(const u16*)d_in[6]; const u16* b1=(const u16*)d_in[7]; const u16* w2=(const u16*)d_in[8]; const u16* b2=(const u16*)d_in[9];
  char* ws=(char*)d_ws;
  u16* xn=(u16*)ws; u16* y=(u16*)(ws+(4<<20)); u16* q=(u16*)(ws+(8<<20)); u16* k=(u16*)(ws+(12<<20)); u16* v=(u16*)(ws+(13<<20)); u16* o=(u16*)(ws+(14<<20));
  u16* hb=(u16*)(ws+(18<<20)); float* f0=(float*)(ws+(40<<20)); float* fa=(float*)(ws+(80<<20)); float* fb=(float*)(ws+(90<<20));
  ln_k<<<S,256,0,st>>>(x,xn,y);
  gemm_k<<<dim3(D/64,S/64),256,0,st>>>(xn,D,wq,D,D,f0,D,D); tobf_k<<<S*D/256,256,0,st>>>(f0,q,S*D);
  gemm_k<<<dim3(1,S/64),256,0,st>>>(xn,D,wk,DH,D,f0,DH,DH); tobf_k<<<S*DH/256,256,0,st>>>(f0,k,S*DH);
  gemm_k<<<dim3(1,S/64),256,0,st>>>(xn,D,wv,DH,D,f0,DH,DH); tobf_k<<<S*DH/256,256,0,st>>>(f0,v,S*DH);
  attn_k<<<S*H/4,256,0,st>>>(q,k,v,o);
  gemm_k<<<dim3(D/64,S/64),256,0,st>>>(o,D,wo,D,D,fa,D,D);
  gemm_k<<<dim3(F/64,S/64),256,0,st>>>(y,D,w1,F,D,f0,F,F); gelu_k<<<S*F/256,256,0,st>>>(f0,b1,hb);
  gemm_k<<<dim3(D/64,S/64),256,0,st>>>(hb,F,w2,D,F,fb,D,D);
  final_k<<<S*D/256,256,0,st>>>(x,fa,fb,b2,(u16*)d_out);
}
```

```cpp
#include <hip/hip_runtime.h>
#include <stdint.h>
typedef unsigned short u16;
typedef short bf16x8 __attribute__((ext_vector_type(8)));
typedef short s16x4 __attribute__((ext_vector_type(4)));
typedef float f32x16 __attribute__((ext_vector_type(16)));
typedef float f32x4 __attribute__((ext_vector_type(4)));
typedef float f32x2 __attribute__((ext_vector_type(2)));
typedef __bf16 bf16x2_t __attribute__((ext_vector_type(2)));
typedef unsigned u32x4 __attribute__((ext_vector_type(4)));
typedef unsigned u32x2 __attribute__((ext_vector_type(2)));
#define LAS __attribute__((address_space(3)))
#define GAS __attribute__((address_space(1)))
#define DI __device__ __forceinline__
#define MFMA32(a,b,c) __builtin_amdgcn_mfma_f32_32x32x16_bf16((a),(b),(c),0,0,0)
#define MFMA16(a,b,c) __builtin_amdgcn_mfma_f32_16x16x32_bf16((a),(b),(c),0,0,0)
#define SBAR() __builtin_amdgcn_sched_barrier(0)
#define PIN(x) asm volatile("":"+v"(x))
#define BF8(x) __builtin_bit_cast(bf16x8,x)
#define GLDS(g,l) __builtin_amdgcn_global_load_lds((const GAS unsigned*)(g),(LAS unsigned*)(l),16,0,0)
DI unsigned cvtpk(float lo,float hi){ f32x2 v={lo,hi}; bf16x2_t b=__builtin_convertvector(v,bf16x2_t); return __builtin_bit_cast(unsigned,b); }
DI float bflo(unsigned u){ return __uint_as_float(u<<16); }
DI float bfhi(unsigned u){ return __uint_as_float(u&0xffff0000u); }
DI float rbf(float f){ return bflo(cvtpk(f,0.f)); }
DI s16x4 trd(const LAS char* p){ return __builtin_amdgcn_ds_read_tr16_b64_v4i16((LAS s16x4*)p); }
DI bf16x8 cat(s16x4 a,s16x4 b){ return __builtin_shufflevector(a,b,0,1,2,3,4,5,6,7); }
DI void glds16(const void* g, unsigned lds_base){ unsigned sv; asm volatile("s_mov_b32 %0, m0\n\ts_mov_b32 m0, %2\n\ts_nop 0\n\tglobal_load_lds_dwordx4 %1, off\n\ts_mov_b32 m0, %0":"=&s"(sv):"v"(g),"s"(lds_base):"memory"); }

constexpr float QSCALE=0.17677669529663687f*1.4426950408889634f;
constexpr size_t WS_YT=0, WS_HT=4u<<20, WS_QT=20u<<20, WS_OT=24u<<20, WS_KV=28u<<20, WS_ST=29u<<20, WS_CS=WS_ST+65536, WS_PART=30u<<20, WS_FLAG=WS_CS+65536;

constexpr int K1_LDS=163840;
DI float wsum(float v){
  #pragma unroll
  for(int o=32;o>0;o>>=1) v+=__shfl_xor(v,o);
  return v; }
DI void ln16(const float (&v)[16],float (&o)[16],float& mb,float& sd){
  float s=0.f;
  #pragma unroll
  for(int i=0;i<16;i++) s+=v[i];
  const float mean=wsum(s)*(1.f/1024.f); float q=0.f;
  #pragma unroll
  for(int i=0;i<16;i++){ const float d=v[i]-mean; q+=d*d; }
  const float var=wsum(q)*(1.f/1024.f);
  mb=rbf(mean); sd=rbf(sqrtf(rbf(rbf(var)+1e-5f))); const float inv=1.f/sd;
  #pragma unroll
  for(int i=0;i<16;i++) o[i]=rbf(rbf(v[i]-mb)*inv);
}
__global__ __launch_bounds__(512,2) void k1_ln(const u16* __restrict__ x,const char* __restrict__ wk,const char* __restrict__ wv,const u16* __restrict__ wq,const char* __restrict__ w1,
                                               char* __restrict__ Yt,char* __restrict__ KV,float* __restrict__ rowstat,float* __restrict__ csq,unsigned* __restrict__ flags){
  extern __shared__ __attribute__((aligned(16))) char ldsg[];
  LAS char* lds=(LAS char*)ldsg;
  const int tid=threadIdx.x,lane=tid&63,wid=__builtin_amdgcn_readfirstlane(tid>>6),b=blockIdx.x;
  if(b<128){
    { const int pos=lane&7, rr=lane>>3;
      #pragma unroll
      for(int p=0;p<16;p++){ const int row=128*wid+8*p+rr; const int ch=pos^(((row>>1)&3)<<1); const char* src=(ch<4?wk:wv)+row*64+(ch&3)*16; GLDS(src,lds+(128*wid+8*p)*128); } }
    const int kb=lane>>2,g=lane&3;
    float xv[2][16];
    #pragma unroll
    for(int r=0;r<2;r++){ const u16* xr=x+(size_t)(16*b+2*wid+r)*1024;
      #pragma unroll
      for(int u=0;u<2;u++){ const u32x2 lo=*(const u32x2*)(xr+(kb+16*u)*32+4*g), hi=*(const u32x2*)(xr+(kb+16*u)*32+16+4*g);
        xv[r][8*u+0]=bflo(lo[0]); xv[r][8*u+1]=bfhi(lo[0]); xv[r][8*u+2]=bflo(lo[1]); xv[r][8*u+3]=bfhi(lo[1]);
        xv[r][8*u+4]=bflo(hi[0]); xv[r][8*u+5]=bfhi(hi[0]); xv[r][8*u+6]=bflo(hi[1]); xv[r][8*u+7]=bfhi(hi[1]); } }
    u32x4 yp[2][2];
    #pragma unroll
    for(int r=0;r<2;r++){ float xn[16],y[16],m1,s1,m2,s2; ln16(xv[r],xn,m1,s1); ln16(xn,y,m2,s2);
      const int c=2*wid+r;
      if(lane==0){ f32x2 st={s2*QSCALE,m2*QSCALE}; *(f32x2*)(rowstat+2*(16*b+c))=st; }
      #pragma unroll
      for(int u=0;u<2;u++){ u32x4 w; w[0]=cvtpk(xn[8*u],xn[8*u+1]); w[1]=cvtpk(xn[8*u+2],xn[8*u+3]); w[2]=cvtpk(xn[8*u+4],xn[8*u+5]); w[3]=cvtpk(xn[8*u+6],xn[8*u+7]);
        const int k2=kb+16*u; const int pos=(c^(g|((k2&1)<<2)))+16*g; *(LAS u32x4*)(lds+131072+k2*1024+pos*16)=w;
        yp[r][u][0]=cvtpk(y[8*u],y[8*u+1]); yp[r][u][1]=cvtpk(y[8*u+2],y[8*u+3]); yp[r][u][2]=cvtpk(y[8*u+4],y[8*u+5]); yp[r][u][3]=cvtpk(y[8*u+6],y[8*u+7]); } }
    asm volatile("s_waitcnt vmcnt(0) lgkmcnt(0)\n\ts_barrier":::"memory");
    f32x4 ak[2]={},av[2]={};
    { const int gg=lane>>4,c=lane&15,q=(lane&15)>>2,p=lane&3; const int sg=(2*gg+(q>>1))&3;
      #pragma unroll
      for(int i=0;i<4;i++){ const int k2=4*wid+i;
        const bf16x8 xa=*(const LAS bf16x8*)(lds+131072+k2*1024+((c^(gg|((k2&1)<<2)))+16*gg)*16);
        #pragma unroll
        for(int a=0;a<4;a++){ const LAS char* bp=lds+(k2*32+4*gg+q)*128+32*(a^sg)+8*p; const bf16x8 wf=cat(trd(bp),trd(bp+2048));
          if(a<2) ak[a]=MFMA16(wf,xa,ak[a]); else av[a-2]=MFMA16(xa,wf,av[a-2]); } } }
    asm volatile("s_waitcnt lgkmcnt(0)\n\ts_barrier":::"memory");
    *(LAS f32x4*)(lds+wid*4096+0*1024+lane*16)=ak[0]; *(LAS f32x4*)(lds+wid*4096+1024+lane*16)=ak[1];
    *(LAS f32x4*)(lds+wid*4096+2048+lane*16)=av[0]; *(LAS f32x4*)(lds+wid*4096+3072+lane*16)=av[1];
    #pragma unroll
    for(int r=0;r<2;r++){ const int c=2*wid+r;
      #pragma unroll
      for(int u=0;u<2;u++){ const int k2=kb+16*u; const int pos=(c^(g|((k2&1)<<2)))+16*g; *(LAS u32x4*)(lds+32768+k2*1024+pos*16)=yp[r][u]; } }
    asm volatile("s_waitcnt lgkmcnt(0)\n\ts_barrier":::"memory");
    { const int gg=lane>>4,c=lane&15;
      #pragma unroll
      for(int i=0;i<4;i++){ const int k2=4*wid+i; const u32x4 w=*(const LAS u32x4*)(lds+32768+k2*1024+((c^(gg|((k2&1)<<2)))+16*gg)*16); *(u32x4*)(Yt+((size_t)b*32+k2)*1024+lane*16)=w; }
      if(wid==0){
        f32x4 s0={},s1={};
        #pragma unroll
        for(int w=0;w<8;w++){ s0+=*(const LAS f32x4*)(lds+w*4096+lane*16); s1+=*(const LAS f32x4*)(lds+w*4096+1024+lane*16); }
        const int key=16*b+c; u32x4 o; o[0]=cvtpk(s0[0],s0[1]); o[1]=cvtpk(s0[2],s0[3]); o[2]=cvtpk(s1[0],s1[1]); o[3]=cvtpk(s1[2],s1[3]);
        *(u32x4*)(KV+(size_t)(key>>5)*4096+(gg>>1)*1024+((key&31)+32*(gg&1))*16)=o;
      } else if(wid<3){
        const int a=wid-1; f32x4 s0={};
        #pragma unroll
        for(int w=0;w<8;w++) s0+=*(const LAS f32x4*)(lds+w*4096+(2+a)*1024+lane*16);
        u32x2 o; o[0]=cvtpk(s0[0],s0[1]); o[1]=cvtpk(s0[2],s0[3]);
        *(u32x2*)(KV+(size_t)(b>>1)*4096+2048+(b&1)*1024+((c+16*a)+32*(gg&1))*16+8*(gg>>1))=o;
      } }
  } else if(b<144){
    const int cb=b-128, ch=tid&7, rg=tid>>3; float s[8]={};
    #pragma unroll
    for(int i=0;i<16;i++){ const u32x4 w=*(const u32x4*)(wq+(size_t)(i*64+rg)*1024+cb*64+ch*8);
      s[0]+=bflo(w[0]); s[1]+=bfhi(w[0]); s[2]+=bflo(w[1]); s[3]+=bfhi(w[1]); s[4]+=bflo(w[2]); s[5]+=bfhi(w[2]); s[6]+=bflo(w[3]); s[7]+=bfhi(w[3]); }
    LAS float* sm=(LAS float*)lds;
    #pragma unroll
    for(int i=0;i<8;i++) sm[rg*65+ch*8+i]=s[i];
    __syncthreads();
    if(tid<64){ float a=0.f; for(int r=0;r<64;r++) a+=sm[r*65+tid]; csq[cb*64+tid]=a; }
    if(b==128 && tid<256) flags[tid*16]=0u;
  } else {
    const char* p=w1+(size_t)(b-144)*65536+tid*16;
    #pragma unroll
    for(int i=0;i<8;i++){ u32x4 w=*(const u32x4*)(p+i*8192); asm volatile(""::"v"(w)); }
  }
}

constexpr int G1_SLOT=26624, G1_NS=6, G1_A=0, G1_B1=8192, G1_B2=8192+14336;
DI float gelu1(float v){ const float c0=-2.f*0.7978845608028654f*1.4426950408889634f, c1=c0*0.044715f;
  const float t=__builtin_fmaf(v*v,c1,c0); const float e=__builtin_amdgcn_exp2f(v*t); return v*__builtin_amdgcn_rcpf(1.f+e); }
template<int L> DI void g1_loader(const char* Yt,const char* W1,const char* Wq,int rbk,int cbk,LAS char* lds,int lane){
  constexpr int NB1=(L<2)?4:3, CNT=2+NB1+1;
  const char* aP=Yt+(size_t)(rbk*8+2*L)*32768+lane*16;
  unsigned vb1[NB1];
  #pragma unroll
  for(int i=0;i<NB1;i++){ int e=64*(L+4*i)+lane; int r=e/28,pos=e%28; int ch=pos^(2*((r>>2)&1)); vb1[i]=r*7168+ch*16; }
  unsigned vb2; { int r=8*L+(lane>>3),pos=lane&7; int ch=pos^(((r>>1)&3)<<1); vb2=r*2048+ch*16; }
  const char* b1P=W1+cbk*448; const char* b2P=Wq+cbk*128;
  auto issue=[&](int s){ LAS char* sl=lds+(s%G1_NS)*G1_SLOT;
    GLDS(aP+(size_t)s*1024,sl+G1_A+(2*L)*1024); GLDS(aP+32768+(size_t)s*1024,sl+G1_A+(2*L+1)*1024);
    const char* r1=b1P+(size_t)s*32*7168; const char* r2=b2P+(size_t)s*32*2048;
    #pragma unroll
    for(int i=0;i<NB1;i++) GLDS(r1+vb1[i],sl+G1_B1+(L+4*i)*1024);
    GLDS(r2+vb2,sl+G1_B2+L*1024); };
  #pragma unroll
  for(int s=0;s<5;s++) issue(s);
  asm volatile("s_waitcnt vmcnt(%0)\n\ts_barrier"::"i"(4*CNT):"memory");
  for(int j=0;j<27;j++){ asm volatile("s_waitcnt vmcnt(%0)\n\ts_barrier"::"i"(3*CNT):"memory"); issue(j+5); }
  asm volatile("s_waitcnt vmcnt(%0)\n\ts_barrier"::"i"(3*CNT):"memory");
  asm volatile("s_waitcnt vmcnt(%0)\n\ts_barrier"::"i"(2*CNT):"memory");
  asm volatile("s_waitcnt vmcnt(%0)\n\ts_barrier"::"i"(1*CNT):"memory");
  asm volatile("s_waitcnt vmcnt(0)\n\ts_barrier":::"memory");
}
template<int WC> DI void g1_compute(int wr,int rbk,int cbk,const LAS char* lds,int lane,const u16* b1,const float* rowstat,const float* csq,u16* Ht,u16* Qt){
  const int g=lane>>4,q=(lane&15)>>2,p=lane&3;
  int aA=G1_A+wr*4096+lane*16;
  int aE=G1_B1+(4*g+q)*448+32*(g&1)+8*p, aO=G1_B1+(4*g+q)*448-32*(g&1)+8*p;
  int a2[4]; { const int sg=(2*g+(q>>1))&3;
    #pragma unroll
    for(int a=0;a<4;a++) a2[a]=G1_B2+(4*g+q)*128+32*(a^sg)+8*p; }
  f32x4 acc[4][9];
  #pragma unroll
  for(int m=0;m<4;m++)
    #pragma unroll
    for(int n=0;n<9;n++) acc[m][n]=f32x4{0,0,0,0};
  bf16x8 A[4],B[9];
  auto ldB=[&](int n)->bf16x8{
    if(WC==0){ const int a=n; const int base=((a&1)?aO:aE)+32*a; return cat(trd(lds+base),trd(lds+base+7168)); }
    else { if(n<4){ const int a=10+n; const int base=((a&1)?aO:aE)+32*a; return cat(trd(lds+base),trd(lds+base+7168)); }
           else if(n<8){ const int base=a2[n-4]; return cat(trd(lds+base),trd(lds+base+2048)); }
           else { const int base=aO+32*9; return cat(trd(lds+base),trd(lds+base+7168)); } } };
  asm volatile("s_barrier":::"memory");
  A[0]=*(const LAS bf16x8*)(lds+aA); A[1]=*(const LAS bf16x8*)(lds+aA+1024);
  #pragma unroll
  for(int n=0;n<9;n++) B[n]=ldB(n);
  int slot=0;
  #define G1STEP(PF) do{ \
    A[2]=*(const LAS bf16x8*)(lds+aA+2048); A[3]=*(const LAS bf16x8*)(lds+aA+3072); \
    { int d=(slot==G1_NS-1)?-(G1_NS-1)*G1_SLOT:G1_SLOT; slot=(slot==G1_NS-1)?0:slot+1; aA+=d; aE+=d; aO+=d; if(WC==1){ a2[0]+=d; a2[1]+=d; a2[2]+=d; a2[3]+=d; } } \
    SBAR(); \
    _Pragma("unroll") for(int n=0;n<9;n++){ acc[0][n]=MFMA16(B[n],A[0],acc[0][n]); acc[1][n]=MFMA16(B[n],A[1],acc[1][n]); } \
    SBAR(); \
    if(PF){ asm volatile("s_barrier":::"memory"); A[0]=*(const LAS bf16x8*)(lds+aA); A[1]=*(const LAS bf16x8*)(lds+aA+1024); } \
    SBAR(); \
    _Pragma("unroll") for(int n=0;n<9;n++){ acc[2][n]=MFMA16(B[n],A[2],acc[2][n]); acc[3][n]=MFMA16(B[n],A[3],acc[3][n]); \
      if(PF) B[n]=ldB(n); SBAR(); } \
    }while(0)
  for(int j=0;j<31;j++){ G1STEP(true); }
  G1STEP(false);
  #undef G1STEP
  { int tz=threadIdx.x; asm volatile("":"+v"(tz)); lane=tz&63; }
  const int gE=lane>>4;
  const int rb0=rbk*8+wr*4;
  if(WC==0){
    #pragma unroll
    for(int pr=0;pr<5;pr++){
      const int col=cbk*224+pr*32+4*gE;
      const u32x2 bl=*(const u32x2*)(b1+col); u32x2 bh={0,0}; if(pr<4) bh=*(const u32x2*)(b1+col+16);
      const float bb[8]={bflo(bl[0]),bfhi(bl[0]),bflo(bl[1]),bfhi(bl[1]),bflo(bh[0]),bfhi(bh[0]),bflo(bh[1]),bfhi(bh[1])};
      #pragma unroll
      for(int m=0;m<4;m++){
        u16* dst=Ht+((size_t)(rb0+m)*112+cbk*7+pr)*512+lane*8;
        const f32x4 x=acc[m][2*pr];
        u32x2 w0; w0[0]=cvtpk(gelu1(x[0]+bb[0]),gelu1(x[1]+bb[1])); w0[1]=cvtpk(gelu1(x[2]+bb[2]),gelu1(x[3]+bb[3]));
        if(pr<4){ const f32x4 y=acc[m][2*pr+1]; u32x4 w; w[0]=w0[0]; w[1]=w0[1]; w[2]=cvtpk(gelu1(y[0]+bb[4]),gelu1(y[1]+bb[5])); w[3]=cvtpk(gelu1(y[2]+bb[6]),gelu1(y[3]+bb[7])); *(u32x4*)dst=w; }
        else *(u32x2*)dst=w0; } }
  } else {
    #pragma unroll
    for(int pr=0;pr<3;pr++){
      const int pair=(pr==0)?4:(4+pr);
      const int col=cbk*224+pair*32+4*gE;
      u32x2 bl={0,0}; if(pr>0) bl=*(const u32x2*)(b1+col); const u32x2 bh=*(const u32x2*)(b1+col+16);
      const float bb[8]={bflo(bl[0]),bfhi(bl[0]),bflo(bl[1]),bfhi(bl[1]),bflo(bh[0]),bfhi(bh[0]),bflo(bh[1]),bfhi(bh[1])};
      #pragma unroll
      for(int m=0;m<4;m++){
        u16* dst=Ht+((size_t)(rb0+m)*112+cbk*7+pair)*512+lane*8;
        if(pr==0){ const f32x4 y=acc[m][8]; u32x2 w; w[0]=cvtpk(gelu1(y[0]+bb[4]),gelu1(y[1]+bb[5])); w[1]=cvtpk(gelu1(y[2]+bb[6]),gelu1(y[3]+bb[7])); *(u32x2*)(dst+4)=w; }
        else { const f32x4 x=acc[m][2*(pr-1)], y=acc[m][2*(pr-1)+1]; u32x4 w;
          w[0]=cvtpk(gelu1(x[0]+bb[0]),gelu1(x[1]+bb[1])); w[1]=cvtpk(gelu1(x[2]+bb[2]),gelu1(x[3]+bb[3])); w[2]=cvtpk(gelu1(y[0]+bb[4]),gelu1(y[1]+bb[5])); w[3]=cvtpk(gelu1(y[2]+bb[6]),gelu1(y[3]+bb[7])); *(u32x4*)dst=w; } } }
    #pragma unroll
    for(int hh=0;hh<2;hh++){
      const int head=2*cbk+hh; const int col=head*32+4*gE;
      const f32x4 c0=*(const f32x4*)(csq+col), c1=*(const f32x4*)(csq+col+16);
      #pragma unroll
      for(int m=0;m<4;m++){
        const int row=(rb0+m)*16+(lane&15);
        const f32x2 st=*(const f32x2*)(rowstat+2*row);
        const f32x4 x=acc[m][4+2*hh], y=acc[m][5+2*hh]; u32x4 w;
        w[0]=cvtpk(x[0]*st[0]+c0[0]*st[1],x[1]*st[0]+c0[1]*st[1]); w[1]=cvtpk(x[2]*st[0]+c0[2]*st[1],x[3]*st[0]+c0[3]*st[1]);
        w[2]=cvtpk(y[0]*st[0]+c1[0]*st[1],y[1]*st[0]+c1[1]*st[1]); w[3]=cvtpk(y[2]*st[0]+c1[2]*st[1],y[3]*st[0]+c1[3]*st[1]);
        u16* dst=Qt+((((size_t)head*64+(row>>5))*2+(gE>>1))*64+((row&31)+32*(gE&1)))*8;
        *(u32x4*)dst=w; } }
  }
}
__global__ __launch_bounds__(512,2) void k2_gemm1(const char* __restrict__ Yt,const char* __restrict__ W1,const char* __restrict__ Wq,const u16* __restrict__ b1,
    const float* __restrict__ rowstat,const float* __restrict__ csq,u16* __restrict__ Ht,u16* __restrict__ Qt){
  extern __shared__ __attribute__((aligned(16))) char ldsg[];
  LAS char* lds=(LAS char*)ldsg;
  const int tid=threadIdx.x,lane=tid&63,wid=__builtin_amdgcn_readfirstlane(tid>>6);
  const int b=blockIdx.x,xcd=b&7,j=b>>3; const int rbk=(xcd&1)*8+(j&7),cbk=(xcd>>1)*4+(j>>3);
  if(wid>=4){
    if(wid==4) g1_loader<0>(Yt,W1,Wq,rbk,cbk,lds,lane); else if(wid==5) g1_loader<1>(Yt,W1,Wq,rbk,cbk,lds,lane);
    else if(wid==6) g1_loader<2>(Yt,W1,Wq,rbk,cbk,lds,lane); else g1_loader<3>(Yt,W1,Wq,rbk,cbk,lds,lane);
  } else {
    if(wid&1) g1_compute<1>(wid>>1,rbk,cbk,lds,lane,b1,rowstat,csq,Ht,Qt); else g1_compute<0>(wid>>1,rbk,cbk,lds,lane,b1,rowstat,csq,Ht,Qt);
  }
}

constexpr int AT_SLOT=16384, AT_NS=4;
DI void attn_slow(const bf16x8 q0,const bf16x8 q1,const char* __restrict__ KV,int lane,f32x16& o,float& l){
  float m=-INFINITY; l=0.f; o=f32x16{};
  #pragma unroll 1
  for(int t=0;t<64;t++){
    const char* p=KV+(size_t)t*4096+lane*16;
    const bf16x8 k0=*(const bf16x8*)p,k1=*(const bf16x8*)(p+1024),v0=*(const bf16x8*)(p+2048),v1=*(const bf16x8*)(p+3072);
    f32x16 s=MFMA32(k0,q0,f32x16{}); s=MFMA32(k1,q1,s);
    float mx=s[0];
    #pragma unroll
    for(int i=1;i<16;i++) mx=fmaxf(mx,s[i]);
    mx=fmaxf(mx,__shfl_xor(mx,32));
    const float mn=fmaxf(m,mx); const float f=__builtin_amdgcn_exp2f(m-mn); m=mn;
    float a=0.f; u32x4 p0,p1;
    #pragma unroll
    for(int i=0;i<16;i++){ s[i]=__builtin_amdgcn_exp2f(s[i]-m); }
    #pragma unroll
    for(int i=0;i<4;i++){ p0[i]=cvtpk(s[2*i],s[2*i+1]); p1[i]=cvtpk(s[8+2*i],s[8+2*i+1]); a+=bflo(p0[i])+bfhi(p0[i])+bflo(p1[i])+bfhi(p1[i]); }
    a+=__shfl_xor(a,32); l=l*f+a;
    #pragma unroll
    for(int i=0;i<16;i++) o[i]*=f;
    o=MFMA32(v0,BF8(p0),o); o=MFMA32(v1,BF8(p1),o);
  }
}
__global__ __launch_bounds__(512,2) void k3_attn(const u16* __restrict__ Qt,const char* __restrict__ KV,u16* __restrict__ Ot,const char* __restrict__ w2,const char* __restrict__ wo){
  extern __shared__ __attribute__((aligned(16))) char lds[];
  const int tid=threadIdx.x, lane=tid&63, wid=__builtin_amdgcn_readfirstlane(tid>>6), r=lane&31, h=lane>>5;
  const int head=blockIdx.x>>3, pb=(blockIdx.x&7)*8+wid;
  const unsigned lds0=(unsigned)(uintptr_t)lds;
  const char* src=KV+wid*2048+lane*16;
  auto dma=[&](int c){ const unsigned dst=lds0+(c&(AT_NS-1))*AT_SLOT+wid*2048;
    glds16(src+(size_t)c*AT_SLOT,(unsigned)__builtin_amdgcn_readfirstlane(dst)); glds16(src+(size_t)c*AT_SLOT+1024,(unsigned)__builtin_amdgcn_readfirstlane(dst+1024)); };
  dma(0); dma(1); dma(2);
  const u16* qp=Qt+((size_t)(head*64+pb)*2)*512+lane*8;
  const bf16x8 qf0=*(const bf16x8*)qp, qf1=*(const bf16x8*)(qp+512);
  bf16x8 ones; { const bool on=((lane&15)==((lane>>4)&1)); for(int j=0;j<8;j++) ones[j]= on?(short)0x3f80:(short)0; }
  f32x16 o=f32x16{}, negm, sc, sn; f32x4 l16=f32x4{};
  const LAS char* lp=(const LAS char*)lds+lane*16;
  asm volatile("s_waitcnt vmcnt(0)\n\ts_barrier":::"memory");
  { const bf16x8 k0=*(const LAS bf16x8*)(lp+0), k1=*(const LAS bf16x8*)(lp+1024);
    f32x16 s=MFMA32(k0,qf0,f32x16{}); s=MFMA32(k1,qf1,s);
    float m=s[0];
    #pragma unroll
    for(int i=1;i<16;i++) m=fmaxf(m,s[i]);
    auto rr=__builtin_amdgcn_permlane32_swap(__float_as_uint(m),__float_as_uint(m),false,false);
    m=fmaxf(__uint_as_float(rr[0]),__uint_as_float(rr[1]));
    #pragma unroll
    for(int i=0;i<16;i++){ negm[i]=-m; sc[i]=s[i]-m; }
    PIN(negm); }
  #define EX(S,i) S[i]=__builtin_amdgcn_exp2f(S[i])
  #define STEP(SC,SN,KADDR,V1ADDR,V0ADDR,DOQK,DOLK,DOLV0) do{ \
      u32x4 p0; \
      if(DOQK){ SN=MFMA32(k0,qf0,negm); if(DOLK) k0=*(const LAS bf16x8*)(KADDR); } SBAR(); \
      EX(SC,0); EX(SC,1); EX(SC,2); PIN(SC); SBAR(); \
      o=MFMA32(v1,BF8(p1),o); v1=*(const LAS bf16x8*)(V1ADDR); SBAR(); \
      EX(SC,3); EX(SC,4); EX(SC,5); PIN(SC); SBAR(); \
      if(DOQK){ SN=MFMA32(k1,qf1,SN); if(DOLK) k1=*(const LAS bf16x8*)((KADDR)+1024); } SBAR(); \
      EX(SC,6); EX(SC,7); p0[0]=cvtpk(SC[0],SC[1]); p0[1]=cvtpk(SC[2],SC[3]); PIN(SC); PIN(p0); SBAR(); \
      l16=MFMA16(ones,BF8(p1),l16); SBAR(); \
      p0[2]=cvtpk(SC[4],SC[5]); p0[3]=cvtpk(SC[6],SC[7]); EX(SC,8); EX(SC,9); PIN(SC); PIN(p0); SBAR(); \
      o=MFMA32(v0,BF8(p0),o); if(DOLV0) v0=*(const LAS bf16x8*)(V0ADDR); SBAR(); \
      EX(SC,10); EX(SC,11); EX(SC,12); PIN(SC); SBAR(); \
      l16=MFMA16(ones,BF8(p0),l16); SBAR(); \
      EX(SC,13); EX(SC,14); EX(SC,15); \
      p1[0]=cvtpk(SC[8],SC[9]); p1[1]=cvtpk(SC[10],SC[11]); p1[2]=cvtpk(SC[12],SC[13]); p1[3]=cvtpk(SC[14],SC[15]); PIN(p1); SBAR(); \
    }while(0)
  bf16x8 k0,k1,v0,v1; u32x4 p1=u32x4{0,0,0,0};
  k0=*(const LAS bf16x8*)(lp+4096); k1=*(const LAS bf16x8*)(lp+4096+1024); v0=*(const LAS bf16x8*)(lp+2048); v1=v0;
  for(int c=0;c<15;c++){
    const LAS char* cur=lp+(c&(AT_NS-1))*AT_SLOT; const LAS char* nxt=lp+((c+1)&(AT_NS-1))*AT_SLOT;
    STEP(sc,sn,cur+8192, cur+3072,       cur+4096+2048, true,true,true);
    STEP(sn,sc,cur+12288,cur+4096+3072,  cur+8192+2048, true,true,true);
    if(c+2<16) asm volatile("s_waitcnt vmcnt(2)\n\ts_barrier":::"memory"); else asm volatile("s_waitcnt vmcnt(0)\n\ts_barrier":::"memory");
    if(c+3<16) dma(c+3);
    STEP(sc,sn,nxt,      cur+8192+3072,  cur+12288+2048,true,true,true);
    STEP(sn,sc,nxt+4096, cur+12288+3072, nxt+2048,      true,true,true);
  }
  { const LAS char* cur=lp+(15&(AT_NS-1))*AT_SLOT;
    STEP(sc,sn,cur+8192, cur+3072,       cur+4096+2048, true,true,true);
    STEP(sn,sc,cur+12288,cur+4096+3072,  cur+8192+2048, true,true,true);
    STEP(sc,sn,cur,      cur+8192+3072,  cur+12288+2048,true,false,true);
    STEP(sn,sc,cur,      cur+12288+3072, cur,           false,false,false);
    o=MFMA32(v1,BF8(p1),o); l16=MFMA16(ones,BF8(p1),l16); }
  #undef STEP
  #undef EX
  float l; { const float a=__shfl(l16[0],r&15), b=__shfl(l16[1],r&15); l=(r<16)?a:b; }
  { float chk=l;
    #pragma unroll
    for(int i=0;i<16;i++) chk+=fabsf(o[i])*0.f;
    const bool bad=!(chk<1.0e18f);
    if(__any(bad)){ f32x16 o2; float l2; attn_slow(qf0,qf1,KV,lane,o2,l2); o=o2; l=l2; } }
  const float inv=1.f/l;
  u16* op=Ot+((size_t)(pb*2+(r>>4))*32+head)*512+((r&15)+16*h)*8;
  u32x4 wa,wb;
  wa[0]=cvtpk(o[0]*inv,o[1]*inv); wa[1]=cvtpk(o[2]*inv,o[3]*inv); wa[2]=cvtpk(o[8]*inv,o[9]*inv); wa[3]=cvtpk(o[10]*inv,o[11]*inv);
  wb[0]=cvtpk(o[4]*inv,o[5]*inv); wb[1]=cvtpk(o[6]*inv,o[7]*inv); wb[2]=cvtpk(o[12]*inv,o[13]*inv); wb[3]=cvtpk(o[14]*inv,o[15]*inv);
  *(u32x4*)op=wa; *(u32x4*)(op+256)=wb;
  { const int b=blockIdx.x; const char* p=(b<199? w2+(size_t)b*36864 : wo+(size_t)(b-199)*36864)+tid*16; const size_t lim=(b<199)?(size_t)3584*2048-(size_t)b*36864:(size_t)1024*2048-(size_t)(b-199)*36864;
    (void)p; (void)lim; }
}

constexpr int G2_SLOT=24576  , G2_NS=6, G2_STEPS=72;
template<int L> DI void g2_loader(const char* Ht,const char* Ot,const char* W2,const char* Wo,int rbk,int cbk,LAS char* lds,int lane){
  constexpr int CNT=6;
  unsigned vb[2];
  #pragma unroll
  for(int i=0;i<2;i++){ const int r=8*(2*L+i)+(lane>>3),pos=lane&7; const int ch=pos^(((r>>1)&3)<<1); vb[i]=r*2048+ch*16; }
  const char* hA=Ht+(size_t)(rbk*8+2*L)*112*1024+lane*16; const char* oA=Ot+(size_t)(rbk*8+2*L)*32*1024+lane*16;
  const char* w2P=W2+cbk*128; const char* woP=Wo+cbk*128;
  auto issue=[&](int s){ LAS char* sl=lds+(s%G2_NS)*G2_SLOT;
    const char* a; size_t rs; const char* w;
    if(s<56){ a=hA+(size_t)s*2048; rs=112*1024; w=w2P+(size_t)s*64*2048; } else { a=oA+(size_t)(s-56)*2048; rs=32*1024; w=woP+(size_t)(s-56)*64*2048; }
    GLDS(a,sl+(2*L)*2048); GLDS(a+1024,sl+(2*L)*2048+1024); GLDS(a+rs,sl+(2*L+1)*2048); GLDS(a+rs+1024,sl+(2*L+1)*2048+1024);
    GLDS(w+vb[0],sl+16384+(2*L)*1024); GLDS(w+vb[1],sl+16384+(2*L+1)*1024); };
  #pragma unroll
  for(int s=0;s<5;s++) issue(s);
  asm volatile("s_waitcnt vmcnt(%0)\n\ts_barrier"::"i"(4*CNT):"memory");
  for(int j=0;j<G2_STEPS-5;j++){ asm volatile("s_waitcnt vmcnt(%0)\n\ts_barrier"::"i"(3*CNT):"memory"); issue(j+5); }
  asm volatile("s_waitcnt vmcnt(%0)\n\ts_barrier"::"i"(3*CNT):"memory");
  asm volatile("s_waitcnt vmcnt(%0)\n\ts_barrier"::"i"(2*CNT):"memory");
  asm volatile("s_waitcnt vmcnt(%0)\n\ts_barrier"::"i"(1*CNT):"memory");
  asm volatile("s_waitcnt vmcnt(0)\n\ts_barrier":::"memory");
}
DI void g2_compute(int wr,int wc,int rbk,int cbk,const LAS char* lds,int lane,const u16* x,const u16* b2,u16* out){
  const int g=lane>>4,q=(lane&15)>>2,p=lane&3; const int sg=(2*g+(q>>1))&3;
  int aA=wr*8192+lane*16;
  int aB[2]; aB[0]=16384+(4*g+q)*128+32*((2*wc)^sg)+8*p; aB[1]=16384+(4*g+q)*128+32*((2*wc+1)^sg)+8*p;
  f32x4 acc[4][2];
  #pragma unroll
  for(int m=0;m<4;m++){ acc[m][0]=f32x4{0,0,0,0}; acc[m][1]=f32x4{0,0,0,0}; }
  bf16x8 A[2][4],B[2][2];
  auto rd=[&](int kb){
    #pragma unroll
    for(int m=0;m<4;m++) A[kb][m]=*(const LAS bf16x8*)(lds+aA+m*2048+kb*1024);
    #pragma unroll
    for(int n=0;n<2;n++) B[kb][n]=cat(trd(lds+aB[n]+kb*4096),trd(lds+aB[n]+kb*4096+2048)); };
  asm volatile("s_barrier":::"memory");
  rd(0);
  int slot=0;
  for(int j=0;j<G2_STEPS;j++){
    rd(1);
    { int d=(slot==G2_NS-1)?-(G2_NS-1)*G2_SLOT:G2_SLOT; slot=(slot==G2_NS-1)?0:slot+1; aA+=d; aB[0]+=d; aB[1]+=d; }
    SBAR();
    #pragma unroll
    for(int m=0;m<4;m++){ acc[m][0]=MFMA16(B[0][0],A[0][m],acc[m][0]); acc[m][1]=MFMA16(B[0][1],A[0][m],acc[m][1]); }
    SBAR();
    if(j<G2_STEPS-1){ asm volatile("s_barrier":::"memory"); rd(0); }
    SBAR();
    #pragma unroll
    for(int m=0;m<4;m++){ acc[m][0]=MFMA16(B[1][0],A[1][m],acc[m][0]); acc[m][1]=MFMA16(B[1][1],A[1][m],acc[m][1]); }
    SBAR();
  }
  const int c=lane&15;
  #pragma unroll
  for(int n=0;n<2;n++){ const int col=cbk*64+wc*32+16*n+4*g; const u32x2 bb=*(const u32x2*)(b2+col);
    #pragma unroll
    for(int m=0;m<4;m++){ const int row=rbk*128+wr*64+m*16+c; const u32x2 xx=*(const u32x2*)(x+(size_t)row*1024+col);
      u32x2 w; w[0]=cvtpk(acc[m][n][0]+bflo(xx[0])+bflo(bb[0]),acc[m][n][1]+bfhi(xx[0])+bfhi(bb[0])); w[1]=cvtpk(acc[m][n][2]+bflo(xx[1])+bflo(bb[1]),acc[m][n][3]+bfhi(xx[1])+bfhi(bb[1]));
      *(u32x2*)(out+(size_t)row*1024+col)=w; } }
}
__global__ __launch_bounds__(512,2) void k4_gemm2(const char* __restrict__ Ht,const char* __restrict__ Ot,const char* __restrict__ W2,const char* __restrict__ Wo,
    const u16* __restrict__ x,const u16* __restrict__ b2,u16* __restrict__ out){
  extern __shared__ __attribute__((aligned(16))) char ldsg[];
  LAS char* lds=(LAS char*)ldsg;
  const int tid=threadIdx.x,lane=tid&63,wid=__builtin_amdgcn_readfirstlane(tid>>6);
  const int b=blockIdx.x,xcd=b&7,j=b>>3; const int rbk=(xcd&3)*4+(j&3),cbk=(xcd>>2)*8+(j>>2);
  if(wid>=4){
    if(wid==4) g2_loader<0>(Ht,Ot,W2,Wo,rbk,cbk,lds,lane); else if(wid==5) g2_loader<1>(Ht,Ot,W2,Wo,rbk,cbk,lds,lane);
    else if(wid==6) g2_loader<2>(Ht,Ot,W2,Wo,rbk,cbk,lds,lane); else g2_loader<3>(Ht,Ot,W2,Wo,rbk,cbk,lds,lane);
  } else g2_compute(wid>>1,wid&1,rbk,cbk,lds,lane,x,b2,out);
}

extern "C" void kernel_launch(void* const* d_in,const int* in_sizes,int n_in,void* d_out,int out_size,void* d_ws,size_t ws_size,hipStream_t st){
  const u16* x=(const u16*)d_in[0]; const char* wq=(const char*)d_in[1]; const char* wk=(const char*)d_in[2]; const char* wv=(const char*)d_in[3]; const char* wo=(const char*)d_in[4];
  const char* w1=(const char*)d_in[6]; const u16* b1=(const u16*)d_in[7]; const char* w2=(const char*)d_in[8]; const u16* b2=(const u16*)d_in[9];
  char* ws=(char*)d_ws;
  (void)hipFuncSetAttribute((const void*)k1_ln,hipFuncAttributeMaxDynamicSharedMemorySize,K1_LDS);
  (void)hipFuncSetAttribute((const void*)k2_gemm1,hipFuncAttributeMaxDynamicSharedMemorySize,G1_NS*G1_SLOT);
  (void)hipFuncSetAttribute((const void*)k3_attn,hipFuncAttributeMaxDynamicSharedMemorySize,AT_NS*AT_SLOT);
  (void)hipFuncSetAttribute((const void*)k4_gemm2,hipFuncAttributeMaxDynamicSharedMemorySize,G2_NS*G2_SLOT);
  hipLaunchKernelGGL(k1_ln,dim3(256),dim3(512),K1_LDS,st,x,wk,wv,(const u16*)wq,w1,ws+WS_YT,ws+WS_KV,(float*)(ws+WS_ST),(float*)(ws+WS_CS),(unsigned*)(ws+WS_FLAG));
  hipLaunchKernelGGL(k2_gemm1,dim3(256),dim3(512),G1_NS*G1_SLOT,st,ws+WS_YT,w1,wq,b1,(const float*)(ws+WS_ST),(const float*)(ws+WS_CS),(u16*)(ws+WS_HT),(u16*)(ws+WS_QT));
  hipLaunchKernelGGL(k3_attn,dim3(256),dim3(512),AT_NS*AT_SLOT,st,(const u16*)(ws+WS_QT),ws+WS_KV,(u16*)(ws+WS_OT),w2,wo);
  hipLaunchKernelGGL(k4_gemm2,dim3(256),dim3(512),G2_NS*G2_SLOT,st,ws+WS_HT,ws+WS_OT,w2,wo,x,b2,(u16*)d_out);
}
```

```cpp
#include <hip/hip_runtime.h>
#include <stdint.h>
typedef unsigned short u16;
typedef short bf16x8 __attribute__((ext_vector_type(8)));
typedef short s16x4 __attribute__((ext_vector_type(4)));
typedef float f32x16 __attribute__((ext_vector_type(16)));
typedef float f32x4 __attribute__((ext_vector_type(4)));
typedef float f32x2 __attribute__((ext_vector_type(2)));
typedef __bf16 bf16x2_t __attribute__((ext_vector_type(2)));
typedef unsigned u32x4 __attribute__((ext_vector_type(4)));
typedef unsigned u32x2 __attribute__((ext_vector_type(2)));
#define LAS __attribute__((address_space(3)))
#define DI __device__ __forceinline__
#define MFMA32(a,b,c) __builtin_amdgcn_mfma_f32_32x32x16_bf16((a),(b),(c),0,0,0)
#define MFMA16(a,b,c) __builtin_amdgcn_mfma_f32_16x16x32_bf16((a),(b),(c),0,0,0)
#define SBAR() __builtin_amdgcn_sched_barrier(0)
#define PIN(x) asm volatile("":"+v"(x))
#define BF8(x) __builtin_bit_cast(bf16x8,x)
DI unsigned cvtpk(float lo,float hi){ f32x2 v={lo,hi}; bf16x2_t b=__builtin_convertvector(v,bf16x2_t); return __builtin_bit_cast(unsigned,b); }
DI float bflo(unsigned u){ return __uint_as_float(u<<16); }
DI float bfhi(unsigned u){ return __uint_as_float(u&0xffff0000u); }
DI float rbf(float f){ return bflo(cvtpk(f,0.f)); }
DI s16x4 trd(const LAS char* p){ return __builtin_amdgcn_ds_read_tr16_b64_v4i16((LAS s16x4*)p); }
DI bf16x8 cat(s16x4 a,s16x4 b){ return __builtin_shufflevector(a,b,0,1,2,3,4,5,6,7); }

constexpr float QSCALE=0.17677669529663687f*1.4426950408889634f;
constexpr size_t WS_YT=0, WS_HT=4u<<20, WS_OT=24u<<20, WS_KV=28u<<20, WS_ST=29u<<20, WS_FLAG=WS_ST+131072, WS_PART=30u<<20;

typedef __amdgpu_buffer_rsrc_t rsrc_t;
DI rsrc_t mkrsrc(const void* p,unsigned bytes){ return __builtin_amdgcn_make_buffer_rsrc((void*)p,(short)0,(int)bytes,0x00020000); }
#define BST16(w,rs,off,aux) __builtin_amdgcn_raw_buffer_store_b128(w,rs,off,0,aux)
constexpr int AUX_WT=16;
constexpr int K1_LDS=114688;
template<int CTRL> DI float dpp_add(float v){ return v+__int_as_float(__builtin_amdgcn_update_dpp(0,__float_as_int(v),CTRL,0xf,0xf,true)); }
DI void ln16x2(const f32x2 (&v)[2][8],unsigned (&o)[2][8],float (&mb)[2],float (&sd)[2]){
  f32x2 s2[2]; float mean[2],var[2],inv[2];
  #pragma unroll
  for(int r=0;r<2;r++){ s2[r]=((v[r][0]+v[r][1])+(v[r][2]+v[r][3]))+((v[r][4]+v[r][5])+(v[r][6]+v[r][7])); mean[r]=s2[r][0]+s2[r][1]; }
  #define WS2(a) do{ a[0]=dpp_add<0xB1>(a[0]); a[1]=dpp_add<0xB1>(a[1]); a[0]=dpp_add<0x4E>(a[0]); a[1]=dpp_add<0x4E>(a[1]); a[0]=dpp_add<0x141>(a[0]); a[1]=dpp_add<0x141>(a[1]); a[0]=dpp_add<0x140>(a[0]); a[1]=dpp_add<0x140>(a[1]); \
    { auto p0=__builtin_amdgcn_permlane16_swap(__float_as_uint(a[0]),__float_as_uint(a[0]),false,false); auto p1=__builtin_amdgcn_permlane16_swap(__float_as_uint(a[1]),__float_as_uint(a[1]),false,false); \
      a[0]=__uint_as_float(p0[0])+__uint_as_float(p0[1]); a[1]=__uint_as_float(p1[0])+__uint_as_float(p1[1]); } \
    { auto p0=__builtin_amdgcn_permlane32_swap(__float_as_uint(a[0]),__float_as_uint(a[0]),false,false); auto p1=__builtin_amdgcn_permlane32_swap(__float_as_uint(a[1]),__float_as_uint(a[1]),false,false); \
      a[0]=__uint_as_float(p0[0])+__uint_as_float(p0[1]); a[1]=__uint_as_float(p1[0])+__uint_as_float(p1[1]); } }while(0)
  WS2(mean);
  #pragma unroll
  for(int r=0;r<2;r++){ mean[r]*=(1.f/1024.f); f32x2 d[8];
    #pragma unroll
    for(int i=0;i<8;i++){ d[i]=v[r][i]-mean[r]; d[i]*=d[i]; }
    const f32x2 q=((d[0]+d[1])+(d[2]+d[3]))+((d[4]+d[5])+(d[6]+d[7])); var[r]=q[0]+q[1]; }
  WS2(var);
  #undef WS2
  #pragma unroll
  for(int r=0;r<2;r++){ mb[r]=rbf(mean[r]); sd[r]=rbf(__builtin_amdgcn_sqrtf(rbf(rbf(var[r]*(1.f/1024.f))+1e-5f))); inv[r]=1.f/sd[r]; }
  #pragma unroll
  for(int i=0;i<8;i++)
    #pragma unroll
    for(int r=0;r<2;r++){ const f32x2 d=v[r][i]-mb[r]; const unsigned t=cvtpk(d[0],d[1]); const f32x2 e=f32x2{bflo(t),bfhi(t)}*inv[r]; o[r][i]=cvtpk(e[0],e[1]); }
}
__global__ __launch_bounds__(1024) void k1_ln(const u16* __restrict__ x,const char* __restrict__ wk,const char* __restrict__ wv,const char* __restrict__ wq,const char* __restrict__ w1,
                                               char* __restrict__ Yt,char* __restrict__ KV,float* __restrict__ rowstat,unsigned* __restrict__ flags){
  extern __shared__ __attribute__((aligned(16))) char ldsg[];
  LAS char* lds=(LAS char*)ldsg;
  const int tid=threadIdx.x,lane=tid&63,wid=__builtin_amdgcn_readfirstlane(tid>>6),b=blockIdx.x,i16=b>>1,part=b&1;
  if(wid>=8){
    const int l=wid-8;
    asm volatile("s_barrier":::"memory");
    { const rsrc_t rK=mkrsrc(part?wv:wk,1024*64); const unsigned o0=l*8192, l0=(unsigned)(uintptr_t)lds+l*8192; const int rr=lane>>2,pos=lane&3; const unsigned vK=rr*64+(pos^(2*((rr>>2)&1)))*16;
      asm volatile("s_add_u32 m0, %[l0], 0\n\ts_add_u32 s45, %[o0], 0\n\tbuffer_load_dwordx4 %[vK], %[rK], s45 offen offset:0 lds\n\tbuffer_load_dwordx4 %[vK], %[rK], s45 offen offset:1024 lds\n\tbuffer_load_dwordx4 %[vK], %[rK], s45 offen offset:2048 lds\n\tbuffer_load_dwordx4 %[vK], %[rK], s45 offen offset:3072 lds\n\ts_add_u32 m0, %[l0], 4096\n\ts_add_u32 s45, %[o0], 4096\n\tbuffer_load_dwordx4 %[vK], %[rK], s45 offen offset:0 lds\n\tbuffer_load_dwordx4 %[vK], %[rK], s45 offen offset:1024 lds\n\tbuffer_load_dwordx4 %[vK], %[rK], s45 offen offset:2048 lds\n\tbuffer_load_dwordx4 %[vK], %[rK], s45 offen offset:3072 lds"::[l0]"s"(l0),[o0]"s"(o0),[rK]"s"(rK),[vK]"v"(vK):"memory","scc","s45"); }
    unsigned pf0=0;
    if((b*8+l)<216){
      const int id=(b*8+l)*64+lane; const int row=id/72, u=id%72; const char* p=(u<56)? w1+(size_t)row*7168+u*128 : wq+(size_t)row*2048+(u-56)*128;
      asm volatile("global_load_dword %0, %1, off":"=v"(pf0):"v"(p):"memory");
      asm volatile("s_waitcnt vmcnt(1)":::"memory");
    } else asm volatile("s_waitcnt vmcnt(0)":::"memory");
    asm volatile("s_barrier":::"memory");
    if(b==0 && l<4){ flags[(l*64+lane)*16]=0u; if(l==0 && lane<32) flags[4096+lane*16]=0u; }
    asm volatile("s_barrier":::"memory");
    asm volatile("s_waitcnt vmcnt(0)":"+v"(pf0)::"memory");
  } else {
    const int kb=lane>>2,g=lane&3;
    u32x2 xl[2][4];
    { const u16* xr=x+(size_t)(16*i16+2*wid)*1024+kb*32+4*g; const u16* xs=xr+1024;
      asm volatile("global_load_dwordx2 %0, %8, off\n\tglobal_load_dwordx2 %1, %8, off offset:32\n\tglobal_load_dwordx2 %2, %8, off offset:1024\n\tglobal_load_dwordx2 %3, %8, off offset:1056\n\t"
                   "global_load_dwordx2 %4, %9, off\n\tglobal_load_dwordx2 %5, %9, off offset:32\n\tglobal_load_dwordx2 %6, %9, off offset:1024\n\tglobal_load_dwordx2 %7, %9, off offset:1056\n\ts_barrier\n\ts_waitcnt vmcnt(0)"
                   :"=&v"(xl[0][0]),"=&v"(xl[0][1]),"=&v"(xl[0][2]),"=&v"(xl[0][3]),"=&v"(xl[1][0]),"=&v"(xl[1][1]),"=&v"(xl[1][2]),"=&v"(xl[1][3]):"v"(xr),"v"(xs):"memory"); }
    float s2r[2],m2r[2];
    { f32x2 xv[2][8]; unsigned xn[2][8],yp[2][8]; float m1[2],s1[2];
      #pragma unroll
      for(int r=0;r<2;r++)
        #pragma unroll
        for(int i=0;i<4;i++){ xv[r][2*i]=f32x2{bflo(xl[r][i][0]),bfhi(xl[r][i][0])}; xv[r][2*i+1]=f32x2{bflo(xl[r][i][1]),bfhi(xl[r][i][1])}; }
      ln16x2(xv,xn,m1,s1);
      #pragma unroll
      for(int r=0;r<2;r++){ const int c=2*wid+r;
        #pragma unroll
        for(int u=0;u<2;u++){ const int k2=kb+16*u; const int pos=(c^(g|((k2&1)<<2)))+16*g; *(LAS u32x4*)(lds+65536+k2*1024+pos*16)=u32x4{xn[r][4*u],xn[r][4*u+1],xn[r][4*u+2],xn[r][4*u+3]}; }
        #pragma unroll
        for(int i=0;i<8;i++) xv[r][i]=f32x2{bflo(xn[r][i]),bfhi(xn[r][i])}; }
      ln16x2(xv,yp,m2r,s2r);
      #pragma unroll
      for(int r=0;r<2;r++){ const int c=2*wid+r; const int pos=(c^(g|((kb&1)<<2)))+16*g; const u32x4 w=part? u32x4{yp[r][4],yp[r][5],yp[r][6],yp[r][7]} : u32x4{yp[r][0],yp[r][1],yp[r][2],yp[r][3]}; *(LAS u32x4*)(lds+98304+kb*1024+pos*16)=w; } }
    PIN(s2r[0]); PIN(s2r[1]); PIN(m2r[0]); PIN(m2r[1]);
    asm volatile("s_waitcnt lgkmcnt(0)\n\ts_barrier":::"memory");
    {
      const int gg=lane>>4,cc=lane&15; u32x4 w[2];
      #pragma unroll
      for(int i=0;i<2;i++){ const int k2=wid+8*i; w[i]=*(const LAS u32x4*)(lds+98304+k2*1024+((cc^(gg|((k2&1)<<2)))+16*gg)*16); }
      const rsrc_t ry=mkrsrc(Yt,2048*1024*2);
      #pragma unroll
      for(int i=0;i<2;i++){ const int k2=wid+8*i+16*part; BST16(w[i],ry,(unsigned)((i16*32+k2)*1024+lane*16),AUX_WT); } }
    f32x4 ac[2]={};
    { const int gg=lane>>4,cc=lane&15,q=(lane&15)>>2,p=lane&3;
      bf16x8 xa[4],wf[4][2];
      #pragma unroll
      for(int i=0;i<4;i++){ const int k2=4*wid+i;
        xa[i]=*(const LAS bf16x8*)(lds+65536+k2*1024+((cc^(gg|((i&1)<<2)))+16*gg)*16);
        #pragma unroll
        for(int a=0;a<2;a++){ const LAS char* bp=lds+(k2*32+4*gg+q)*64+32*(a^(gg&1))+8*p; wf[i][a]=cat(trd(bp),trd(bp+1024)); } }
      SBAR();
      if(part==0){
        #pragma unroll
        for(int i=0;i<4;i++){ ac[0]=MFMA16(wf[i][0],xa[i],ac[0]); ac[1]=MFMA16(wf[i][1],xa[i],ac[1]); }
      } else {
        #pragma unroll
        for(int i=0;i<4;i++){ ac[0]=MFMA16(xa[i],wf[i][0],ac[0]); ac[1]=MFMA16(xa[i],wf[i][1],ac[1]); } } }
    PIN(ac[0]); PIN(ac[1]);
    *(LAS f32x4*)(lds+wid*8192+lane*16)=ac[0]; *(LAS f32x4*)(lds+wid*8192+1024+lane*16)=ac[1];
    asm volatile("s_waitcnt lgkmcnt(0)\n\ts_barrier":::"memory");
    if(wid<2){ const int gg=lane>>4,cc=lane&15; f32x4 pr[8];
      #pragma unroll
      for(int w=0;w<8;w++) pr[w]=*(const LAS f32x4*)(lds+w*8192+wid*1024+lane*16);
      const f32x4 s0=((pr[0]+pr[1])+(pr[2]+pr[3]))+((pr[4]+pr[5])+(pr[6]+pr[7]));
      u32x2 o; o[0]=cvtpk(s0[0],s0[1]); o[1]=cvtpk(s0[2],s0[3]);
      if(part==0){ const int key=16*i16+cc;
        *(u32x2*)(KV+(size_t)(key>>5)*4096+(gg>>1)*1024+((key&31)+32*(gg&1))*16+8*wid)=o;
      } else *(u32x2*)(KV+(size_t)(i16>>1)*4096+2048+(i16&1)*1024+((cc+16*wid)+32*(gg&1))*16+8*(gg>>1))=o; }
    if(part==0 && lane<2){ f32x2 st={(lane?s2r[1]:s2r[0])*QSCALE,(lane?m2r[1]:m2r[0])*QSCALE}; *(f32x2*)(rowstat+2*(16*i16+2*wid+lane))=st; }
  }
}

#define G1_ASM "s_barrier\n\tds_read_b128 v[8:11], %[aA]\n\tds_read_b64_tr_b16 v[24:25], %[bE] offset:0\n\tds_read_b64_tr_b16 v[26:27], %[bE] offset:7168\n\tds_read_b128 v[12:15], %[aA] offset:1024\n\tds_read_b128 v[16:19], %[aA] offset:2048\n\tds_read_b128 v[20:23], %[aA] offset:3072\n\tds_read_b64_tr_b16 v[28:29], %[bO] offset:32\n\tds_read_b64_tr_b16 v[30:31], %[bO] offset:7200\n\tds_read_b64_tr_b16 v[32:33], %[bE] offset:64\n\tds_read_b64_tr_b16 v[34:35], %[bE] offset:7232\n\tds_read_b64_tr_b16 v[36:37], %[bO] offset:96\n\tds_read_b64_tr_b16 v[38:39], %[bO] offset:7264\n\tds_read_b64_tr_b16 v[40:41], %[bE] offset:128\n\tds_read_b64_tr_b16 v[42:43], %[bE] offset:7296\n\tds_read_b64_tr_b16 v[44:45], %[bO] offset:160\n\tds_read_b64_tr_b16 v[46:47], %[bO] offset:7328\n\tds_read_b64_tr_b16 v[48:49], %[bQ0] offset:0\n\tds_read_b64_tr_b16 v[50:51], %[bQ0] offset:2048\n\tds_read_b64_tr_b16 v[52:53], %[bQ1] offset:0\n\tds_read_b64_tr_b16 v[54:55], %[bQ1] offset:2048\n\tds_read_b64_tr_b16 v[56:57], %[b8] offset:0\n\tds_read_b64_tr_b16 v[58:59], %[b8] offset:7168\n\ts_add_u32 %[slot], %[slot], 1\n\ts_cmp_eq_u32 %[slot], 6\n\ts_cselect_b32 %[dlt], %[cW], %[cS]\n\ts_cselect_b32 %[slot], 0, %[slot]\n\tv_add_u32 %[aA], %[dlt], %[aA]\n\tv_add_u32 %[b8], %[dlt], %[b8]\n\tv_add_u32 %[bE], %[dlt], %[bE]\n\tv_add_u32 %[bO], %[dlt], %[bO]\n\tv_add_u32 %[bQ0], %[dlt], %[bQ0]\n\tv_add_u32 %[bQ1], %[dlt], %[bQ1]\n\ts_waitcnt lgkmcnt(0)\n\ts_barrier\n\tG1_loop%=:\n\tv_mfma_f32_16x16x32_bf16 a[0:3], v[24:27], v[8:11], a[0:3]\n\tds_read_b128 v[60:63], %[aA]\n\tv_mfma_f32_16x16x32_bf16 a[32:35], v[24:27], v[12:15], a[32:35]\n\tds_read_b64_tr_b16 v[76:77], %[bE] offset:0\n\tv_mfma_f32_16x16x32_bf16 a[64:67], v[24:27], v[16:19], a[64:67]\n\tds_read_b64_tr_b16 v[78:79], %[bE] offset:7168\n\tv_mfma_f32_16x16x32_bf16 a[96:99], v[24:27], v[20:23], a[96:99]\n\tds_read_b128 v[64:67], %[aA] offset:1024\n\tv_mfma_f32_16x16x32_bf16 a[4:7], v[28:31], v[8:11], a[4:7]\n\tds_read_b128 v[68:71], %[aA] offset:2048\n\tv_mfma_f32_16x16x32_bf16 a[36:39], v[28:31], v[12:15], a[36:39]\n\tds_read_b128 v[72:75], %[aA] offset:3072\n\tv_mfma_f32_16x16x32_bf16 a[68:71], v[28:31], v[16:19], a[68:71]\n\tds_read_b64_tr_b16 v[80:81], %[bO] offset:32\n\tv_mfma_f32_16x16x32_bf16 a[100:103], v[28:31], v[20:23], a[100:103]\n\tds_read_b64_tr_b16 v[82:83], %[bO] offset:7200\n\tv_mfma_f32_16x16x32_bf16 a[8:11], v[32:35], v[8:11], a[8:11]\n\tds_read_b64_tr_b16 v[84:85], %[bE] offset:64\n\tv_mfma_f32_16x16x32_bf16 a[40:43], v[32:35], v[12:15], a[40:43]\n\tds_read_b64_tr_b16 v[86:87], %[bE] offset:7232\n\tv_mfma_f32_16x16x32_bf16 a[72:75], v[32:35], v[16:19], a[72:75]\n\tds_read_b64_tr_b16 v[88:89], %[bO] offset:96\n\tv_mfma_f32_16x16x32_bf16 a[104:107], v[32:35], v[20:23], a[104:107]\n\tds_read_b64_tr_b16 v[90:91], %[bO] offset:7264\n\tv_mfma_f32_16x16x32_bf16 a[12:15], v[36:39], v[8:11], a[12:15]\n\tds_read_b64_tr_b16 v[92:93], %[bE] offset:128\n\tv_mfma_f32_16x16x32_bf16 a[44:47], v[36:39], v[12:15], a[44:47]\n\tds_read_b64_tr_b16 v[94:95], %[bE] offset:7296\n\tv_mfma_f32_16x16x32_bf16 a[76:79], v[36:39], v[16:19], a[76:79]\n\tds_read_b64_tr_b16 v[96:97], %[bO] offset:160\n\tv_mfma_f32_16x16x32_bf16 a[108:111], v[36:39], v[20:23], a[108:111]\n\tds_read_b64_tr_b16 v[98:99], %[bO] offset:7328\n\tv_mfma_f32_16x16x32_bf16 a[16:19], v[40:43], v[8:11], a[16:19]\n\tds_read_b64_tr_b16 v[100:101], %[bQ0] offset:0\n\tv_mfma_f32_16x16x32_bf16 a[48:51], v[40:43], v[12:15], a[48:51]\n\tds_read_b64_tr_b16 v[102:103], %[bQ0] offset:2048\n\tv_mfma_f32_16x16x32_bf16 a[80:83], v[40:43], v[16:19], a[80:83]\n\tds_read_b64_tr_b16 v[104:105], %[bQ1] offset:0\n\tv_mfma_f32_16x16x32_bf16 a[112:115], v[40:43], v[20:23], a[112:115]\n\tds_read_b64_tr_b16 v[106:107], %[bQ1] offset:2048\n\tv_mfma_f32_16x16x32_bf16 a[20:23], v[44:47], v[8:11], a[20:23]\n\tds_read_b64_tr_b16 v[108:109], %[b8] offset:0\n\tv_mfma_f32_16x16x32_bf16 a[52:55], v[44:47], v[12:15], a[52:55]\n\tds_read_b64_tr_b16 v[110:111], %[b8] offset:7168\n\tv_mfma_f32_16x16x32_bf16 a[84:87], v[44:47], v[16:19], a[84:87]\n\ts_add_u32 %[slot], %[slot], 1\n\tv_mfma_f32_16x16x32_bf16 a[116:119], v[44:47], v[20:23], a[116:119]\n\ts_cmp_eq_u32 %[slot], 6\n\tv_mfma_f32_16x16x32_bf16 a[24:27], v[48:51], v[8:11], a[24:27]\n\ts_cselect_b32 %[dlt], %[cW], %[cS]\n\tv_mfma_f32_16x16x32_bf16 a[56:59], v[48:51], v[12:15], a[56:59]\n\ts_cselect_b32 %[slot], 0, %[slot]\n\tv_mfma_f32_16x16x32_bf16 a[88:91], v[48:51], v[16:19], a[88:91]\n\tv_add_u32 %[aA], %[dlt], %[aA]\n\tv_mfma_f32_16x16x32_bf16 a[120:123], v[48:51], v[20:23], a[120:123]\n\tv_add_u32 %[b8], %[dlt], %[b8]\n\tv_mfma_f32_16x16x32_bf16 a[28:31], v[52:55], v[8:11], a[28:31]\n\tv_add_u32 %[bE], %[dlt], %[bE]\n\tv_mfma_f32_16x16x32_bf16 a[60:63], v[52:55], v[12:15], a[60:63]\n\tv_add_u32 %[bO], %[dlt], %[bO]\n\tv_mfma_f32_16x16x32_bf16 a[92:95], v[52:55], v[16:19], a[92:95]\n\tv_add_u32 %[bQ0], %[dlt], %[bQ0]\n\tv_mfma_f32_16x16x32_bf16 a[124:127], v[52:55], v[20:23], a[124:127]\n\tv_add_u32 %[bQ1], %[dlt], %[bQ1]\n\tv_mfma_f32_16x16x32_bf16 v[112:115], v[56:59], v[8:11], v[112:115]\n\tv_mfma_f32_16x16x32_bf16 v[116:119], v[56:59], v[12:15], v[116:119]\n\tv_mfma_f32_16x16x32_bf16 v[120:123], v[56:59], v[16:19], v[120:123]\n\tv_mfma_f32_16x16x32_bf16 v[124:127], v[56:59], v[20:23], v[124:127]\n\ts_waitcnt lgkmcnt(0)\n\tv_mfma_f32_16x16x32_bf16 a[0:3], v[76:79], v[60:63], a[0:3]\n\tds_read_b128 v[8:11], %[aA]\n\tv_mfma_f32_16x16x32_bf16 a[32:35], v[76:79], v[64:67], a[32:35]\n\tds_read_b64_tr_b16 v[24:25], %[bE] offset:0\n\tv_mfma_f32_16x16x32_bf16 a[64:67], v[76:79], v[68:71], a[64:67]\n\tds_read_b64_tr_b16 v[26:27], %[bE] offset:7168\n\tv_mfma_f32_16x16x32_bf16 a[96:99], v[76:79], v[72:75], a[96:99]\n\tds_read_b128 v[12:15], %[aA] offset:1024\n\tv_mfma_f32_16x16x32_bf16 a[4:7], v[80:83], v[60:63], a[4:7]\n\tds_read_b128 v[16:19], %[aA] offset:2048\n\tv_mfma_f32_16x16x32_bf16 a[36:39], v[80:83], v[64:67], a[36:39]\n\tds_read_b128 v[20:23], %[aA] offset:3072\n\tv_mfma_f32_16x16x32_bf16 a[68:71], v[80:83], v[68:71], a[68:71]\n\tds_read_b64_tr_b16 v[28:29], %[bO] offset:32\n\tv_mfma_f32_16x16x32_bf16 a[100:103], v[80:83], v[72:75], a[100:103]\n\tds_read_b64_tr_b16 v[30:31], %[bO] offset:7200\n\tv_mfma_f32_16x16x32_bf16 a[8:11], v[84:87], v[60:63], a[8:11]\n\tds_read_b64_tr_b16 v[32:33], %[bE] offset:64\n\tv_mfma_f32_16x16x32_bf16 a[40:43], v[84:87], v[64:67], a[40:43]\n\tds_read_b64_tr_b16 v[34:35], %[bE] offset:7232\n\tv_mfma_f32_16x16x32_bf16 a[72:75], v[84:87], v[68:71], a[72:75]\n\tds_read_b64_tr_b16 v[36:37], %[bO] offset:96\n\tv_mfma_f32_16x16x32_bf16 a[104:107], v[84:87], v[72:75], a[104:107]\n\tds_read_b64_tr_b16 v[38:39], %[bO] offset:7264\n\tv_mfma_f32_16x16x32_bf16 a[12:15], v[88:91], v[60:63], a[12:15]\n\tds_read_b64_tr_b16 v[40:41], %[bE] offset:128\n\tv_mfma_f32_16x16x32_bf16 a[44:47], v[88:91], v[64:67], a[44:47]\n\tds_read_b64_tr_b16 v[42:43], %[bE] offset:7296\n\tv_mfma_f32_16x16x32_bf16 a[76:79], v[88:91], v[68:71], a[76:79]\n\tds_read_b64_tr_b16 v[44:45], %[bO] offset:160\n\tv_mfma_f32_16x16x32_bf16 a[108:111], v[88:91], v[72:75], a[108:111]\n\tds_read_b64_tr_b16 v[46:47], %[bO] offset:7328\n\tv_mfma_f32_16x16x32_bf16 a[16:19], v[92:95], v[60:63], a[16:19]\n\tds_read_b64_tr_b16 v[48:49], %[bQ0] offset:0\n\tv_mfma_f32_16x16x32_bf16 a[48:51], v[92:95], v[64:67], a[48:51]\n\tds_read_b64_tr_b16 v[50:51], %[bQ0] offset:2048\n\tv_mfma_f32_16x16x32_bf16 a[80:83], v[92:95], v[68:71], a[80:83]\n\tds_read_b64_tr_b16 v[52:53], %[bQ1] offset:0\n\tv_mfma_f32_16x16x32_bf16 a[112:115], v[92:95], v[72:75], a[112:115]\n\tds_read_b64_tr_b16 v[54:55], %[bQ1] offset:2048\n\tv_mfma_f32_16x16x32_bf16 a[20:23], v[96:99], v[60:63], a[20:23]\n\tds_read_b64_tr_b16 v[56:57], %[b8] offset:0\n\tv_mfma_f32_16x16x32_bf16 a[52:55], v[96:99], v[64:67], a[52:55]\n\tds_read_b64_tr_b16 v[58:59], %[b8] offset:7168\n\tv_mfma_f32_16x16x32_bf16 a[84:87], v[96:99], v[68:71], a[84:87]\n\ts_add_u32 %[slot], %[slot], 1\n\tv_mfma_f32_16x16x32_bf16 a[116:119], v[96:99], v[72:75], a[116:119]\n\ts_cmp_eq_u32 %[slot], 6\n\tv_mfma_f32_16x16x32_bf16 a[24:27], v[100:103], v[60:63], a[24:27]\n\ts_cselect_b32 %[dlt], %[cW], %[cS]\n\tv_mfma_f32_16x16x32_bf16 a[56:59], v[100:103], v[64:67], a[56:59]\n\ts_cselect_b32 %[slot], 0, %[slot]\n\tv_mfma_f32_16x16x32_bf16 a[88:91], v[100:103], v[68:71], a[88:91]\n\tv_add_u32 %[aA], %[dlt], %[aA]\n\tv_mfma_f32_16x16x32_bf16 a[120:123], v[100:103], v[72:75], a[120:123]\n\tv_add_u32 %[b8], %[dlt], %[b8]\n\tv_mfma_f32_16x16x32_bf16 a[28:31], v[104:107], v[60:63], a[28:31]\n\tv_add_u32 %[bE], %[dlt], %[bE]\n\tv_mfma_f32_16x16x32_bf16 a[60:63], v[104:107], v[64:67], a[60:63]\n\tv_add_u32 %[bO], %[dlt], %[bO]\n\tv_mfma_f32_16x16x32_bf16 a[92:95], v[104:107], v[68:71], a[92:95]\n\tv_add_u32 %[bQ0], %[dlt], %[bQ0]\n\tv_mfma_f32_16x16x32_bf16 a[124:127], v[104:107], v[72:75], a[124:127]\n\tv_add_u32 %[bQ1], %[dlt], %[bQ1]\n\tv_mfma_f32_16x16x32_bf16 v[112:115], v[108:111], v[60:63], v[112:115]\n\tv_mfma_f32_16x16x32_bf16 v[116:119], v[108:111], v[64:67], v[116:119]\n\ts_waitcnt lgkmcnt(0)\n\ts_barrier\n\tv_mfma_f32_16x16x32_bf16 v[120:123], v[108:111], v[68:71], v[120:123]\n\tv_mfma_f32_16x16x32_bf16 v[124:127], v[108:111], v[72:75], v[124:127]\n\ts_sub_u32 %[cnt], %[cnt], 1\n\ts_cmp_lg_u32 %[cnt], 0\n\ts_cbranch_scc1 G1_loop%=\n\tv_mfma_f32_16x16x32_bf16 a[0:3], v[24:27], v[8:11], a[0:3]\n\tds_read_b128 v[60:63], %[aA]\n\tv_mfma_f32_16x16x32_bf16 a[32:35], v[24:27], v[12:15], a[32:35]\n\tds_read_b64_tr_b16 v[76:77], %[bE] offset:0\n\tv_mfma_f32_16x16x32_bf16 a[64:67], v[24:27], v[16:19], a[64:67]\n\tds_read_b64_tr_b16 v[78:79], %[bE] offset:7168\n\tv_mfma_f32_16x16x32_bf16 a[96:99], v[24:27], v[20:23], a[96:99]\n\tds_read_b128 v[64:67], %[aA] offset:1024\n\tv_mfma_f32_16x16x32_bf16 a[4:7], v[28:31], v[8:11], a[4:7]\n\tds_read_b128 v[68:71], %[aA] offset:2048\n\tv_mfma_f32_16x16x32_bf16 a[36:39], v[28:31], v[12:15], a[36:39]\n\tds_read_b128 v[72:75], %[aA] offset:3072\n\tv_mfma_f32_16x16x32_bf16 a[68:71], v[28:31], v[16:19], a[68:71]\n\tds_read_b64_tr_b16 v[80:81], %[bO] offset:32\n\tv_mfma_f32_16x16x32_bf16 a[100:103], v[28:31], v[20:23], a[100:103]\n\tds_read_b64_tr_b16 v[82:83], %[bO] offset:7200\n\tv_mfma_f32_16x16x32_bf16 a[8:11], v[32:35], v[8:11], a[8:11]\n\tds_read_b64_tr_b16 v[84:85], %[bE] offset:64\n\tv_mfma_f32_16x16x32_bf16 a[40:43], v[32:35], v[12:15], a[40:43]\n\tds_read_b64_tr_b16 v[86:87], %[bE] offset:7232\n\tv_mfma_f32_16x16x32_bf16 a[72:75], v[32:35], v[16:19], a[72:75]\n\tds_read_b64_tr_b16 v[88:89], %[bO] offset:96\n\tv_mfma_f32_16x16x32_bf16 a[104:107], v[32:35], v[20:23], a[104:107]\n\tds_read_b64_tr_b16 v[90:91], %[bO] offset:7264\n\tv_mfma_f32_16x16x32_bf16 a[12:15], v[36:39], v[8:11], a[12:15]\n\tds_read_b64_tr_b16 v[92:93], %[bE] offset:128\n\tv_mfma_f32_16x16x32_bf16 a[44:47], v[36:39], v[12:15], a[44:47]\n\tds_read_b64_tr_b16 v[94:95], %[bE] offset:7296\n\tv_mfma_f32_16x16x32_bf16 a[76:79], v[36:39], v[16:19], a[76:79]\n\tds_read_b64_tr_b16 v[96:97], %[bO] offset:160\n\tv_mfma_f32_16x16x32_bf16 a[108:111], v[36:39], v[20:23], a[108:111]\n\tds_read_b64_tr_b16 v[98:99], %[bO] offset:7328\n\tv_mfma_f32_16x16x32_bf16 a[16:19], v[40:43], v[8:11], a[16:19]\n\tds_read_b64_tr_b16 v[100:101], %[bQ0] offset:0\n\tv_mfma_f32_16x16x32_bf16 a[48:51], v[40:43], v[12:15], a[48:51]\n\tds_read_b64_tr_b16 v[102:103], %[bQ0] offset:2048\n\tv_mfma_f32_16x16x32_bf16 a[80:83], v[40:43], v[16:19], a[80:83]\n\tds_read_b64_tr_b16 v[104:105], %[bQ1] offset:0\n\tv_mfma_f32_16x16x32_bf16 a[112:115], v[40:43], v[20:23], a[112:115]\n\tds_read_b64_tr_b16 v[106:107], %[bQ1] offset:2048\n\tv_mfma_f32_16x16x32_bf16 a[20:23], v[44:47], v[8:11], a[20:23]\n\tds_read_b64_tr_b16 v[108:109], %[b8] offset:0\n\tv_mfma_f32_16x16x32_bf16 a[52:55], v[44:47], v[12:15], a[52:55]\n\tds_read_b64_tr_b16 v[110:111], %[b8] offset:7168\n\tv_mfma_f32_16x16x32_bf16 a[84:87], v[44:47], v[16:19], a[84:87]\n\ts_add_u32 %[slot], %[slot], 1\n\tv_mfma_f32_16x16x32_bf16 a[116:119], v[44:47], v[20:23], a[116:119]\n\ts_cmp_eq_u32 %[slot], 6\n\tv_mfma_f32_16x16x32_bf16 a[24:27], v[48:51], v[8:11], a[24:27]\n\ts_cselect_b32 %[dlt], %[cW], %[cS]\n\tv_mfma_f32_16x16x32_bf16 a[56:59], v[48:51], v[12:15], a[56:59]\n\ts_cselect_b32 %[slot], 0, %[slot]\n\tv_mfma_f32_16x16x32_bf16 a[88:91], v[48:51], v[16:19], a[88:91]\n\tv_add_u32 %[aA], %[dlt], %[aA]\n\tv_mfma_f32_16x16x32_bf16 a[120:123], v[48:51], v[20:23], a[120:123]\n\tv_add_u32 %[b8], %[dlt], %[b8]\n\tv_mfma_f32_16x16x32_bf16 a[28:31], v[52:55], v[8:11], a[28:31]\n\tv_add_u32 %[bE], %[dlt], %[bE]\n\tv_mfma_f32_16x16x32_bf16 a[60:63], v[52:55], v[12:15], a[60:63]\n\tv_add_u32 %[bO], %[dlt], %[bO]\n\tv_mfma_f32_16x16x32_bf16 a[92:95], v[52:55], v[16:19], a[92:95]\n\tv_add_u32 %[bQ0], %[dlt], %[bQ0]\n\tv_mfma_f32_16x16x32_bf16 a[124:127], v[52:55], v[20:23], a[124:127]\n\tv_add_u32 %[bQ1], %[dlt], %[bQ1]\n\tv_mfma_f32_16x16x32_bf16 v[112:115], v[56:59], v[8:11], v[112:115]\n\tv_mfma_f32_16x16x32_bf16 v[116:119], v[56:59], v[12:15], v[116:119]\n\tv_mfma_f32_16x16x32_bf16 v[120:123], v[56:59], v[16:19], v[120:123]\n\tv_mfma_f32_16x16x32_bf16 v[124:127], v[56:59], v[20:23], v[124:127]\n\ts_waitcnt lgkmcnt(0)\n\tv_mfma_f32_16x16x32_bf16 a[0:3], v[76:79], v[60:63], a[0:3]\n\tv_mfma_f32_16x16x32_bf16 a[32:35], v[76:79], v[64:67], a[32:35]\n\tv_mfma_f32_16x16x32_bf16 a[64:67], v[76:79], v[68:71], a[64:67]\n\tv_mfma_f32_16x16x32_bf16 a[96:99], v[76:79], v[72:75], a[96:99]\n\tv_mfma_f32_16x16x32_bf16 a[4:7], v[80:83], v[60:63], a[4:7]\n\tv_mfma_f32_16x16x32_bf16 a[36:39], v[80:83], v[64:67], a[36:39]\n\tv_mfma_f32_16x16x32_bf16 a[68:71], v[80:83], v[68:71], a[68:71]\n\tv_mfma_f32_16x16x32_bf16 a[100:103], v[80:83], v[72:75], a[100:103]\n\tv_mfma_f32_16x16x32_bf16 a[8:11], v[84:87], v[60:63], a[8:11]\n\tv_mfma_f32_16x16x32_bf16 a[40:43], v[84:87], v[64:67], a[40:43]\n\tv_mfma_f32_16x16x32_bf16 a[72:75], v[84:87], v[68:71], a[72:75]\n\tv_mfma_f32_16x16x32_bf16 a[104:107], v[84:87], v[72:75], a[104:107]\n\tv_mfma_f32_16x16x32_bf16 a[12:15], v[88:91], v[60:63], a[12:15]\n\tv_mfma_f32_16x16x32_bf16 a[44:47], v[88:91], v[64:67], a[44:47]\n\tv_mfma_f32_16x16x32_bf16 a[76:79], v[88:91], v[68:71], a[76:79]\n\tv_mfma_f32_16x16x32_bf16 a[108:111], v[88:91], v[72:75], a[108:111]\n\tv_mfma_f32_16x16x32_bf16 a[16:19], v[92:95], v[60:63], a[16:19]\n\tv_mfma_f32_16x16x32_bf16 a[48:51], v[92:95], v[64:67], a[48:51]\n\tv_mfma_f32_16x16x32_bf16 a[80:83], v[92:95], v[68:71], a[80:83]\n\tv_mfma_f32_16x16x32_bf16 a[112:115], v[92:95], v[72:75], a[112:115]\n\tv_mfma_f32_16x16x32_bf16 a[20:23], v[96:99], v[60:63], a[20:23]\n\tv_mfma_f32_16x16x32_bf16 a[52:55], v[96:99], v[64:67], a[52:55]\n\tv_mfma_f32_16x16x32_bf16 a[84:87], v[96:99], v[68:71], a[84:87]\n\tv_mfma_f32_16x16x32_bf16 a[116:119], v[96:99], v[72:75], a[116:119]\n\tv_mfma_f32_16x16x32_bf16 a[24:27], v[100:103], v[60:63], a[24:27]\n\tv_mfma_f32_16x16x32_bf16 a[56:59], v[100:103], v[64:67], a[56:59]\n\tv_mfma_f32_16x16x32_bf16 a[88:91], v[100:103], v[68:71], a[88:91]\n\tv_mfma_f32_16x16x32_bf16 a[120:123], v[100:103], v[72:75], a[120:123]\n\tv_mfma_f32_16x16x32_bf16 a[28:31], v[104:107], v[60:63], a[28:31]\n\tv_mfma_f32_16x16x32_bf16 a[60:63], v[104:107], v[64:67], a[60:63]\n\tv_mfma_f32_16x16x32_bf16 a[92:95], v[104:107], v[68:71], a[92:95]\n\tv_mfma_f32_16x16x32_bf16 a[124:127], v[104:107], v[72:75], a[124:127]\n\tv_mfma_f32_16x16x32_bf16 v[112:115], v[108:111], v[60:63], v[112:115]\n\tv_mfma_f32_16x16x32_bf16 v[116:119], v[108:111], v[64:67], v[116:119]\n\tv_mfma_f32_16x16x32_bf16 v[120:123], v[108:111], v[68:71], v[120:123]\n\tv_mfma_f32_16x16x32_bf16 v[124:127], v[108:111], v[72:75], v[124:127]\n\ts_nop 7\n\ts_nop 7"
#define G1_CLOB "v8","v9","v10","v11","v12","v13","v14","v15","v16","v17","v18","v19","v20","v21","v22","v23","v24","v25","v26","v27","v28","v29","v30","v31","v32","v33","v34","v35","v36","v37","v38","v39","v40","v41","v42","v43","v44","v45","v46","v47","v48","v49","v50","v51","v52","v53","v54","v55","v56","v57","v58","v59","v60","v61","v62","v63","v64","v65","v66","v67","v68","v69","v70","v71","v72","v73","v74","v75","v76","v77","v78","v79","v80","v81","v82","v83","v84","v85","v86","v87","v88","v89","v90","v91","v92","v93","v94","v95","v96","v97","v98","v99","v100","v101","v102","v103","v104","v105","v106","v107","v108","v109","v110","v111","a0","a1","a2","a3","a4","a5","a6","a7","a8","a9","a10","a11","a12","a13","a14","a15","a16","a17","a18","a19","a20","a21","a22","a23","a24","a25","a26","a27","a28","a29","a30","a31","a32","a33","a34","a35","a36","a37","a38","a39","a40","a41","a42","a43","a44","a45","a46","a47","a48","a49","a50","a51","a52","a53","a54","a55","a56","a57","a58","a59","a60","a61","a62","a63","a64","a65","a66","a67","a68","a69","a70","a71","a72","a73","a74","a75","a76","a77","a78","a79","a80","a81","a82","a83","a84","a85","a86","a87","a88","a89","a90","a91","a92","a93","a94","a95","a96","a97","a98","a99","a100","a101","a102","a103","a104","a105","a106","a107","a108","a109","a110","a111","a112","a113","a114","a115","a116","a117","a118","a119","a120","a121","a122","a123","a124","a125","a126","a127"
#define G2_ASM "s_barrier\n\tds_read_b128 v[8:11], %[aA]\n\tds_read_b64_tr_b16 v[40:41], %[b0] offset:0\n\tds_read_b64_tr_b16 v[42:43], %[b0] offset:4096\n\tds_read_b128 v[12:15], %[aA] offset:1024\n\tds_read_b128 v[16:19], %[aA] offset:2048\n\tds_read_b128 v[20:23], %[aA] offset:3072\n\tds_read_b128 v[24:27], %[aA] offset:4096\n\tds_read_b128 v[28:31], %[aA] offset:5120\n\tds_read_b128 v[32:35], %[aA] offset:6144\n\tds_read_b128 v[36:39], %[aA] offset:7168\n\tds_read_b64_tr_b16 v[44:45], %[b1] offset:0\n\tds_read_b64_tr_b16 v[46:47], %[b1] offset:4096\n\tds_read_b64_tr_b16 v[48:49], %[b2] offset:0\n\tds_read_b64_tr_b16 v[50:51], %[b2] offset:4096\n\tds_read_b64_tr_b16 v[52:53], %[b3] offset:0\n\tds_read_b64_tr_b16 v[54:55], %[b3] offset:4096\n\ts_add_u32 %[slot], %[slot], 1\n\ts_cmp_eq_u32 %[slot], 6\n\ts_cselect_b32 %[dlt], %[cW], %[cS]\n\ts_cselect_b32 %[slot], 0, %[slot]\n\tv_add_u32 %[aA], %[dlt], %[aA]\n\tv_add_u32 %[b0], %[dlt], %[b0]\n\tv_add_u32 %[b1], %[dlt], %[b1]\n\tv_add_u32 %[b2], %[dlt], %[b2]\n\tv_add_u32 %[b3], %[dlt], %[b3]\n\ts_waitcnt lgkmcnt(0)\n\ts_barrier\n\tv_mfma_f32_16x16x32_bf16 a[0:3], v[40:43], v[8:11], 0\n\tds_read_b128 v[56:59], %[aA]\n\tv_mfma_f32_16x16x32_bf16 a[16:19], v[40:43], v[12:15], 0\n\tds_read_b64_tr_b16 v[88:89], %[b0] offset:0\n\tv_mfma_f32_16x16x32_bf16 a[32:35], v[40:43], v[16:19], 0\n\tds_read_b64_tr_b16 v[90:91], %[b0] offset:4096\n\tv_mfma_f32_16x16x32_bf16 a[48:51], v[40:43], v[20:23], 0\n\tds_read_b128 v[60:63], %[aA] offset:1024\n\tv_mfma_f32_16x16x32_bf16 a[64:67], v[40:43], v[24:27], 0\n\tds_read_b128 v[64:67], %[aA] offset:2048\n\tv_mfma_f32_16x16x32_bf16 a[80:83], v[40:43], v[28:31], 0\n\tds_read_b128 v[68:71], %[aA] offset:3072\n\tv_mfma_f32_16x16x32_bf16 a[96:99], v[40:43], v[32:35], 0\n\tds_read_b128 v[72:75], %[aA] offset:4096\n\tv_mfma_f32_16x16x32_bf16 a[112:115], v[40:43], v[36:39], 0\n\tds_read_b128 v[76:79], %[aA] offset:5120\n\tv_mfma_f32_16x16x32_bf16 a[4:7], v[44:47], v[8:11], 0\n\tds_read_b128 v[80:83], %[aA] offset:6144\n\tv_mfma_f32_16x16x32_bf16 a[20:23], v[44:47], v[12:15], 0\n\tds_read_b128 v[84:87], %[aA] offset:7168\n\tv_mfma_f32_16x16x32_bf16 a[36:39], v[44:47], v[16:19], 0\n\tds_read_b64_tr_b16 v[92:93], %[b1] offset:0\n\tv_mfma_f32_16x16x32_bf16 a[52:55], v[44:47], v[20:23], 0\n\tds_read_b64_tr_b16 v[94:95], %[b1] offset:4096\n\tv_mfma_f32_16x16x32_bf16 a[68:71], v[44:47], v[24:27], 0\n\tds_read_b64_tr_b16 v[96:97], %[b2] offset:0\n\tv_mfma_f32_16x16x32_bf16 a[84:87], v[44:47], v[28:31], 0\n\tds_read_b64_tr_b16 v[98:99], %[b2] offset:4096\n\tv_mfma_f32_16x16x32_bf16 a[100:103], v[44:47], v[32:35], 0\n\tds_read_b64_tr_b16 v[100:101], %[b3] offset:0\n\tv_mfma_f32_16x16x32_bf16 a[116:119], v[44:47], v[36:39], 0\n\tds_read_b64_tr_b16 v[102:103], %[b3] offset:4096\n\tv_mfma_f32_16x16x32_bf16 a[8:11], v[48:51], v[8:11], 0\n\ts_add_u32 %[slot], %[slot], 1\n\tv_mfma_f32_16x16x32_bf16 a[24:27], v[48:51], v[12:15], 0\n\ts_cmp_eq_u32 %[slot], 6\n\tv_mfma_f32_16x16x32_bf16 a[40:43], v[48:51], v[16:19], 0\n\ts_cselect_b32 %[dlt], %[cW], %[cS]\n\tv_mfma_f32_16x16x32_bf16 a[56:59], v[48:51], v[20:23], 0\n\ts_cselect_b32 %[slot], 0, %[slot]\n\tv_mfma_f32_16x16x32_bf16 a[72:75], v[48:51], v[24:27], 0\n\tv_add_u32 %[aA], %[dlt], %[aA]\n\tv_mfma_f32_16x16x32_bf16 a[88:91], v[48:51], v[28:31], 0\n\tv_add_u32 %[b0], %[dlt], %[b0]\n\tv_mfma_f32_16x16x32_bf16 a[104:107], v[48:51], v[32:35], 0\n\tv_add_u32 %[b1], %[dlt], %[b1]\n\tv_mfma_f32_16x16x32_bf16 a[120:123], v[48:51], v[36:39], 0\n\tv_add_u32 %[b2], %[dlt], %[b2]\n\tv_mfma_f32_16x16x32_bf16 a[12:15], v[52:55], v[8:11], 0\n\tv_add_u32 %[b3], %[dlt], %[b3]\n\tv_mfma_f32_16x16x32_bf16 a[28:31], v[52:55], v[12:15], 0\n\tv_mfma_f32_16x16x32_bf16 a[44:47], v[52:55], v[16:19], 0\n\tv_mfma_f32_16x16x32_bf16 a[60:63], v[52:55], v[20:23], 0\n\tv_mfma_f32_16x16x32_bf16 a[76:79], v[52:55], v[24:27], 0\n\tv_mfma_f32_16x16x32_bf16 a[92:95], v[52:55], v[28:31], 0\n\tv_mfma_f32_16x16x32_bf16 a[108:111], v[52:55], v[32:35], 0\n\tv_mfma_f32_16x16x32_bf16 a[124:127], v[52:55], v[36:39], 0\n\ts_waitcnt lgkmcnt(0)\n\tv_mfma_f32_16x16x32_bf16 a[0:3], v[88:91], v[56:59], a[0:3]\n\tds_read_b128 v[8:11], %[aA]\n\tv_mfma_f32_16x16x32_bf16 a[16:19], v[88:91], v[60:63], a[16:19]\n\tds_read_b64_tr_b16 v[40:41], %[b0] offset:0\n\tv_mfma_f32_16x16x32_bf16 a[32:35], v[88:91], v[64:67], a[32:35]\n\tds_read_b64_tr_b16 v[42:43], %[b0] offset:4096\n\tv_mfma_f32_16x16x32_bf16 a[48:51], v[88:91], v[68:71], a[48:51]\n\tds_read_b128 v[12:15], %[aA] offset:1024\n\tv_mfma_f32_16x16x32_bf16 a[64:67], v[88:91], v[72:75], a[64:67]\n\tds_read_b128 v[16:19], %[aA] offset:2048\n\tv_mfma_f32_16x16x32_bf16 a[80:83], v[88:91], v[76:79], a[80:83]\n\tds_read_b128 v[20:23], %[aA] offset:3072\n\tv_mfma_f32_16x16x32_bf16 a[96:99], v[88:91], v[80:83], a[96:99]\n\tds_read_b128 v[24:27], %[aA] offset:4096\n\tv_mfma_f32_16x16x32_bf16 a[112:115], v[88:91], v[84:87], a[112:115]\n\tds_read_b128 v[28:31], %[aA] offset:5120\n\tv_mfma_f32_16x16x32_bf16 a[4:7], v[92:95], v[56:59], a[4:7]\n\tds_read_b128 v[32:35], %[aA] offset:6144\n\tv_mfma_f32_16x16x32_bf16 a[20:23], v[92:95], v[60:63], a[20:23]\n\tds_read_b128 v[36:39], %[aA] offset:7168\n\tv_mfma_f32_16x16x32_bf16 a[36:39], v[92:95], v[64:67], a[36:39]\n\tds_read_b64_tr_b16 v[44:45], %[b1] offset:0\n\tv_mfma_f32_16x16x32_bf16 a[52:55], v[92:95], v[68:71], a[52:55]\n\tds_read_b64_tr_b16 v[46:47], %[b1] offset:4096\n\tv_mfma_f32_16x16x32_bf16 a[68:71], v[92:95], v[72:75], a[68:71]\n\tds_read_b64_tr_b16 v[48:49], %[b2] offset:0\n\tv_mfma_f32_16x16x32_bf16 a[84:87], v[92:95], v[76:79], a[84:87]\n\tds_read_b64_tr_b16 v[50:51], %[b2] offset:4096\n\tv_mfma_f32_16x16x32_bf16 a[100:103], v[92:95], v[80:83], a[100:103]\n\tds_read_b64_tr_b16 v[52:53], %[b3] offset:0\n\tv_mfma_f32_16x16x32_bf16 a[116:119], v[92:95], v[84:87], a[116:119]\n\tds_read_b64_tr_b16 v[54:55], %[b3] offset:4096\n\tv_mfma_f32_16x16x32_bf16 a[8:11], v[96:99], v[56:59], a[8:11]\n\ts_add_u32 %[slot], %[slot], 1\n\tv_mfma_f32_16x16x32_bf16 a[24:27], v[96:99], v[60:63], a[24:27]\n\ts_cmp_eq_u32 %[slot], 6\n\tv_mfma_f32_16x16x32_bf16 a[40:43], v[96:99], v[64:67], a[40:43]\n\ts_cselect_b32 %[dlt], %[cW], %[cS]\n\tv_mfma_f32_16x16x32_bf16 a[56:59], v[96:99], v[68:71], a[56:59]\n\ts_cselect_b32 %[slot], 0, %[slot]\n\tv_mfma_f32_16x16x32_bf16 a[72:75], v[96:99], v[72:75], a[72:75]\n\tv_add_u32 %[aA], %[dlt], %[aA]\n\tv_mfma_f32_16x16x32_bf16 a[88:91], v[96:99], v[76:79], a[88:91]\n\tv_add_u32 %[b0], %[dlt], %[b0]\n\tv_mfma_f32_16x16x32_bf16 a[104:107], v[96:99], v[80:83], a[104:107]\n\tv_add_u32 %[b1], %[dlt], %[b1]\n\tv_mfma_f32_16x16x32_bf16 a[120:123], v[96:99], v[84:87], a[120:123]\n\tv_add_u32 %[b2], %[dlt], %[b2]\n\tv_mfma_f32_16x16x32_bf16 a[12:15], v[100:103], v[56:59], a[12:15]\n\tv_add_u32 %[b3], %[dlt], %[b3]\n\tv_mfma_f32_16x16x32_bf16 a[28:31], v[100:103], v[60:63], a[28:31]\n\tv_mfma_f32_16x16x32_bf16 a[44:47], v[100:103], v[64:67], a[44:47]\n\tv_mfma_f32_16x16x32_bf16 a[60:63], v[100:103], v[68:71], a[60:63]\n\tv_mfma_f32_16x16x32_bf16 a[76:79], v[100:103], v[72:75], a[76:79]\n\tv_mfma_f32_16x16x32_bf16 a[92:95], v[100:103], v[76:79], a[92:95]\n\ts_waitcnt lgkmcnt(0)\n\ts_barrier\n\tv_mfma_f32_16x16x32_bf16 a[108:111], v[100:103], v[80:83], a[108:111]\n\tv_mfma_f32_16x16x32_bf16 a[124:127], v[100:103], v[84:87], a[124:127]\n\ts_sub_u32 %[cnt], %[cnt], 1\n\tG2_loop%=:\n\tv_mfma_f32_16x16x32_bf16 a[0:3], v[40:43], v[8:11], a[0:3]\n\tds_read_b128 v[56:59], %[aA]\n\tv_mfma_f32_16x16x32_bf16 a[16:19], v[40:43], v[12:15], a[16:19]\n\tds_read_b64_tr_b16 v[88:89], %[b0] offset:0\n\tv_mfma_f32_16x16x32_bf16 a[32:35], v[40:43], v[16:19], a[32:35]\n\tds_read_b64_tr_b16 v[90:91], %[b0] offset:4096\n\tv_mfma_f32_16x16x32_bf16 a[48:51], v[40:43], v[20:23], a[48:51]\n\tds_read_b128 v[60:63], %[aA] offset:1024\n\tv_mfma_f32_16x16x32_bf16 a[64:67], v[40:43], v[24:27], a[64:67]\n\tds_read_b128 v[64:67], %[aA] offset:2048\n\tv_mfma_f32_16x16x32_bf16 a[80:83], v[40:43], v[28:31], a[80:83]\n\tds_read_b128 v[68:71], %[aA] offset:3072\n\tv_mfma_f32_16x16x32_bf16 a[96:99], v[40:43], v[32:35], a[96:99]\n\tds_read_b128 v[72:75], %[aA] offset:4096\n\tv_mfma_f32_16x16x32_bf16 a[112:115], v[40:43], v[36:39], a[112:115]\n\tds_read_b128 v[76:79], %[aA] offset:5120\n\tv_mfma_f32_16x16x32_bf16 a[4:7], v[44:47], v[8:11], a[4:7]\n\tds_read_b128 v[80:83], %[aA] offset:6144\n\tv_mfma_f32_16x16x32_bf16 a[20:23], v[44:47], v[12:15], a[20:23]\n\tds_read_b128 v[84:87], %[aA] offset:7168\n\tv_mfma_f32_16x16x32_bf16 a[36:39], v[44:47], v[16:19], a[36:39]\n\tds_read_b64_tr_b16 v[92:93], %[b1] offset:0\n\tv_mfma_f32_16x16x32_bf16 a[52:55], v[44:47], v[20:23], a[52:55]\n\tds_read_b64_tr_b16 v[94:95], %[b1] offset:4096\n\tv_mfma_f32_16x16x32_bf16 a[68:71], v[44:47], v[24:27], a[68:71]\n\tds_read_b64_tr_b16 v[96:97], %[b2] offset:0\n\tv_mfma_f32_16x16x32_bf16 a[84:87], v[44:47], v[28:31], a[84:87]\n\tds_read_b64_tr_b16 v[98:99], %[b2] offset:4096\n\tv_mfma_f32_16x16x32_bf16 a[100:103], v[44:47], v[32:35], a[100:103]\n\tds_read_b64_tr_b16 v[100:101], %[b3] offset:0\n\tv_mfma_f32_16x16x32_bf16 a[116:119], v[44:47], v[36:39], a[116:119]\n\tds_read_b64_tr_b16 v[102:103], %[b3] offset:4096\n\tv_mfma_f32_16x16x32_bf16 a[8:11], v[48:51], v[8:11], a[8:11]\n\ts_add_u32 %[slot], %[slot], 1\n\tv_mfma_f32_16x16x32_bf16 a[24:27], v[48:51], v[12:15], a[24:27]\n\ts_cmp_eq_u32 %[slot], 6\n\tv_mfma_f32_16x16x32_bf16 a[40:43], v[48:51], v[16:19], a[40:43]\n\ts_cselect_b32 %[dlt], %[cW], %[cS]\n\tv_mfma_f32_16x16x32_bf16 a[56:59], v[48:51], v[20:23], a[56:59]\n\ts_cselect_b32 %[slot], 0, %[slot]\n\tv_mfma_f32_16x16x32_bf16 a[72:75], v[48:51], v[24:27], a[72:75]\n\tv_add_u32 %[aA], %[dlt], %[aA]\n\tv_mfma_f32_16x16x32_bf16 a[88:91], v[48:51], v[28:31], a[88:91]\n\tv_add_u32 %[b0], %[dlt], %[b0]\n\tv_mfma_f32_16x16x32_bf16 a[104:107], v[48:51], v[32:35], a[104:107]\n\tv_add_u32 %[b1], %[dlt], %[b1]\n\tv_mfma_f32_16x16x32_bf16 a[120:123], v[48:51], v[36:39], a[120:123]\n\tv_add_u32 %[b2], %[dlt], %[b2]\n\tv_mfma_f32_16x16x32_bf16 a[12:15], v[52:55], v[8:11], a[12:15]\n\tv_add_u32 %[b3], %[dlt], %[b3]\n\tv_mfma_f32_16x16x32_bf16 a[28:31], v[52:55], v[12:15], a[28:31]\n\tv_mfma_f32_16x16x32_bf16 a[44:47], v[52:55], v[16:19], a[44:47]\n\tv_mfma_f32_16x16x32_bf16 a[60:63], v[52:55], v[20:23], a[60:63]\n\tv_mfma_f32_16x16x32_bf16 a[76:79], v[52:55], v[24:27], a[76:79]\n\tv_mfma_f32_16x16x32_bf16 a[92:95], v[52:55], v[28:31], a[92:95]\n\tv_mfma_f32_16x16x32_bf16 a[108:111], v[52:55], v[32:35], a[108:111]\n\tv_mfma_f32_16x16x32_bf16 a[124:127], v[52:55], v[36:39], a[124:127]\n\ts_waitcnt lgkmcnt(0)\n\tv_mfma_f32_16x16x32_bf16 a[0:3], v[88:91], v[56:59], a[0:3]\n\tds_read_b128 v[8:11], %[aA]\n\tv_mfma_f32_16x16x32_bf16 a[16:19], v[88:91], v[60:63], a[16:19]\n\tds_read_b64_tr_b16 v[40:41], %[b0] offset:0\n\tv_mfma_f32_16x16x32_bf16 a[32:35], v[88:91], v[64:67], a[32:35]\n\tds_read_b64_tr_b16 v[42:43], %[b0] offset:4096\n\tv_mfma_f32_16x16x32_bf16 a[48:51], v[88:91], v[68:71], a[48:51]\n\tds_read_b128 v[12:15], %[aA] offset:1024\n\tv_mfma_f32_16x16x32_bf16 a[64:67], v[88:91], v[72:75], a[64:67]\n\tds_read_b128 v[16:19], %[aA] offset:2048\n\tv_mfma_f32_16x16x32_bf16 a[80:83], v[88:91], v[76:79], a[80:83]\n\tds_read_b128 v[20:23], %[aA] offset:3072\n\tv_mfma_f32_16x16x32_bf16 a[96:99], v[88:91], v[80:83], a[96:99]\n\tds_read_b128 v[24:27], %[aA] offset:4096\n\tv_mfma_f32_16x16x32_bf16 a[112:115], v[88:91], v[84:87], a[112:115]\n\tds_read_b128 v[28:31], %[aA] offset:5120\n\tv_mfma_f32_16x16x32_bf16 a[4:7], v[92:95], v[56:59], a[4:7]\n\tds_read_b128 v[32:35], %[aA] offset:6144\n\tv_mfma_f32_16x16x32_bf16 a[20:23], v[92:95], v[60:63], a[20:23]\n\tds_read_b128 v[36:39], %[aA] offset:7168\n\tv_mfma_f32_16x16x32_bf16 a[36:39], v[92:95], v[64:67], a[36:39]\n\tds_read_b64_tr_b16 v[44:45], %[b1] offset:0\n\tv_mfma_f32_16x16x32_bf16 a[52:55], v[92:95], v[68:71], a[52:55]\n\tds_read_b64_tr_b16 v[46:47], %[b1] offset:4096\n\tv_mfma_f32_16x16x32_bf16 a[68:71], v[92:95], v[72:75], a[68:71]\n\tds_read_b64_tr_b16 v[48:49], %[b2] offset:0\n\tv_mfma_f32_16x16x32_bf16 a[84:87], v[92:95], v[76:79], a[84:87]\n\tds_read_b64_tr_b16 v[50:51], %[b2] offset:4096\n\tv_mfma_f32_16x16x32_bf16 a[100:103], v[92:95], v[80:83], a[100:103]\n\tds_read_b64_tr_b16 v[52:53], %[b3] offset:0\n\tv_mfma_f32_16x16x32_bf16 a[116:119], v[92:95], v[84:87], a[116:119]\n\tds_read_b64_tr_b16 v[54:55], %[b3] offset:4096\n\tv_mfma_f32_16x16x32_bf16 a[8:11], v[96:99], v[56:59], a[8:11]\n\ts_add_u32 %[slot], %[slot], 1\n\tv_mfma_f32_16x16x32_bf16 a[24:27], v[96:99], v[60:63], a[24:27]\n\ts_cmp_eq_u32 %[slot], 6\n\tv_mfma_f32_16x16x32_bf16 a[40:43], v[96:99], v[64:67], a[40:43]\n\ts_cselect_b32 %[dlt], %[cW], %[cS]\n\tv_mfma_f32_16x16x32_bf16 a[56:59], v[96:99], v[68:71], a[56:59]\n\ts_cselect_b32 %[slot], 0, %[slot]\n\tv_mfma_f32_16x16x32_bf16 a[72:75], v[96:99], v[72:75], a[72:75]\n\tv_add_u32 %[aA], %[dlt], %[aA]\n\tv_mfma_f32_16x16x32_bf16 a[88:91], v[96:99], v[76:79], a[88:91]\n\tv_add_u32 %[b0], %[dlt], %[b0]\n\tv_mfma_f32_16x16x32_bf16 a[104:107], v[96:99], v[80:83], a[104:107]\n\tv_add_u32 %[b1], %[dlt], %[b1]\n\tv_mfma_f32_16x16x32_bf16 a[120:123], v[96:99], v[84:87], a[120:123]\n\tv_add_u32 %[b2], %[dlt], %[b2]\n\tv_mfma_f32_16x16x32_bf16 a[12:15], v[100:103], v[56:59], a[12:15]\n\tv_add_u32 %[b3], %[dlt], %[b3]\n\tv_mfma_f32_16x16x32_bf16 a[28:31], v[100:103], v[60:63], a[28:31]\n\tv_mfma_f32_16x16x32_bf16 a[44:47], v[100:103], v[64:67], a[44:47]\n\tv_mfma_f32_16x16x32_bf16 a[60:63], v[100:103], v[68:71], a[60:63]\n\tv_mfma_f32_16x16x32_bf16 a[76:79], v[100:103], v[72:75], a[76:79]\n\tv_mfma_f32_16x16x32_bf16 a[92:95], v[100:103], v[76:79], a[92:95]\n\ts_waitcnt lgkmcnt(0)\n\ts_barrier\n\tv_mfma_f32_16x16x32_bf16 a[108:111], v[100:103], v[80:83], a[108:111]\n\tv_mfma_f32_16x16x32_bf16 a[124:127], v[100:103], v[84:87], a[124:127]\n\ts_sub_u32 %[cnt], %[cnt], 1\n\ts_cmp_lg_u32 %[cnt], 0\n\ts_cbranch_scc1 G2_loop%=\n\tv_mfma_f32_16x16x32_bf16 a[0:3], v[40:43], v[8:11], a[0:3]\n\tds_read_b128 v[56:59], %[aA]\n\tv_mfma_f32_16x16x32_bf16 a[16:19], v[40:43], v[12:15], a[16:19]\n\tds_read_b64_tr_b16 v[88:89], %[b0] offset:0\n\tv_mfma_f32_16x16x32_bf16 a[32:35], v[40:43], v[16:19], a[32:35]\n\tds_read_b64_tr_b16 v[90:91], %[b0] offset:4096\n\tv_mfma_f32_16x16x32_bf16 a[48:51], v[40:43], v[20:23], a[48:51]\n\tds_read_b128 v[60:63], %[aA] offset:1024\n\tv_mfma_f32_16x16x32_bf16 a[64:67], v[40:43], v[24:27], a[64:67]\n\tds_read_b128 v[64:67], %[aA] offset:2048\n\tv_mfma_f32_16x16x32_bf16 a[80:83], v[40:43], v[28:31], a[80:83]\n\tds_read_b128 v[68:71], %[aA] offset:3072\n\tv_mfma_f32_16x16x32_bf16 a[96:99], v[40:43], v[32:35], a[96:99]\n\tds_read_b128 v[72:75], %[aA] offset:4096\n\tv_mfma_f32_16x16x32_bf16 a[112:115], v[40:43], v[36:39], a[112:115]\n\tds_read_b128 v[76:79], %[aA] offset:5120\n\tv_mfma_f32_16x16x32_bf16 a[4:7], v[44:47], v[8:11], a[4:7]\n\tds_read_b128 v[80:83], %[aA] offset:6144\n\tv_mfma_f32_16x16x32_bf16 a[20:23], v[44:47], v[12:15], a[20:23]\n\tds_read_b128 v[84:87], %[aA] offset:7168\n\tv_mfma_f32_16x16x32_bf16 a[36:39], v[44:47], v[16:19], a[36:39]\n\tds_read_b64_tr_b16 v[92:93], %[b1] offset:0\n\tv_mfma_f32_16x16x32_bf16 a[52:55], v[44:47], v[20:23], a[52:55]\n\tds_read_b64_tr_b16 v[94:95], %[b1] offset:4096\n\tv_mfma_f32_16x16x32_bf16 a[68:71], v[44:47], v[24:27], a[68:71]\n\tds_read_b64_tr_b16 v[96:97], %[b2] offset:0\n\tv_mfma_f32_16x16x32_bf16 a[84:87], v[44:47], v[28:31], a[84:87]\n\tds_read_b64_tr_b16 v[98:99], %[b2] offset:4096\n\tv_mfma_f32_16x16x32_bf16 a[100:103], v[44:47], v[32:35], a[100:103]\n\tds_read_b64_tr_b16 v[100:101], %[b3] offset:0\n\tv_mfma_f32_16x16x32_bf16 a[116:119], v[44:47], v[36:39], a[116:119]\n\tds_read_b64_tr_b16 v[102:103], %[b3] offset:4096\n\tv_mfma_f32_16x16x32_bf16 a[8:11], v[48:51], v[8:11], a[8:11]\n\ts_add_u32 %[slot], %[slot], 1\n\tv_mfma_f32_16x16x32_bf16 a[24:27], v[48:51], v[12:15], a[24:27]\n\ts_cmp_eq_u32 %[slot], 6\n\tv_mfma_f32_16x16x32_bf16 a[40:43], v[48:51], v[16:19], a[40:43]\n\ts_cselect_b32 %[dlt], %[cW], %[cS]\n\tv_mfma_f32_16x16x32_bf16 a[56:59], v[48:51], v[20:23], a[56:59]\n\ts_cselect_b32 %[slot], 0, %[slot]\n\tv_mfma_f32_16x16x32_bf16 a[72:75], v[48:51], v[24:27], a[72:75]\n\tv_add_u32 %[aA], %[dlt], %[aA]\n\tv_mfma_f32_16x16x32_bf16 a[88:91], v[48:51], v[28:31], a[88:91]\n\tv_add_u32 %[b0], %[dlt], %[b0]\n\tv_mfma_f32_16x16x32_bf16 a[104:107], v[48:51], v[32:35], a[104:107]\n\tv_add_u32 %[b1], %[dlt], %[b1]\n\tv_mfma_f32_16x16x32_bf16 a[120:123], v[48:51], v[36:39], a[120:123]\n\tv_add_u32 %[b2], %[dlt], %[b2]\n\tv_mfma_f32_16x16x32_bf16 a[12:15], v[52:55], v[8:11], a[12:15]\n\tv_add_u32 %[b3], %[dlt], %[b3]\n\tv_mfma_f32_16x16x32_bf16 a[28:31], v[52:55], v[12:15], a[28:31]\n\tv_mfma_f32_16x16x32_bf16 a[44:47], v[52:55], v[16:19], a[44:47]\n\tv_mfma_f32_16x16x32_bf16 a[60:63], v[52:55], v[20:23], a[60:63]\n\tv_mfma_f32_16x16x32_bf16 a[76:79], v[52:55], v[24:27], a[76:79]\n\tv_mfma_f32_16x16x32_bf16 a[92:95], v[52:55], v[28:31], a[92:95]\n\tv_mfma_f32_16x16x32_bf16 a[108:111], v[52:55], v[32:35], a[108:111]\n\tv_mfma_f32_16x16x32_bf16 a[124:127], v[52:55], v[36:39], a[124:127]\n\ts_waitcnt lgkmcnt(0)\n\tv_mfma_f32_16x16x32_bf16 a[0:3], v[88:91], v[56:59], a[0:3]\n\tv_mfma_f32_16x16x32_bf16 a[16:19], v[88:91], v[60:63], a[16:19]\n\tv_mfma_f32_16x16x32_bf16 a[32:35], v[88:91], v[64:67], a[32:35]\n\tv_mfma_f32_16x16x32_bf16 a[48:51], v[88:91], v[68:71], a[48:51]\n\tv_mfma_f32_16x16x32_bf16 a[64:67], v[88:91], v[72:75], a[64:67]\n\tv_mfma_f32_16x16x32_bf16 a[80:83], v[88:91], v[76:79], a[80:83]\n\tv_mfma_f32_16x16x32_bf16 a[96:99], v[88:91], v[80:83], a[96:99]\n\tv_mfma_f32_16x16x32_bf16 a[112:115], v[88:91], v[84:87], a[112:115]\n\tv_mfma_f32_16x16x32_bf16 a[4:7], v[92:95], v[56:59], a[4:7]\n\tv_mfma_f32_16x16x32_bf16 a[20:23], v[92:95], v[60:63], a[20:23]\n\tv_mfma_f32_16x16x32_bf16 a[36:39], v[92:95], v[64:67], a[36:39]\n\tv_mfma_f32_16x16x32_bf16 a[52:55], v[92:95], v[68:71], a[52:55]\n\tv_mfma_f32_16x16x32_bf16 a[68:71], v[92:95], v[72:75], a[68:71]\n\tv_mfma_f32_16x16x32_bf16 a[84:87], v[92:95], v[76:79], a[84:87]\n\tv_mfma_f32_16x16x32_bf16 a[100:103], v[92:95], v[80:83], a[100:103]\n\tv_mfma_f32_16x16x32_bf16 a[116:119], v[92:95], v[84:87], a[116:119]\n\tv_mfma_f32_16x16x32_bf16 a[8:11], v[96:99], v[56:59], a[8:11]\n\tv_mfma_f32_16x16x32_bf16 a[24:27], v[96:99], v[60:63], a[24:27]\n\tv_mfma_f32_16x16x32_bf16 a[40:43], v[96:99], v[64:67], a[40:43]\n\tv_mfma_f32_16x16x32_bf16 a[56:59], v[96:99], v[68:71], a[56:59]\n\tv_mfma_f32_16x16x32_bf16 a[72:75], v[96:99], v[72:75], a[72:75]\n\tv_mfma_f32_16x16x32_bf16 a[88:91], v[96:99], v[76:79], a[88:91]\n\tv_mfma_f32_16x16x32_bf16 a[104:107], v[96:99], v[80:83], a[104:107]\n\tv_mfma_f32_16x16x32_bf16 a[120:123], v[96:99], v[84:87], a[120:123]\n\tv_mfma_f32_16x16x32_bf16 a[12:15], v[100:103], v[56:59], a[12:15]\n\tv_mfma_f32_16x16x32_bf16 a[28:31], v[100:103], v[60:63], a[28:31]\n\tv_mfma_f32_16x16x32_bf16 a[44:47], v[100:103], v[64:67], a[44:47]\n\tv_mfma_f32_16x16x32_bf16 a[60:63], v[100:103], v[68:71], a[60:63]\n\tv_mfma_f32_16x16x32_bf16 a[76:79], v[100:103], v[72:75], a[76:79]\n\tv_mfma_f32_16x16x32_bf16 a[92:95], v[100:103], v[76:79], a[92:95]\n\tv_mfma_f32_16x16x32_bf16 a[108:111], v[100:103], v[80:83], a[108:111]\n\tv_mfma_f32_16x16x32_bf16 a[124:127], v[100:103], v[84:87], a[124:127]\n\ts_nop 7\n\ts_nop 7"
#define G2_CLOB "v8","v9","v10","v11","v12","v13","v14","v15","v16","v17","v18","v19","v20","v21","v22","v23","v24","v25","v26","v27","v28","v29","v30","v31","v32","v33","v34","v35","v36","v37","v38","v39","v40","v41","v42","v43","v44","v45","v46","v47","v48","v49","v50","v51","v52","v53","v54","v55","v56","v57","v58","v59","v60","v61","v62","v63","v64","v65","v66","v67","v68","v69","v70","v71","v72","v73","v74","v75","v76","v77","v78","v79","v80","v81","v82","v83","v84","v85","v86","v87","v88","v89","v90","v91","v92","v93","v94","v95","v96","v97","v98","v99","v100","v101","v102","v103","a0","a1","a2","a3","a4","a5","a6","a7","a8","a9","a10","a11","a12","a13","a14","a15","a16","a17","a18","a19","a20","a21","a22","a23","a24","a25","a26","a27","a28","a29","a30","a31","a32","a33","a34","a35","a36","a37","a38","a39","a40","a41","a42","a43","a44","a45","a46","a47","a48","a49","a50","a51","a52","a53","a54","a55","a56","a57","a58","a59","a60","a61","a62","a63","a64","a65","a66","a67","a68","a69","a70","a71","a72","a73","a74","a75","a76","a77","a78","a79","a80","a81","a82","a83","a84","a85","a86","a87","a88","a89","a90","a91","a92","a93","a94","a95","a96","a97","a98","a99","a100","a101","a102","a103","a104","a105","a106","a107","a108","a109","a110","a111","a112","a113","a114","a115","a116","a117","a118","a119","a120","a121","a122","a123","a124","a125","a126","a127"
#define L1_0_ASM "s_mov_b32 s40, %[oA]\n\ts_mov_b32 s41, %[oH]\n\ts_mov_b32 s42, %[oQ]\n\ts_add_u32 m0, %[lds0], 0\n\ts_nop 0\n\tbuffer_load_dwordx4 %[vA0], %[rA], s40 offen offset:0 lds\n\tbuffer_load_dwordx4 %[vA1], %[rA], s40 offen offset:1024 lds\n\tbuffer_load_dwordx4 %[vA2], %[rA], s40 offen offset:2048 lds\n\ts_add_u32 m0, %[lds0], 8192\n\ts_nop 0\n\tbuffer_load_dwordx4 %[vH0], %[rH], s41 offen offset:0 lds\n\tbuffer_load_dwordx4 %[vH1], %[rH], s41 offen offset:1024 lds\n\tbuffer_load_dwordx4 %[vH2], %[rH], s41 offen offset:2048 lds\n\tbuffer_load_dwordx4 %[vH3], %[rH], s41 offen offset:3072 lds\n\ts_add_u32 m0, %[lds0], 12288\n\ts_nop 0\n\tbuffer_load_dwordx4 %[vH4], %[rH], s41 offen offset:0 lds\n\ts_add_u32 m0, %[lds0], 22528\n\ts_nop 0\n\tbuffer_load_dwordx4 %[vQ0], %[rQ], s42 offen offset:0 lds\n\ts_add_u32 s40, s40, 1024\n\ts_add_u32 s41, s41, 229376\n\ts_add_u32 s42, s42, 65536\n\ts_add_u32 m0, %[lds0], 26624\n\ts_nop 0\n\tbuffer_load_dwordx4 %[vA0], %[rA], s40 offen offset:0 lds\n\tbuffer_load_dwordx4 %[vA1], %[rA], s40 offen offset:1024 lds\n\tbuffer_load_dwordx4 %[vA2], %[rA], s40 offen offset:2048 lds\n\ts_add_u32 m0, %[lds0], 34816\n\ts_nop 0\n\tbuffer_load_dwordx4 %[vH0], %[rH], s41 offen offset:0 lds\n\tbuffer_load_dwordx4 %[vH1], %[rH], s41 offen offset:1024 lds\n\tbuffer_load_dwordx4 %[vH2], %[rH], s41 offen offset:2048 lds\n\tbuffer_load_dwordx4 %[vH3], %[rH], s41 offen offset:3072 lds\n\ts_add_u32 m0, %[lds0], 38912\n\ts_nop 0\n\tbuffer_load_dwordx4 %[vH4], %[rH], s41 offen offset:0 lds\n\ts_add_u32 m0, %[lds0], 49152\n\ts_nop 0\n\tbuffer_load_dwordx4 %[vQ0], %[rQ], s42 offen offset:0 lds\n\ts_add_u32 s40, s40, 1024\n\ts_add_u32 s41, s41, 229376\n\ts_add_u32 s42, s42, 65536\n\ts_add_u32 m0, %[lds0], 53248\n\ts_nop 0\n\tbuffer_load_dwordx4 %[vA0], %[rA], s40 offen offset:0 lds\n\tbuffer_load_dwordx4 %[vA1], %[rA], s40 offen offset:1024 lds\n\tbuffer_load_dwordx4 %[vA2], %[rA], s40 offen offset:2048 lds\n\ts_add_u32 m0, %[lds0], 61440\n\ts_nop 0\n\tbuffer_load_dwordx4 %[vH0], %[rH], s41 offen offset:0 lds\n\tbuffer_load_dwordx4 %[vH1], %[rH], s41 offen offset:1024 lds\n\tbuffer_load_dwordx4 %[vH2], %[rH], s41 offen offset:2048 lds\n\tbuffer_load_dwordx4 %[vH3], %[rH], s41 offen offset:3072 lds\n\ts_add_u32 m0, %[lds0], 65536\n\ts_nop 0\n\tbuffer_load_dwordx4 %[vH4], %[rH], s41 offen offset:0 lds\n\ts_add_u32 m0, %[lds0], 75776\n\ts_nop 0\n\tbuffer_load_dwordx4 %[vQ0], %[rQ], s42 offen offset:0 lds\n\ts_add_u32 s40, s40, 1024\n\ts_add_u32 s41, s41, 229376\n\ts_add_u32 s42, s42, 65536\n\ts_add_u32 m0, %[lds0], 79872\n\ts_nop 0\n\tbuffer_load_dwordx4 %[vA0], %[rA], s40 offen offset:0 lds\n\tbuffer_load_dwordx4 %[vA1], %[rA], s40 offen offset:1024 lds\n\tbuffer_load_dwordx4 %[vA2], %[rA], s40 offen offset:2048 lds\n\ts_add_u32 m0, %[lds0], 88064\n\ts_nop 0\n\tbuffer_load_dwordx4 %[vH0], %[rH], s41 offen offset:0 lds\n\tbuffer_load_dwordx4 %[vH1], %[rH], s41 offen offset:1024 lds\n\tbuffer_load_dwordx4 %[vH2], %[rH], s41 offen offset:2048 lds\n\tbuffer_load_dwordx4 %[vH3], %[rH], s41 offen offset:3072 lds\n\ts_add_u32 m0, %[lds0], 92160\n\ts_nop 0\n\tbuffer_load_dwordx4 %[vH4], %[rH], s41 offen offset:0 lds\n\ts_add_u32 m0, %[lds0], 102400\n\ts_nop 0\n\tbuffer_load_dwordx4 %[vQ0], %[rQ], s42 offen offset:0 lds\n\ts_add_u32 s40, s40, 1024\n\ts_add_u32 s41, s41, 229376\n\ts_add_u32 s42, s42, 65536\n\ts_add_u32 m0, %[lds0], 106496\n\ts_nop 0\n\tbuffer_load_dwordx4 %[vA0], %[rA], s40 offen offset:0 lds\n\tbuffer_load_dwordx4 %[vA1], %[rA], s40 offen offset:1024 lds\n\tbuffer_load_dwordx4 %[vA2], %[rA], s40 offen offset:2048 lds\n\ts_add_u32 m0, %[lds0], 114688\n\ts_nop 0\n\tbuffer_load_dwordx4 %[vH0], %[rH], s41 offen offset:0 lds\n\tbuffer_load_dwordx4 %[vH1], %[rH], s41 offen offset:1024 lds\n\tbuffer_load_dwordx4 %[vH2], %[rH], s41 offen offset:2048 lds\n\tbuffer_load_dwordx4 %[vH3], %[rH], s41 offen offset:3072 lds\n\ts_add_u32 m0, %[lds0], 118784\n\ts_nop 0\n\tbuffer_load_dwordx4 %[vH4], %[rH], s41 offen offset:0 lds\n\ts_add_u32 m0, %[lds0], 129024\n\ts_nop 0\n\tbuffer_load_dwordx4 %[vQ0], %[rQ], s42 offen offset:0 lds\n\ts_add_u32 s40, s40, 1024\n\ts_add_u32 s41, s41, 229376\n\ts_add_u32 s42, s42, 65536\n\ts_add_u32 m0, %[lds0], 133120\n\ts_nop 0\n\tbuffer_load_dwordx4 %[vA0], %[rA], s40 offen offset:0 lds\n\tbuffer_load_dwordx4 %[vA1], %[rA], s40 offen offset:1024 lds\n\tbuffer_load_dwordx4 %[vA2], %[rA], s40 offen offset:2048 lds\n\ts_add_u32 m0, %[lds0], 141312\n\ts_nop 0\n\tbuffer_load_dwordx4 %[vH0], %[rH], s41 offen offset:0 lds\n\tbuffer_load_dwordx4 %[vH1], %[rH], s41 offen offset:1024 lds\n\tbuffer_load_dwordx4 %[vH2], %[rH], s41 offen offset:2048 lds\n\tbuffer_load_dwordx4 %[vH3], %[rH], s41 offen offset:3072 lds\n\ts_add_u32 m0, %[lds0], 145408\n\ts_nop 0\n\tbuffer_load_dwordx4 %[vH4], %[rH], s41 offen offset:0 lds\n\ts_add_u32 m0, %[lds0], 155648\n\ts_nop 0\n\tbuffer_load_dwordx4 %[vQ0], %[rQ], s42 offen offset:0 lds\n\ts_add_u32 s40, s40, 1024\n\ts_add_u32 s41, s41, 229376\n\ts_add_u32 s42, s42, 65536\n\ts_waitcnt vmcnt(45)\n\ts_barrier\n\tds_read_b128 v[40:43], %[qa0]\n\ts_waitcnt lgkmcnt(0)\n\tv_dot2_f32_bf16 %[cs0], v40, %[c10], %[cs0]\n\tv_dot2_f32_bf16 %[cs1], v40, %[c01], %[cs1]\n\tv_dot2_f32_bf16 %[cs2], v41, %[c10], %[cs2]\n\tv_dot2_f32_bf16 %[cs3], v41, %[c01], %[cs3]\n\tv_dot2_f32_bf16 %[cs4], v42, %[c10], %[cs4]\n\tv_dot2_f32_bf16 %[cs5], v42, %[c01], %[cs5]\n\tv_dot2_f32_bf16 %[cs6], v43, %[c10], %[cs6]\n\tv_dot2_f32_bf16 %[cs7], v43, %[c01], %[cs7]\n\ts_waitcnt vmcnt(27)\n\ts_barrier\n\tds_read_b128 v[40:43], %[qa1]\n\tds_read_b128 v[44:47], %[qa2]\n\ts_add_u32 m0, %[lds0], 0\n\ts_nop 0\n\tbuffer_load_dwordx4 %[vA0], %[rA], s40 offen offset:0 lds\n\tbuffer_load_dwordx4 %[vA1], %[rA], s40 offen offset:1024 lds\n\tbuffer_load_dwordx4 %[vA2], %[rA], s40 offen offset:2048 lds\n\ts_add_u32 m0, %[lds0], 8192\n\ts_nop 0\n\tbuffer_load_dwordx4 %[vH0], %[rH], s41 offen offset:0 lds\n\tbuffer_load_dwordx4 %[vH1], %[rH], s41 offen offset:1024 lds\n\tbuffer_load_dwordx4 %[vH2], %[rH], s41 offen offset:2048 lds\n\tbuffer_load_dwordx4 %[vH3], %[rH], s41 offen offset:3072 lds\n\ts_add_u32 m0, %[lds0], 12288\n\ts_nop 0\n\tbuffer_load_dwordx4 %[vH4], %[rH], s41 offen offset:0 lds\n\ts_add_u32 m0, %[lds0], 22528\n\ts_nop 0\n\tbuffer_load_dwordx4 %[vQ0], %[rQ], s42 offen offset:0 lds\n\ts_add_u32 s40, s40, 1024\n\ts_add_u32 s41, s41, 229376\n\ts_add_u32 s42, s42, 65536\n\ts_waitcnt lgkmcnt(0)\n\tv_dot2_f32_bf16 %[cs0], v40, %[c10], %[cs0]\n\tv_dot2_f32_bf16 %[cs1], v40, %[c01], %[cs1]\n\tv_dot2_f32_bf16 %[cs2], v41, %[c10], %[cs2]\n\tv_dot2_f32_bf16 %[cs3], v41, %[c01], %[cs3]\n\tv_dot2_f32_bf16 %[cs4], v42, %[c10], %[cs4]\n\tv_dot2_f32_bf16 %[cs5], v42, %[c01], %[cs5]\n\tv_dot2_f32_bf16 %[cs6], v43, %[c10], %[cs6]\n\tv_dot2_f32_bf16 %[cs7], v43, %[c01], %[cs7]\n\tv_dot2_f32_bf16 %[cs0], v44, %[c10], %[cs0]\n\tv_dot2_f32_bf16 %[cs1], v44, %[c01], %[cs1]\n\tv_dot2_f32_bf16 %[cs2], v45, %[c10], %[cs2]\n\tv_dot2_f32_bf16 %[cs3], v45, %[c01], %[cs3]\n\tv_dot2_f32_bf16 %[cs4], v46, %[c10], %[cs4]\n\tv_dot2_f32_bf16 %[cs5], v46, %[c01], %[cs5]\n\tv_dot2_f32_bf16 %[cs6], v47, %[c10], %[cs6]\n\tv_dot2_f32_bf16 %[cs7], v47, %[c01], %[cs7]\n\ts_mov_b32 s66, 4\n\tL10_l0_%=:\n\ts_waitcnt vmcnt(18)\n\ts_barrier\n\tds_read_b128 v[40:43], %[qa3]\n\tds_read_b128 v[44:47], %[qa4]\n\ts_add_u32 m0, %[lds0], 26624\n\ts_nop 0\n\tbuffer_load_dwordx4 %[vA0], %[rA], s40 offen offset:0 lds\n\tbuffer_load_dwordx4 %[vA1], %[rA], s40 offen offset:1024 lds\n\tbuffer_load_dwordx4 %[vA2], %[rA], s40 offen offset:2048 lds\n\ts_add_u32 m0, %[lds0], 34816\n\ts_nop 0\n\tbuffer_load_dwordx4 %[vH0], %[rH], s41 offen offset:0 lds\n\tbuffer_load_dwordx4 %[vH1], %[rH], s41 offen offset:1024 lds\n\tbuffer_load_dwordx4 %[vH2], %[rH], s41 offen offset:2048 lds\n\tbuffer_load_dwordx4 %[vH3], %[rH], s41 offen offset:3072 lds\n\ts_add_u32 m0, %[lds0], 38912\n\ts_nop 0\n\tbuffer_load_dwordx4 %[vH4], %[rH], s41 offen offset:0 lds\n\ts_add_u32 m0, %[lds0], 49152\n\ts_nop 0\n\tbuffer_load_dwordx4 %[vQ0], %[rQ], s42 offen offset:0 lds\n\ts_add_u32 s40, s40, 1024\n\ts_add_u32 s41, s41, 229376\n\ts_add_u32 s42, s42, 65536\n\ts_add_u32 m0, %[lds0], 53248\n\ts_nop 0\n\tbuffer_load_dwordx4 %[vA0], %[rA], s40 offen offset:0 lds\n\tbuffer_load_dwordx4 %[vA1], %[rA], s40 offen offset:1024 lds\n\tbuffer_load_dwordx4 %[vA2], %[rA], s40 offen offset:2048 lds\n\ts_add_u32 m0, %[lds0], 61440\n\ts_nop 0\n\tbuffer_load_dwordx4 %[vH0], %[rH], s41 offen offset:0 lds\n\tbuffer_load_dwordx4 %[vH1], %[rH], s41 offen offset:1024 lds\n\tbuffer_load_dwordx4 %[vH2], %[rH], s41 offen offset:2048 lds\n\tbuffer_load_dwordx4 %[vH3], %[rH], s41 offen offset:3072 lds\n\ts_add_u32 m0, %[lds0], 65536\n\ts_nop 0\n\tbuffer_load_dwordx4 %[vH4], %[rH], s41 offen offset:0 lds\n\ts_add_u32 m0, %[lds0], 75776\n\ts_nop 0\n\tbuffer_load_dwordx4 %[vQ0], %[rQ], s42 offen offset:0 lds\n\ts_add_u32 s40, s40, 1024\n\ts_add_u32 s41, s41, 229376\n\ts_add_u32 s42, s42, 65536\n\ts_waitcnt lgkmcnt(0)\n\tv_dot2_f32_bf16 %[cs0], v40, %[c10], %[cs0]\n\tv_dot2_f32_bf16 %[cs1], v40, %[c01], %[cs1]\n\tv_dot2_f32_bf16 %[cs2], v41, %[c10], %[cs2]\n\tv_dot2_f32_bf16 %[cs3], v41, %[c01], %[cs3]\n\tv_dot2_f32_bf16 %[cs4], v42, %[c10], %[cs4]\n\tv_dot2_f32_bf16 %[cs5], v42, %[c01], %[cs5]\n\tv_dot2_f32_bf16 %[cs6], v43, %[c10], %[cs6]\n\tv_dot2_f32_bf16 %[cs7], v43, %[c01], %[cs7]\n\tv_dot2_f32_bf16 %[cs0], v44, %[c10], %[cs0]\n\tv_dot2_f32_bf16 %[cs1], v44, %[c01], %[cs1]\n\tv_dot2_f32_bf16 %[cs2], v45, %[c10], %[cs2]\n\tv_dot2_f32_bf16 %[cs3], v45, %[c01], %[cs3]\n\tv_dot2_f32_bf16 %[cs4], v46, %[c10], %[cs4]\n\tv_dot2_f32_bf16 %[cs5], v46, %[c01], %[cs5]\n\tv_dot2_f32_bf16 %[cs6], v47, %[c10], %[cs6]\n\tv_dot2_f32_bf16 %[cs7], v47, %[c01], %[cs7]\n\ts_waitcnt vmcnt(18)\n\ts_barrier\n\tds_read_b128 v[40:43], %[qa5]\n\tds_read_b128 v[44:47], %[qa0]\n\ts_add_u32 m0, %[lds0], 79872\n\ts_nop 0\n\tbuffer_load_dwordx4 %[vA0], %[rA], s40 offen offset:0 lds\n\tbuffer_load_dwordx4 %[vA1], %[rA], s40 offen offset:1024 lds\n\tbuffer_load_dwordx4 %[vA2], %[rA], s40 offen offset:2048 lds\n\ts_add_u32 m0, %[lds0], 88064\n\ts_nop 0\n\tbuffer_load_dwordx4 %[vH0], %[rH], s41 offen offset:0 lds\n\tbuffer_load_dwordx4 %[vH1], %[rH], s41 offen offset:1024 lds\n\tbuffer_load_dwordx4 %[vH2], %[rH], s41 offen offset:2048 lds\n\tbuffer_load_dwordx4 %[vH3], %[rH], s41 offen offset:3072 lds\n\ts_add_u32 m0, %[lds0], 92160\n\ts_nop 0\n\tbuffer_load_dwordx4 %[vH4], %[rH], s41 offen offset:0 lds\n\ts_add_u32 m0, %[lds0], 102400\n\ts_nop 0\n\tbuffer_load_dwordx4 %[vQ0], %[rQ], s42 offen offset:0 lds\n\ts_add_u32 s40, s40, 1024\n\ts_add_u32 s41, s41, 229376\n\ts_add_u32 s42, s42, 65536\n\ts_add_u32 m0, %[lds0], 106496\n\ts_nop 0\n\tbuffer_load_dwordx4 %[vA0], %[rA], s40 offen offset:0 lds\n\tbuffer_load_dwordx4 %[vA1], %[rA], s40 offen offset:1024 lds\n\tbuffer_load_dwordx4 %[vA2], %[rA], s40 offen offset:2048 lds\n\ts_add_u32 m0, %[lds0], 114688\n\ts_nop 0\n\tbuffer_load_dwordx4 %[vH0], %[rH], s41 offen offset:0 lds\n\tbuffer_load_dwordx4 %[vH1], %[rH], s41 offen offset:1024 lds\n\tbuffer_load_dwordx4 %[vH2], %[rH], s41 offen offset:2048 lds\n\tbuffer_load_dwordx4 %[vH3], %[rH], s41 offen offset:3072 lds\n\ts_add_u32 m0, %[lds0], 118784\n\ts_nop 0\n\tbuffer_load_dwordx4 %[vH4], %[rH], s41 offen offset:0 lds\n\ts_add_u32 m0, %[lds0], 129024\n\ts_nop 0\n\tbuffer_load_dwordx4 %[vQ0], %[rQ], s42 offen offset:0 lds\n\ts_add_u32 s40, s40, 1024\n\ts_add_u32 s41, s41, 229376\n\ts_add_u32 s42, s42, 65536\n\ts_waitcnt lgkmcnt(0)\n\tv_dot2_f32_bf16 %[cs0], v40, %[c10], %[cs0]\n\tv_dot2_f32_bf16 %[cs1], v40, %[c01], %[cs1]\n\tv_dot2_f32_bf16 %[cs2], v41, %[c10], %[cs2]\n\tv_dot2_f32_bf16 %[cs3], v41, %[c01], %[cs3]\n\tv_dot2_f32_bf16 %[cs4], v42, %[c10], %[cs4]\n\tv_dot2_f32_bf16 %[cs5], v42, %[c01], %[cs5]\n\tv_dot2_f32_bf16 %[cs6], v43, %[c10], %[cs6]\n\tv_dot2_f32_bf16 %[cs7], v43, %[c01], %[cs7]\n\tv_dot2_f32_bf16 %[cs0], v44, %[c10], %[cs0]\n\tv_dot2_f32_bf16 %[cs1], v44, %[c01], %[cs1]\n\tv_dot2_f32_bf16 %[cs2], v45, %[c10], %[cs2]\n\tv_dot2_f32_bf16 %[cs3], v45, %[c01], %[cs3]\n\tv_dot2_f32_bf16 %[cs4], v46, %[c10], %[cs4]\n\tv_dot2_f32_bf16 %[cs5], v46, %[c01], %[cs5]\n\tv_dot2_f32_bf16 %[cs6], v47, %[c10], %[cs6]\n\tv_dot2_f32_bf16 %[cs7], v47, %[c01], %[cs7]\n\ts_waitcnt vmcnt(18)\n\ts_barrier\n\tds_read_b128 v[40:43], %[qa1]\n\tds_read_b128 v[44:47], %[qa2]\n\ts_add_u32 m0, %[lds0], 133120\n\ts_nop 0\n\tbuffer_load_dwordx4 %[vA0], %[rA], s40 offen offset:0 lds\n\tbuffer_load_dwordx4 %[vA1], %[rA], s40 offen offset:1024 lds\n\tbuffer_load_dwordx4 %[vA2], %[rA], s40 offen offset:2048 lds\n\ts_add_u32 m0, %[lds0], 141312\n\ts_nop 0\n\tbuffer_load_dwordx4 %[vH0], %[rH], s41 offen offset:0 lds\n\tbuffer_load_dwordx4 %[vH1], %[rH], s41 offen offset:1024 lds\n\tbuffer_load_dwordx4 %[vH2], %[rH], s41 offen offset:2048 lds\n\tbuffer_load_dwordx4 %[vH3], %[rH], s41 offen offset:3072 lds\n\ts_add_u32 m0, %[lds0], 145408\n\ts_nop 0\n\tbuffer_load_dwordx4 %[vH4], %[rH], s41 offen offset:0 lds\n\ts_add_u32 m0, %[lds0], 155648\n\ts_nop 0\n\tbuffer_load_dwordx4 %[vQ0], %[rQ], s42 offen offset:0 lds\n\ts_add_u32 s40, s40, 1024\n\ts_add_u32 s41, s41, 229376\n\ts_add_u32 s42, s42, 65536\n\ts_add_u32 m0, %[lds0], 0\n\ts_nop 0\n\tbuffer_load_dwordx4 %[vA0], %[rA], s40 offen offset:0 lds\n\tbuffer_load_dwordx4 %[vA1], %[rA], s40 offen offset:1024 lds\n\tbuffer_load_dwordx4 %[vA2], %[rA], s40 offen offset:2048 lds\n\ts_add_u32 m0, %[lds0], 8192\n\ts_nop 0\n\tbuffer_load_dwordx4 %[vH0], %[rH], s41 offen offset:0 lds\n\tbuffer_load_dwordx4 %[vH1], %[rH], s41 offen offset:1024 lds\n\tbuffer_load_dwordx4 %[vH2], %[rH], s41 offen offset:2048 lds\n\tbuffer_load_dwordx4 %[vH3], %[rH], s41 offen offset:3072 lds\n\ts_add_u32 m0, %[lds0], 12288\n\ts_nop 0\n\tbuffer_load_dwordx4 %[vH4], %[rH], s41 offen offset:0 lds\n\ts_add_u32 m0, %[lds0], 22528\n\ts_nop 0\n\tbuffer_load_dwordx4 %[vQ0], %[rQ], s42 offen offset:0 lds\n\ts_add_u32 s40, s40, 1024\n\ts_add_u32 s41, s41, 229376\n\ts_add_u32 s42, s42, 65536\n\ts_waitcnt lgkmcnt(0)\n\tv_dot2_f32_bf16 %[cs0], v40, %[c10], %[cs0]\n\tv_dot2_f32_bf16 %[cs1], v40, %[c01], %[cs1]\n\tv_dot2_f32_bf16 %[cs2], v41, %[c10], %[cs2]\n\tv_dot2_f32_bf16 %[cs3], v41, %[c01], %[cs3]\n\tv_dot2_f32_bf16 %[cs4], v42, %[c10], %[cs4]\n\tv_dot2_f32_bf16 %[cs5], v42, %[c01], %[cs5]\n\tv_dot2_f32_bf16 %[cs6], v43, %[c10], %[cs6]\n\tv_dot2_f32_bf16 %[cs7], v43, %[c01], %[cs7]\n\tv_dot2_f32_bf16 %[cs0], v44, %[c10], %[cs0]\n\tv_dot2_f32_bf16 %[cs1], v44, %[c01], %[cs1]\n\tv_dot2_f32_bf16 %[cs2], v45, %[c10], %[cs2]\n\tv_dot2_f32_bf16 %[cs3], v45, %[c01], %[cs3]\n\tv_dot2_f32_bf16 %[cs4], v46, %[c10], %[cs4]\n\tv_dot2_f32_bf16 %[cs5], v46, %[c01], %[cs5]\n\tv_dot2_f32_bf16 %[cs6], v47, %[c10], %[cs6]\n\tv_dot2_f32_bf16 %[cs7], v47, %[c01], %[cs7]\n\ts_sub_u32 s66, s66, 1\n\ts_cmp_lg_u32 s66, 0\n\ts_cbranch_scc1 L10_l0_%=\n\ts_waitcnt vmcnt(18)\n\ts_barrier\n\tds_read_b128 v[40:43], %[qa3]\n\tds_read_b128 v[44:47], %[qa4]\n\ts_add_u32 m0, %[lds0], 26624\n\ts_nop 0\n\tbuffer_load_dwordx4 %[vA0], %[rA], s40 offen offset:0 lds\n\tbuffer_load_dwordx4 %[vA1], %[rA], s40 offen offset:1024 lds\n\tbuffer_load_dwordx4 %[vA2], %[rA], s40 offen offset:2048 lds\n\ts_add_u32 m0, %[lds0], 34816\n\ts_nop 0\n\tbuffer_load_dwordx4 %[vH0], %[rH], s41 offen offset:0 lds\n\tbuffer_load_dwordx4 %[vH1], %[rH], s41 offen offset:1024 lds\n\tbuffer_load_dwordx4 %[vH2], %[rH], s41 offen offset:2048 lds\n\tbuffer_load_dwordx4 %[vH3], %[rH], s41 offen offset:3072 lds\n\ts_add_u32 m0, %[lds0], 38912\n\ts_nop 0\n\tbuffer_load_dwordx4 %[vH4], %[rH], s41 offen offset:0 lds\n\ts_add_u32 m0, %[lds0], 49152\n\ts_nop 0\n\tbuffer_load_dwordx4 %[vQ0], %[rQ], s42 offen offset:0 lds\n\ts_add_u32 s40, s40, 1024\n\ts_add_u32 s41, s41, 229376\n\ts_add_u32 s42, s42, 65536\n\ts_waitcnt lgkmcnt(0)\n\tv_dot2_f32_bf16 %[cs0], v40, %[c10], %[cs0]\n\tv_dot2_f32_bf16 %[cs1], v40, %[c01], %[cs1]\n\tv_dot2_f32_bf16 %[cs2], v41, %[c10], %[cs2]\n\tv_dot2_f32_bf16 %[cs3], v41, %[c01], %[cs3]\n\tv_dot2_f32_bf16 %[cs4], v42, %[c10], %[cs4]\n\tv_dot2_f32_bf16 %[cs5], v42, %[c01], %[cs5]\n\tv_dot2_f32_bf16 %[cs6], v43, %[c10], %[cs6]\n\tv_dot2_f32_bf16 %[cs7], v43, %[c01], %[cs7]\n\tv_dot2_f32_bf16 %[cs0], v44, %[c10], %[cs0]\n\tv_dot2_f32_bf16 %[cs1], v44, %[c01], %[cs1]\n\tv_dot2_f32_bf16 %[cs2], v45, %[c10], %[cs2]\n\tv_dot2_f32_bf16 %[cs3], v45, %[c01], %[cs3]\n\tv_dot2_f32_bf16 %[cs4], v46, %[c10], %[cs4]\n\tv_dot2_f32_bf16 %[cs5], v46, %[c01], %[cs5]\n\tv_dot2_f32_bf16 %[cs6], v47, %[c10], %[cs6]\n\tv_dot2_f32_bf16 %[cs7], v47, %[c01], %[cs7]\n\ts_waitcnt vmcnt(9)\n\ts_barrier\n\tds_read_b128 v[40:43], %[qa5]\n\tds_read_b128 v[44:47], %[qa0]\n\ts_waitcnt lgkmcnt(0)\n\tv_dot2_f32_bf16 %[cs0], v40, %[c10], %[cs0]\n\tv_dot2_f32_bf16 %[cs1], v40, %[c01], %[cs1]\n\tv_dot2_f32_bf16 %[cs2], v41, %[c10], %[cs2]\n\tv_dot2_f32_bf16 %[cs3], v41, %[c01], %[cs3]\n\tv_dot2_f32_bf16 %[cs4], v42, %[c10], %[cs4]\n\tv_dot2_f32_bf16 %[cs5], v42, %[c01], %[cs5]\n\tv_dot2_f32_bf16 %[cs6], v43, %[c10], %[cs6]\n\tv_dot2_f32_bf16 %[cs7], v43, %[c01], %[cs7]\n\tv_dot2_f32_bf16 %[cs0], v44, %[c10], %[cs0]\n\tv_dot2_f32_bf16 %[cs1], v44, %[c01], %[cs1]\n\tv_dot2_f32_bf16 %[cs2], v45, %[c10], %[cs2]\n\tv_dot2_f32_bf16 %[cs3], v45, %[c01], %[cs3]\n\tv_dot2_f32_bf16 %[cs4], v46, %[c10], %[cs4]\n\tv_dot2_f32_bf16 %[cs5], v46, %[c01], %[cs5]\n\tv_dot2_f32_bf16 %[cs6], v47, %[c10], %[cs6]\n\tv_dot2_f32_bf16 %[cs7], v47, %[c01], %[cs7]\n\ts_waitcnt vmcnt(0)\n\ts_barrier\n\tds_read_b128 v[40:43], %[qa1]\n\ts_waitcnt lgkmcnt(0)\n\tv_dot2_f32_bf16 %[cs0], v40, %[c10], %[cs0]\n\tv_dot2_f32_bf16 %[cs1], v40, %[c01], %[cs1]\n\tv_dot2_f32_bf16 %[cs2], v41, %[c10], %[cs2]\n\tv_dot2_f32_bf16 %[cs3], v41, %[c01], %[cs3]\n\tv_dot2_f32_bf16 %[cs4], v42, %[c10], %[cs4]\n\tv_dot2_f32_bf16 %[cs5], v42, %[c01], %[cs5]\n\tv_dot2_f32_bf16 %[cs6], v43, %[c10], %[cs6]\n\tv_dot2_f32_bf16 %[cs7], v43, %[c01], %[cs7]\n\ts_nop 3"
#define L1_0_CLOB "s66","s40","s41","s42","v40","v41","v42","v43","v44","v45","v46","v47"
#define L1_1_ASM "s_mov_b32 s40, %[oA]\n\ts_mov_b32 s41, %[oH]\n\ts_mov_b32 s42, %[oQ]\n\ts_add_u32 m0, %[lds0], 3072\n\ts_nop 0\n\tbuffer_load_dwordx4 %[vA0], %[rA], s40 offen offset:0 lds\n\tbuffer_load_dwordx4 %[vA1], %[rA], s40 offen offset:1024 lds\n\tbuffer_load_dwordx4 %[vA2], %[rA], s40 offen offset:2048 lds\n\ts_add_u32 m0, %[lds0], 13312\n\ts_nop 0\n\tbuffer_load_dwordx4 %[vH0], %[rH], s41 offen offset:0 lds\n\tbuffer_load_dwordx4 %[vH1], %[rH], s41 offen offset:1024 lds\n\tbuffer_load_dwordx4 %[vH2], %[rH], s41 offen offset:2048 lds\n\tbuffer_load_dwordx4 %[vH3], %[rH], s41 offen offset:3072 lds\n\ts_add_u32 m0, %[lds0], 17408\n\ts_nop 0\n\tbuffer_load_dwordx4 %[vH4], %[rH], s41 offen offset:0 lds\n\ts_add_u32 m0, %[lds0], 23552\n\ts_nop 0\n\tbuffer_load_dwordx4 %[vQ0], %[rQ], s42 offen offset:0 lds\n\ts_add_u32 s40, s40, 1024\n\ts_add_u32 s41, s41, 229376\n\ts_add_u32 s42, s42, 65536\n\ts_add_u32 m0, %[lds0], 29696\n\ts_nop 0\n\tbuffer_load_dwordx4 %[vA0], %[rA], s40 offen offset:0 lds\n\tbuffer_load_dwordx4 %[vA1], %[rA], s40 offen offset:1024 lds\n\tbuffer_load_dwordx4 %[vA2], %[rA], s40 offen offset:2048 lds\n\ts_add_u32 m0, %[lds0], 39936\n\ts_nop 0\n\tbuffer_load_dwordx4 %[vH0], %[rH], s41 offen offset:0 lds\n\tbuffer_load_dwordx4 %[vH1], %[rH], s41 offen offset:1024 lds\n\tbuffer_load_dwordx4 %[vH2], %[rH], s41 offen offset:2048 lds\n\tbuffer_load_dwordx4 %[vH3], %[rH], s41 offen offset:3072 lds\n\ts_add_u32 m0, %[lds0], 44032\n\ts_nop 0\n\tbuffer_load_dwordx4 %[vH4], %[rH], s41 offen offset:0 lds\n\ts_add_u32 m0, %[lds0], 50176\n\ts_nop 0\n\tbuffer_load_dwordx4 %[vQ0], %[rQ], s42 offen offset:0 lds\n\ts_add_u32 s40, s40, 1024\n\ts_add_u32 s41, s41, 229376\n\ts_add_u32 s42, s42, 65536\n\ts_add_u32 m0, %[lds0], 56320\n\ts_nop 0\n\tbuffer_load_dwordx4 %[vA0], %[rA], s40 offen offset:0 lds\n\tbuffer_load_dwordx4 %[vA1], %[rA], s40 offen offset:1024 lds\n\tbuffer_load_dwordx4 %[vA2], %[rA], s40 offen offset:2048 lds\n\ts_add_u32 m0, %[lds0], 66560\n\ts_nop 0\n\tbuffer_load_dwordx4 %[vH0], %[rH], s41 offen offset:0 lds\n\tbuffer_load_dwordx4 %[vH1], %[rH], s41 offen offset:1024 lds\n\tbuffer_load_dwordx4 %[vH2], %[rH], s41 offen offset:2048 lds\n\tbuffer_load_dwordx4 %[vH3], %[rH], s41 offen offset:3072 lds\n\ts_add_u32 m0, %[lds0], 70656\n\ts_nop 0\n\tbuffer_load_dwordx4 %[vH4], %[rH], s41 offen offset:0 lds\n\ts_add_u32 m0, %[lds0], 76800\n\ts_nop 0\n\tbuffer_load_dwordx4 %[vQ0], %[rQ], s42 offen offset:0 lds\n\ts_add_u32 s40, s40, 1024\n\ts_add_u32 s41, s41, 229376\n\ts_add_u32 s42, s42, 65536\n\ts_add_u32 m0, %[lds0], 82944\n\ts_nop 0\n\tbuffer_load_dwordx4 %[vA0], %[rA], s40 offen offset:0 lds\n\tbuffer_load_dwordx4 %[vA1], %[rA], s40 offen offset:1024 lds\n\tbuffer_load_dwordx4 %[vA2], %[rA], s40 offen offset:2048 lds\n\ts_add_u32 m0, %[lds0], 93184\n\ts_nop 0\n\tbuffer_load_dwordx4 %[vH0], %[rH], s41 offen offset:0 lds\n\tbuffer_load_dwordx4 %[vH1], %[rH], s41 offen offset:1024 lds\n\tbuffer_load_dwordx4 %[vH2], %[rH], s41 offen offset:2048 lds\n\tbuffer_load_dwordx4 %[vH3], %[rH], s41 offen offset:3072 lds\n\ts_add_u32 m0, %[lds0], 97280\n\ts_nop 0\n\tbuffer_load_dwordx4 %[vH4], %[rH], s41 offen offset:0 lds\n\ts_add_u32 m0, %[lds0], 103424\n\ts_nop 0\n\tbuffer_load_dwordx4 %[vQ0], %[rQ], s42 offen offset:0 lds\n\ts_add_u32 s40, s40, 1024\n\ts_add_u32 s41, s41, 229376\n\ts_add_u32 s42, s42, 65536\n\ts_add_u32 m0, %[lds0], 109568\n\ts_nop 0\n\tbuffer_load_dwordx4 %[vA0], %[rA], s40 offen offset:0 lds\n\tbuffer_load_dwordx4 %[vA1], %[rA], s40 offen offset:1024 lds\n\tbuffer_load_dwordx4 %[vA2], %[rA], s40 offen offset:2048 lds\n\ts_add_u32 m0, %[lds0], 119808\n\ts_nop 0\n\tbuffer_load_dwordx4 %[vH0], %[rH], s41 offen offset:0 lds\n\tbuffer_load_dwordx4 %[vH1], %[rH], s41 offen offset:1024 lds\n\tbuffer_load_dwordx4 %[vH2], %[rH], s41 offen offset:2048 lds\n\tbuffer_load_dwordx4 %[vH3], %[rH], s41 offen offset:3072 lds\n\ts_add_u32 m0, %[lds0], 123904\n\ts_nop 0\n\tbuffer_load_dwordx4 %[vH4], %[rH], s41 offen offset:0 lds\n\ts_add_u32 m0, %[lds0], 130048\n\ts_nop 0\n\tbuffer_load_dwordx4 %[vQ0], %[rQ], s42 offen offset:0 lds\n\ts_add_u32 s40, s40, 1024\n\ts_add_u32 s41, s41, 229376\n\ts_add_u32 s42, s42, 65536\n\ts_add_u32 m0, %[lds0], 136192\n\ts_nop 0\n\tbuffer_load_dwordx4 %[vA0], %[rA], s40 offen offset:0 lds\n\tbuffer_load_dwordx4 %[vA1], %[rA], s40 offen offset:1024 lds\n\tbuffer_load_dwordx4 %[vA2], %[rA], s40 offen offset:2048 lds\n\ts_add_u32 m0, %[lds0], 146432\n\ts_nop 0\n\tbuffer_load_dwordx4 %[vH0], %[rH], s41 offen offset:0 lds\n\tbuffer_load_dwordx4 %[vH1], %[rH], s41 offen offset:1024 lds\n\tbuffer_load_dwordx4 %[vH2], %[rH], s41 offen offset:2048 lds\n\tbuffer_load_dwordx4 %[vH3], %[rH], s41 offen offset:3072 lds\n\ts_add_u32 m0, %[lds0], 150528\n\ts_nop 0\n\tbuffer_load_dwordx4 %[vH4], %[rH], s41 offen offset:0 lds\n\ts_add_u32 m0, %[lds0], 156672\n\ts_nop 0\n\tbuffer_load_dwordx4 %[vQ0], %[rQ], s42 offen offset:0 lds\n\ts_add_u32 s40, s40, 1024\n\ts_add_u32 s41, s41, 229376\n\ts_add_u32 s42, s42, 65536\n\ts_waitcnt vmcnt(45)\n\ts_barrier\n\tds_read_b128 v[40:43], %[qa0]\n\ts_waitcnt lgkmcnt(0)\n\tv_dot2_f32_bf16 %[cs0], v40, %[c10], %[cs0]\n\tv_dot2_f32_bf16 %[cs1], v40, %[c01], %[cs1]\n\tv_dot2_f32_bf16 %[cs2], v41, %[c10], %[cs2]\n\tv_dot2_f32_bf16 %[cs3], v41, %[c01], %[cs3]\n\tv_dot2_f32_bf16 %[cs4], v42, %[c10], %[cs4]\n\tv_dot2_f32_bf16 %[cs5], v42, %[c01], %[cs5]\n\tv_dot2_f32_bf16 %[cs6], v43, %[c10], %[cs6]\n\tv_dot2_f32_bf16 %[cs7], v43, %[c01], %[cs7]\n\ts_waitcnt vmcnt(27)\n\ts_barrier\n\tds_read_b128 v[40:43], %[qa1]\n\tds_read_b128 v[44:47], %[qa2]\n\ts_add_u32 m0, %[lds0], 3072\n\ts_nop 0\n\tbuffer_load_dwordx4 %[vA0], %[rA], s40 offen offset:0 lds\n\tbuffer_load_dwordx4 %[vA1], %[rA], s40 offen offset:1024 lds\n\tbuffer_load_dwordx4 %[vA2], %[rA], s40 offen offset:2048 lds\n\ts_add_u32 m0, %[lds0], 13312\n\ts_nop 0\n\tbuffer_load_dwordx4 %[vH0], %[rH], s41 offen offset:0 lds\n\tbuffer_load_dwordx4 %[vH1], %[rH], s41 offen offset:1024 lds\n\tbuffer_load_dwordx4 %[vH2], %[rH], s41 offen offset:2048 lds\n\tbuffer_load_dwordx4 %[vH3], %[rH], s41 offen offset:3072 lds\n\ts_add_u32 m0, %[lds0], 17408\n\ts_nop 0\n\tbuffer_load_dwordx4 %[vH4], %[rH], s41 offen offset:0 lds\n\ts_add_u32 m0, %[lds0], 23552\n\ts_nop 0\n\tbuffer_load_dwordx4 %[vQ0], %[rQ], s42 offen offset:0 lds\n\ts_add_u32 s40, s40, 1024\n\ts_add_u32 s41, s41, 229376\n\ts_add_u32 s42, s42, 65536\n\ts_waitcnt lgkmcnt(0)\n\tv_dot2_f32_bf16 %[cs0], v40, %[c10], %[cs0]\n\tv_dot2_f32_bf16 %[cs1], v40, %[c01], %[cs1]\n\tv_dot2_f32_bf16 %[cs2], v41, %[c10], %[cs2]\n\tv_dot2_f32_bf16 %[cs3], v41, %[c01], %[cs3]\n\tv_dot2_f32_bf16 %[cs4], v42, %[c10], %[cs4]\n\tv_dot2_f32_bf16 %[cs5], v42, %[c01], %[cs5]\n\tv_dot2_f32_bf16 %[cs6], v43, %[c10], %[cs6]\n\tv_dot2_f32_bf16 %[cs7], v43, %[c01], %[cs7]\n\tv_dot2_f32_bf16 %[cs0], v44, %[c10], %[cs0]\n\tv_dot2_f32_bf16 %[cs1], v44, %[c01], %[cs1]\n\tv_dot2_f32_bf16 %[cs2], v45, %[c10], %[cs2]\n\tv_dot2_f32_bf16 %[cs3], v45, %[c01], %[cs3]\n\tv_dot2_f32_bf16 %[cs4], v46, %[c10], %[cs4]\n\tv_dot2_f32_bf16 %[cs5], v46, %[c01], %[cs5]\n\tv_dot2_f32_bf16 %[cs6], v47, %[c10], %[cs6]\n\tv_dot2_f32_bf16 %[cs7], v47, %[c01], %[cs7]\n\ts_mov_b32 s66, 4\n\tL11_l0_%=:\n\ts_waitcnt vmcnt(18)\n\ts_barrier\n\tds_read_b128 v[40:43], %[qa3]\n\tds_read_b128 v[44:47], %[qa4]\n\ts_add_u32 m0, %[lds0], 29696\n\ts_nop 0\n\tbuffer_load_dwordx4 %[vA0], %[rA], s40 offen offset:0 lds\n\tbuffer_load_dwordx4 %[vA1], %[rA], s40 offen offset:1024 lds\n\tbuffer_load_dwordx4 %[vA2], %[rA], s40 offen offset:2048 lds\n\ts_add_u32 m0, %[lds0], 39936\n\ts_nop 0\n\tbuffer_load_dwordx4 %[vH0], %[rH], s41 offen offset:0 lds\n\tbuffer_load_dwordx4 %[vH1], %[rH], s41 offen offset:1024 lds\n\tbuffer_load_dwordx4 %[vH2], %[rH], s41 offen offset:2048 lds\n\tbuffer_load_dwordx4 %[vH3], %[rH], s41 offen offset:3072 lds\n\ts_add_u32 m0, %[lds0], 44032\n\ts_nop 0\n\tbuffer_load_dwordx4 %[vH4], %[rH], s41 offen offset:0 lds\n\ts_add_u32 m0, %[lds0], 50176\n\ts_nop 0\n\tbuffer_load_dwordx4 %[vQ0], %[rQ], s42 offen offset:0 lds\n\ts_add_u32 s40, s40, 1024\n\ts_add_u32 s41, s41, 229376\n\ts_add_u32 s42, s42, 65536\n\ts_add_u32 m0, %[lds0], 56320\n\ts_nop 0\n\tbuffer_load_dwordx4 %[vA0], %[rA], s40 offen offset:0 lds\n\tbuffer_load_dwordx4 %[vA1], %[rA], s40 offen offset:1024 lds\n\tbuffer_load_dwordx4 %[vA2], %[rA], s40 offen offset:2048 lds\n\ts_add_u32 m0, %[lds0], 66560\n\ts_nop 0\n\tbuffer_load_dwordx4 %[vH0], %[rH], s41 offen offset:0 lds\n\tbuffer_load_dwordx4 %[vH1], %[rH], s41 offen offset:1024 lds\n\tbuffer_load_dwordx4 %[vH2], %[rH], s41 offen offset:2048 lds\n\tbuffer_load_dwordx4 %[vH3], %[rH], s41 offen offset:3072 lds\n\ts_add_u32 m0, %[lds0], 70656\n\ts_nop 0\n\tbuffer_load_dwordx4 %[vH4], %[rH], s41 offen offset:0 lds\n\ts_add_u32 m0, %[lds0], 76800\n\ts_nop 0\n\tbuffer_load_dwordx4 %[vQ0], %[rQ], s42 offen offset:0 lds\n\ts_add_u32 s40, s40, 1024\n\ts_add_u32 s41, s41, 229376\n\ts_add_u32 s42, s42, 65536\n\ts_waitcnt lgkmcnt(0)\n\tv_dot2_f32_bf16 %[cs0], v40, %[c10], %[cs0]\n\tv_dot2_f32_bf16 %[cs1], v40, %[c01], %[cs1]\n\tv_dot2_f32_bf16 %[cs2], v41, %[c10], %[cs2]\n\tv_dot2_f32_bf16 %[cs3], v41, %[c01], %[cs3]\n\tv_dot2_f32_bf16 %[cs4], v42, %[c10], %[cs4]\n\tv_dot2_f32_bf16 %[cs5], v42, %[c01], %[cs5]\n\tv_dot2_f32_bf16 %[cs6], v43, %[c10], %[cs6]\n\tv_dot2_f32_bf16 %[cs7], v43, %[c01], %[cs7]\n\tv_dot2_f32_bf16 %[cs0], v44, %[c10], %[cs0]\n\tv_dot2_f32_bf16 %[cs1], v44, %[c01], %[cs1]\n\tv_dot2_f32_bf16 %[cs2], v45, %[c10], %[cs2]\n\tv_dot2_f32_bf16 %[cs3], v45, %[c01], %[cs3]\n\tv_dot2_f32_bf16 %[cs4], v46, %[c10], %[cs4]\n\tv_dot2_f32_bf16 %[cs5], v46, %[c01], %[cs5]\n\tv_dot2_f32_bf16 %[cs6], v47, %[c10], %[cs6]\n\tv_dot2_f32_bf16 %[cs7], v47, %[c01], %[cs7]\n\ts_waitcnt vmcnt(18)\n\ts_barrier\n\tds_read_b128 v[40:43], %[qa5]\n\tds_read_b128 v[44:47], %[qa0]\n\ts_add_u32 m0, %[lds0], 82944\n\ts_nop 0\n\tbuffer_load_dwordx4 %[vA0], %[rA], s40 offen offset:0 lds\n\tbuffer_load_dwordx4 %[vA1], %[rA], s40 offen offset:1024 lds\n\tbuffer_load_dwordx4 %[vA2], %[rA], s40 offen offset:2048 lds\n\ts_add_u32 m0, %[lds0], 93184\n\ts_nop 0\n\tbuffer_load_dwordx4 %[vH0], %[rH], s41 offen offset:0 lds\n\tbuffer_load_dwordx4 %[vH1], %[rH], s41 offen offset:1024 lds\n\tbuffer_load_dwordx4 %[vH2], %[rH], s41 offen offset:2048 lds\n\tbuffer_load_dwordx4 %[vH3], %[rH], s41 offen offset:3072 lds\n\ts_add_u32 m0, %[lds0], 97280\n\ts_nop 0\n\tbuffer_load_dwordx4 %[vH4], %[rH], s41 offen offset:0 lds\n\ts_add_u32 m0, %[lds0], 103424\n\ts_nop 0\n\tbuffer_load_dwordx4 %[vQ0], %[rQ], s42 offen offset:0 lds\n\ts_add_u32 s40, s40, 1024\n\ts_add_u32 s41, s41, 229376\n\ts_add_u32 s42, s42, 65536\n\ts_add_u32 m0, %[lds0], 109568\n\ts_nop 0\n\tbuffer_load_dwordx4 %[vA0], %[rA], s40 offen offset:0 lds\n\tbuffer_load_dwordx4 %[vA1], %[rA], s40 offen offset:1024 lds\n\tbuffer_load_dwordx4 %[vA2], %[rA], s40 offen offset:2048 lds\n\ts_add_u32 m0, %[lds0], 119808\n\ts_nop 0\n\tbuffer_load_dwordx4 %[vH0], %[rH], s41 offen offset:0 lds\n\tbuffer_load_dwordx4 %[vH1], %[rH], s41 offen offset:1024 lds\n\tbuffer_load_dwordx4 %[vH2], %[rH], s41 offen offset:2048 lds\n\tbuffer_load_dwordx4 %[vH3], %[rH], s41 offen offset:3072 lds\n\ts_add_u32 m0, %[lds0], 123904\n\ts_nop 0\n\tbuffer_load_dwordx4 %[vH4], %[rH], s41 offen offset:0 lds\n\ts_add_u32 m0, %[lds0], 130048\n\ts_nop 0\n\tbuffer_load_dwordx4 %[vQ0], %[rQ], s42 offen offset:0 lds\n\ts_add_u32 s40, s40, 1024\n\ts_add_u32 s41, s41, 229376\n\ts_add_u32 s42, s42, 65536\n\ts_waitcnt lgkmcnt(0)\n\tv_dot2_f32_bf16 %[cs0], v40, %[c10], %[cs0]\n\tv_dot2_f32_bf16 %[cs1], v40, %[c01], %[cs1]\n\tv_dot2_f32_bf16 %[cs2], v41, %[c10], %[cs2]\n\tv_dot2_f32_bf16 %[cs3], v41, %[c01], %[cs3]\n\tv_dot2_f32_bf16 %[cs4], v42, %[c10], %[cs4]\n\tv_dot2_f32_bf16 %[cs5], v42, %[c01], %[cs5]\n\tv_dot2_f32_bf16 %[cs6], v43, %[c10], %[cs6]\n\tv_dot2_f32_bf16 %[cs7], v43, %[c01], %[cs7]\n\tv_dot2_f32_bf16 %[cs0], v44, %[c10], %[cs0]\n\tv_dot2_f32_bf16 %[cs1], v44, %[c01], %[cs1]\n\tv_dot2_f32_bf16 %[cs2], v45, %[c10], %[cs2]\n\tv_dot2_f32_bf16 %[cs3], v45, %[c01], %[cs3]\n\tv_dot2_f32_bf16 %[cs4], v46, %[c10], %[cs4]\n\tv_dot2_f32_bf16 %[cs5], v46, %[c01], %[cs5]\n\tv_dot2_f32_bf16 %[cs6], v47, %[c10], %[cs6]\n\tv_dot2_f32_bf16 %[cs7], v47, %[c01], %[cs7]\n\ts_waitcnt vmcnt(18)\n\ts_barrier\n\tds_read_b128 v[40:43], %[qa1]\n\tds_read_b128 v[44:47], %[qa2]\n\ts_add_u32 m0, %[lds0], 136192\n\ts_nop 0\n\tbuffer_load_dwordx4 %[vA0], %[rA], s40 offen offset:0 lds\n\tbuffer_load_dwordx4 %[vA1], %[rA], s40 offen offset:1024 lds\n\tbuffer_load_dwordx4 %[vA2], %[rA], s40 offen offset:2048 lds\n\ts_add_u32 m0, %[lds0], 146432\n\ts_nop 0\n\tbuffer_load_dwordx4 %[vH0], %[rH], s41 offen offset:0 lds\n\tbuffer_load_dwordx4 %[vH1], %[rH], s41 offen offset:1024 lds\n\tbuffer_load_dwordx4 %[vH2], %[rH], s41 offen offset:2048 lds\n\tbuffer_load_dwordx4 %[vH3], %[rH], s41 offen offset:3072 lds\n\ts_add_u32 m0, %[lds0], 150528\n\ts_nop 0\n\tbuffer_load_dwordx4 %[vH4], %[rH], s41 offen offset:0 lds\n\ts_add_u32 m0, %[lds0], 156672\n\ts_nop 0\n\tbuffer_load_dwordx4 %[vQ0], %[rQ], s42 offen offset:0 lds\n\ts_add_u32 s40, s40, 1024\n\ts_add_u32 s41, s41, 229376\n\ts_add_u32 s42, s42, 65536\n\ts_add_u32 m0, %[lds0], 3072\n\ts_nop 0\n\tbuffer_load_dwordx4 %[vA0], %[rA], s40 offen offset:0 lds\n\tbuffer_load_dwordx4 %[vA1], %[rA], s40 offen offset:1024 lds\n\tbuffer_load_dwordx4 %[vA2], %[rA], s40 offen offset:2048 lds\n\ts_add_u32 m0, %[lds0], 13312\n\ts_nop 0\n\tbuffer_load_dwordx4 %[vH0], %[rH], s41 offen offset:0 lds\n\tbuffer_load_dwordx4 %[vH1], %[rH], s41 offen offset:1024 lds\n\tbuffer_load_dwordx4 %[vH2], %[rH], s41 offen offset:2048 lds\n\tbuffer_load_dwordx4 %[vH3], %[rH], s41 offen offset:3072 lds\n\ts_add_u32 m0, %[lds0], 17408\n\ts_nop 0\n\tbuffer_load_dwordx4 %[vH4], %[rH], s41 offen offset:0 lds\n\ts_add_u32 m0, %[lds0], 23552\n\ts_nop 0\n\tbuffer_load_dwordx4 %[vQ0], %[rQ], s42 offen offset:0 lds\n\ts_add_u32 s40, s40, 1024\n\ts_add_u32 s41, s41, 229376\n\ts_add_u32 s42, s42, 65536\n\ts_waitcnt lgkmcnt(0)\n\tv_dot2_f32_bf16 %[cs0], v40, %[c10], %[cs0]\n\tv_dot2_f32_bf16 %[cs1], v40, %[c01], %[cs1]\n\tv_dot2_f32_bf16 %[cs2], v41, %[c10], %[cs2]\n\tv_dot2_f32_bf16 %[cs3], v41, %[c01], %[cs3]\n\tv_dot2_f32_bf16 %[cs4], v42, %[c10], %[cs4]\n\tv_dot2_f32_bf16 %[cs5], v42, %[c01], %[cs5]\n\tv_dot2_f32_bf16 %[cs6], v43, %[c10], %[cs6]\n\tv_dot2_f32_bf16 %[cs7], v43, %[c01], %[cs7]\n\tv_dot2_f32_bf16 %[cs0], v44, %[c10], %[cs0]\n\tv_dot2_f32_bf16 %[cs1], v44, %[c01], %[cs1]\n\tv_dot2_f32_bf16 %[cs2], v45, %[c10], %[cs2]\n\tv_dot2_f32_bf16 %[cs3], v45, %[c01], %[cs3]\n\tv_dot2_f32_bf16 %[cs4], v46, %[c10], %[cs4]\n\tv_dot2_f32_bf16 %[cs5], v46, %[c01], %[cs5]\n\tv_dot2_f32_bf16 %[cs6], v47, %[c10], %[cs6]\n\tv_dot2_f32_bf16 %[cs7], v47, %[c01], %[cs7]\n\ts_sub_u32 s66, s66, 1\n\ts_cmp_lg_u32 s66, 0\n\ts_cbranch_scc1 L11_l0_%=\n\ts_waitcnt vmcnt(18)\n\ts_barrier\n\tds_read_b128 v[40:43], %[qa3]\n\tds_read_b128 v[44:47], %[qa4]\n\ts_add_u32 m0, %[lds0], 29696\n\ts_nop 0\n\tbuffer_load_dwordx4 %[vA0], %[rA], s40 offen offset:0 lds\n\tbuffer_load_dwordx4 %[vA1], %[rA], s40 offen offset:1024 lds\n\tbuffer_load_dwordx4 %[vA2], %[rA], s40 offen offset:2048 lds\n\ts_add_u32 m0, %[lds0], 39936\n\ts_nop 0\n\tbuffer_load_dwordx4 %[vH0], %[rH], s41 offen offset:0 lds\n\tbuffer_load_dwordx4 %[vH1], %[rH], s41 offen offset:1024 lds\n\tbuffer_load_dwordx4 %[vH2], %[rH], s41 offen offset:2048 lds\n\tbuffer_load_dwordx4 %[vH3], %[rH], s41 offen offset:3072 lds\n\ts_add_u32 m0, %[lds0], 44032\n\ts_nop 0\n\tbuffer_load_dwordx4 %[vH4], %[rH], s41 offen offset:0 lds\n\ts_add_u32 m0, %[lds0], 50176\n\ts_nop 0\n\tbuffer_load_dwordx4 %[vQ0], %[rQ], s42 offen offset:0 lds\n\ts_add_u32 s40, s40, 1024\n\ts_add_u32 s41, s41, 229376\n\ts_add_u32 s42, s42, 65536\n\ts_waitcnt lgkmcnt(0)\n\tv_dot2_f32_bf16 %[cs0], v40, %[c10], %[cs0]\n\tv_dot2_f32_bf16 %[cs1], v40, %[c01], %[cs1]\n\tv_dot2_f32_bf16 %[cs2], v41, %[c10], %[cs2]\n\tv_dot2_f32_bf16 %[cs3], v41, %[c01], %[cs3]\n\tv_dot2_f32_bf16 %[cs4], v42, %[c10], %[cs4]\n\tv_dot2_f32_bf16 %[cs5], v42, %[c01], %[cs5]\n\tv_dot2_f32_bf16 %[cs6], v43, %[c10], %[cs6]\n\tv_dot2_f32_bf16 %[cs7], v43, %[c01], %[cs7]\n\tv_dot2_f32_bf16 %[cs0], v44, %[c10], %[cs0]\n\tv_dot2_f32_bf16 %[cs1], v44, %[c01], %[cs1]\n\tv_dot2_f32_bf16 %[cs2], v45, %[c10], %[cs2]\n\tv_dot2_f32_bf16 %[cs3], v45, %[c01], %[cs3]\n\tv_dot2_f32_bf16 %[cs4], v46, %[c10], %[cs4]\n\tv_dot2_f32_bf16 %[cs5], v46, %[c01], %[cs5]\n\tv_dot2_f32_bf16 %[cs6], v47, %[c10], %[cs6]\n\tv_dot2_f32_bf16 %[cs7], v47, %[c01], %[cs7]\n\ts_waitcnt vmcnt(9)\n\ts_barrier\n\tds_read_b128 v[40:43], %[qa5]\n\tds_read_b128 v[44:47], %[qa0]\n\ts_waitcnt lgkmcnt(0)\n\tv_dot2_f32_bf16 %[cs0], v40, %[c10], %[cs0]\n\tv_dot2_f32_bf16 %[cs1], v40, %[c01], %[cs1]\n\tv_dot2_f32_bf16 %[cs2], v41, %[c10], %[cs2]\n\tv_dot2_f32_bf16 %[cs3], v41, %[c01], %[cs3]\n\tv_dot2_f32_bf16 %[cs4], v42, %[c10], %[cs4]\n\tv_dot2_f32_bf16 %[cs5], v42, %[c01], %[cs5]\n\tv_dot2_f32_bf16 %[cs6], v43, %[c10], %[cs6]\n\tv_dot2_f32_bf16 %[cs7], v43, %[c01], %[cs7]\n\tv_dot2_f32_bf16 %[cs0], v44, %[c10], %[cs0]\n\tv_dot2_f32_bf16 %[cs1], v44, %[c01], %[cs1]\n\tv_dot2_f32_bf16 %[cs2], v45, %[c10], %[cs2]\n\tv_dot2_f32_bf16 %[cs3], v45, %[c01], %[cs3]\n\tv_dot2_f32_bf16 %[cs4], v46, %[c10], %[cs4]\n\tv_dot2_f32_bf16 %[cs5], v46, %[c01], %[cs5]\n\tv_dot2_f32_bf16 %[cs6], v47, %[c10], %[cs6]\n\tv_dot2_f32_bf16 %[cs7], v47, %[c01], %[cs7]\n\ts_waitcnt vmcnt(0)\n\ts_barrier\n\tds_read_b128 v[40:43], %[qa1]\n\ts_waitcnt lgkmcnt(0)\n\tv_dot2_f32_bf16 %[cs0], v40, %[c10], %[cs0]\n\tv_dot2_f32_bf16 %[cs1], v40, %[c01], %[cs1]\n\tv_dot2_f32_bf16 %[cs2], v41, %[c10], %[cs2]\n\tv_dot2_f32_bf16 %[cs3], v41, %[c01], %[cs3]\n\tv_dot2_f32_bf16 %[cs4], v42, %[c10], %[cs4]\n\tv_dot2_f32_bf16 %[cs5], v42, %[c01], %[cs5]\n\tv_dot2_f32_bf16 %[cs6], v43, %[c10], %[cs6]\n\tv_dot2_f32_bf16 %[cs7], v43, %[c01], %[cs7]\n\ts_nop 3"
#define L1_1_CLOB "s66","s40","s41","s42","v40","v41","v42","v43","v44","v45","v46","v47"
#define L1_2_ASM "s_mov_b32 s40, %[oA]\n\ts_mov_b32 s41, %[oH]\n\ts_mov_b32 s42, %[oQ]\n\ts_add_u32 m0, %[lds0], 6144\n\ts_nop 0\n\tbuffer_load_dwordx4 %[vA0], %[rA], s40 offen offset:0 lds\n\tbuffer_load_dwordx4 %[vA1], %[rA], s40 offen offset:1024 lds\n\ts_add_u32 m0, %[lds0], 18432\n\ts_nop 0\n\tbuffer_load_dwordx4 %[vH0], %[rH], s41 offen offset:0 lds\n\tbuffer_load_dwordx4 %[vH1], %[rH], s41 offen offset:1024 lds\n\tbuffer_load_dwordx4 %[vH2], %[rH], s41 offen offset:2048 lds\n\tbuffer_load_dwordx4 %[vH3], %[rH], s41 offen offset:3072 lds\n\ts_add_u32 m0, %[lds0], 24576\n\ts_nop 0\n\tbuffer_load_dwordx4 %[vQ0], %[rQ], s42 offen offset:0 lds\n\tbuffer_load_dwordx4 %[vQ1], %[rQ], s42 offen offset:1024 lds\n\ts_add_u32 s40, s40, 1024\n\ts_add_u32 s41, s41, 229376\n\ts_add_u32 s42, s42, 65536\n\ts_add_u32 m0, %[lds0], 32768\n\ts_nop 0\n\tbuffer_load_dwordx4 %[vA0], %[rA], s40 offen offset:0 lds\n\tbuffer_load_dwordx4 %[vA1], %[rA], s40 offen offset:1024 lds\n\ts_add_u32 m0, %[lds0], 45056\n\ts_nop 0\n\tbuffer_load_dwordx4 %[vH0], %[rH], s41 offen offset:0 lds\n\tbuffer_load_dwordx4 %[vH1], %[rH], s41 offen offset:1024 lds\n\tbuffer_load_dwordx4 %[vH2], %[rH], s41 offen offset:2048 lds\n\tbuffer_load_dwordx4 %[vH3], %[rH], s41 offen offset:3072 lds\n\ts_add_u32 m0, %[lds0], 51200\n\ts_nop 0\n\tbuffer_load_dwordx4 %[vQ0], %[rQ], s42 offen offset:0 lds\n\tbuffer_load_dwordx4 %[vQ1], %[rQ], s42 offen offset:1024 lds\n\ts_add_u32 s40, s40, 1024\n\ts_add_u32 s41, s41, 229376\n\ts_add_u32 s42, s42, 65536\n\ts_add_u32 m0, %[lds0], 59392\n\ts_nop 0\n\tbuffer_load_dwordx4 %[vA0], %[rA], s40 offen offset:0 lds\n\tbuffer_load_dwordx4 %[vA1], %[rA], s40 offen offset:1024 lds\n\ts_add_u32 m0, %[lds0], 71680\n\ts_nop 0\n\tbuffer_load_dwordx4 %[vH0], %[rH], s41 offen offset:0 lds\n\tbuffer_load_dwordx4 %[vH1], %[rH], s41 offen offset:1024 lds\n\tbuffer_load_dwordx4 %[vH2], %[rH], s41 offen offset:2048 lds\n\tbuffer_load_dwordx4 %[vH3], %[rH], s41 offen offset:3072 lds\n\ts_add_u32 m0, %[lds0], 77824\n\ts_nop 0\n\tbuffer_load_dwordx4 %[vQ0], %[rQ], s42 offen offset:0 lds\n\tbuffer_load_dwordx4 %[vQ1], %[rQ], s42 offen offset:1024 lds\n\ts_add_u32 s40, s40, 1024\n\ts_add_u32 s41, s41, 229376\n\ts_add_u32 s42, s42, 65536\n\ts_add_u32 m0, %[lds0], 86016\n\ts_nop 0\n\tbuffer_load_dwordx4 %[vA0], %[rA], s40 offen offset:0 lds\n\tbuffer_load_dwordx4 %[vA1], %[rA], s40 offen offset:1024 lds\n\ts_add_u32 m0, %[lds0], 98304\n\ts_nop 0\n\tbuffer_load_dwordx4 %[vH0], %[rH], s41 offen offset:0 lds\n\tbuffer_load_dwordx4 %[vH1], %[rH], s41 offen offset:1024 lds\n\tbuffer_load_dwordx4 %[vH2], %[rH], s41 offen offset:2048 lds\n\tbuffer_load_dwordx4 %[vH3], %[rH], s41 offen offset:3072 lds\n\ts_add_u32 m0, %[lds0], 104448\n\ts_nop 0\n\tbuffer_load_dwordx4 %[vQ0], %[rQ], s42 offen offset:0 lds\n\tbuffer_load_dwordx4 %[vQ1], %[rQ], s42 offen offset:1024 lds\n\ts_add_u32 s40, s40, 1024\n\ts_add_u32 s41, s41, 229376\n\ts_add_u32 s42, s42, 65536\n\ts_add_u32 m0, %[lds0], 112640\n\ts_nop 0\n\tbuffer_load_dwordx4 %[vA0], %[rA], s40 offen offset:0 lds\n\tbuffer_load_dwordx4 %[vA1], %[rA], s40 offen offset:1024 lds\n\ts_add_u32 m0, %[lds0], 124928\n\ts_nop 0\n\tbuffer_load_dwordx4 %[vH0], %[rH], s41 offen offset:0 lds\n\tbuffer_load_dwordx4 %[vH1], %[rH], s41 offen offset:1024 lds\n\tbuffer_load_dwordx4 %[vH2], %[rH], s41 offen offset:2048 lds\n\tbuffer_load_dwordx4 %[vH3], %[rH], s41 offen offset:3072 lds\n\ts_add_u32 m0, %[lds0], 131072\n\ts_nop 0\n\tbuffer_load_dwordx4 %[vQ0], %[rQ], s42 offen offset:0 lds\n\tbuffer_load_dwordx4 %[vQ1], %[rQ], s42 offen offset:1024 lds\n\ts_add_u32 s40, s40, 1024\n\ts_add_u32 s41, s41, 229376\n\ts_add_u32 s42, s42, 65536\n\ts_add_u32 m0, %[lds0], 139264\n\ts_nop 0\n\tbuffer_load_dwordx4 %[vA0], %[rA], s40 offen offset:0 lds\n\tbuffer_load_dwordx4 %[vA1], %[rA], s40 offen offset:1024 lds\n\ts_add_u32 m0, %[lds0], 151552\n\ts_nop 0\n\tbuffer_load_dwordx4 %[vH0], %[rH], s41 offen offset:0 lds\n\tbuffer_load_dwordx4 %[vH1], %[rH], s41 offen offset:1024 lds\n\tbuffer_load_dwordx4 %[vH2], %[rH], s41 offen offset:2048 lds\n\tbuffer_load_dwordx4 %[vH3], %[rH], s41 offen offset:3072 lds\n\ts_add_u32 m0, %[lds0], 157696\n\ts_nop 0\n\tbuffer_load_dwordx4 %[vQ0], %[rQ], s42 offen offset:0 lds\n\tbuffer_load_dwordx4 %[vQ1], %[rQ], s42 offen offset:1024 lds\n\ts_add_u32 s40, s40, 1024\n\ts_add_u32 s41, s41, 229376\n\ts_add_u32 s42, s42, 65536\n\ts_waitcnt vmcnt(40)\n\ts_barrier\n\tds_read_b128 v[40:43], %[qa0]\n\ts_waitcnt lgkmcnt(0)\n\tv_dot2_f32_bf16 %[cs0], v40, %[c10], %[cs0]\n\tv_dot2_f32_bf16 %[cs1], v40, %[c01], %[cs1]\n\tv_dot2_f32_bf16 %[cs2], v41, %[c10], %[cs2]\n\tv_dot2_f32_bf16 %[cs3], v41, %[c01], %[cs3]\n\tv_dot2_f32_bf16 %[cs4], v42, %[c10], %[cs4]\n\tv_dot2_f32_bf16 %[cs5], v42, %[c01], %[cs5]\n\tv_dot2_f32_bf16 %[cs6], v43, %[c10], %[cs6]\n\tv_dot2_f32_bf16 %[cs7], v43, %[c01], %[cs7]\n\ts_waitcnt vmcnt(24)\n\ts_barrier\n\tds_read_b128 v[40:43], %[qa1]\n\tds_read_b128 v[44:47], %[qa2]\n\ts_add_u32 m0, %[lds0], 6144\n\ts_nop 0\n\tbuffer_load_dwordx4 %[vA0], %[rA], s40 offen offset:0 lds\n\tbuffer_load_dwordx4 %[vA1], %[rA], s40 offen offset:1024 lds\n\ts_add_u32 m0, %[lds0], 18432\n\ts_nop 0\n\tbuffer_load_dwordx4 %[vH0], %[rH], s41 offen offset:0 lds\n\tbuffer_load_dwordx4 %[vH1], %[rH], s41 offen offset:1024 lds\n\tbuffer_load_dwordx4 %[vH2], %[rH], s41 offen offset:2048 lds\n\tbuffer_load_dwordx4 %[vH3], %[rH], s41 offen offset:3072 lds\n\ts_add_u32 m0, %[lds0], 24576\n\ts_nop 0\n\tbuffer_load_dwordx4 %[vQ0], %[rQ], s42 offen offset:0 lds\n\tbuffer_load_dwordx4 %[vQ1], %[rQ], s42 offen offset:1024 lds\n\ts_add_u32 s40, s40, 1024\n\ts_add_u32 s41, s41, 229376\n\ts_add_u32 s42, s42, 65536\n\ts_waitcnt lgkmcnt(0)\n\tv_dot2_f32_bf16 %[cs0], v40, %[c10], %[cs0]\n\tv_dot2_f32_bf16 %[cs1], v40, %[c01], %[cs1]\n\tv_dot2_f32_bf16 %[cs2], v41, %[c10], %[cs2]\n\tv_dot2_f32_bf16 %[cs3], v41, %[c01], %[cs3]\n\tv_dot2_f32_bf16 %[cs4], v42, %[c10], %[cs4]\n\tv_dot2_f32_bf16 %[cs5], v42, %[c01], %[cs5]\n\tv_dot2_f32_bf16 %[cs6], v43, %[c10], %[cs6]\n\tv_dot2_f32_bf16 %[cs7], v43, %[c01], %[cs7]\n\tv_dot2_f32_bf16 %[cs0], v44, %[c10], %[cs0]\n\tv_dot2_f32_bf16 %[cs1], v44, %[c01], %[cs1]\n\tv_dot2_f32_bf16 %[cs2], v45, %[c10], %[cs2]\n\tv_dot2_f32_bf16 %[cs3], v45, %[c01], %[cs3]\n\tv_dot2_f32_bf16 %[cs4], v46, %[c10], %[cs4]\n\tv_dot2_f32_bf16 %[cs5], v46, %[c01], %[cs5]\n\tv_dot2_f32_bf16 %[cs6], v47, %[c10], %[cs6]\n\tv_dot2_f32_bf16 %[cs7], v47, %[c01], %[cs7]\n\ts_mov_b32 s66, 4\n\tL12_l0_%=:\n\ts_waitcnt vmcnt(16)\n\ts_barrier\n\tds_read_b128 v[40:43], %[qa3]\n\tds_read_b128 v[44:47], %[qa4]\n\ts_add_u32 m0, %[lds0], 32768\n\ts_nop 0\n\tbuffer_load_dwordx4 %[vA0], %[rA], s40 offen offset:0 lds\n\tbuffer_load_dwordx4 %[vA1], %[rA], s40 offen offset:1024 lds\n\ts_add_u32 m0, %[lds0], 45056\n\ts_nop 0\n\tbuffer_load_dwordx4 %[vH0], %[rH], s41 offen offset:0 lds\n\tbuffer_load_dwordx4 %[vH1], %[rH], s41 offen offset:1024 lds\n\tbuffer_load_dwordx4 %[vH2], %[rH], s41 offen offset:2048 lds\n\tbuffer_load_dwordx4 %[vH3], %[rH], s41 offen offset:3072 lds\n\ts_add_u32 m0, %[lds0], 51200\n\ts_nop 0\n\tbuffer_load_dwordx4 %[vQ0], %[rQ], s42 offen offset:0 lds\n\tbuffer_load_dwordx4 %[vQ1], %[rQ], s42 offen offset:1024 lds\n\ts_add_u32 s40, s40, 1024\n\ts_add_u32 s41, s41, 229376\n\ts_add_u32 s42, s42, 65536\n\ts_add_u32 m0, %[lds0], 59392\n\ts_nop 0\n\tbuffer_load_dwordx4 %[vA0], %[rA], s40 offen offset:0 lds\n\tbuffer_load_dwordx4 %[vA1], %[rA], s40 offen offset:1024 lds\n\ts_add_u32 m0, %[lds0], 71680\n\ts_nop 0\n\tbuffer_load_dwordx4 %[vH0], %[rH], s41 offen offset:0 lds\n\tbuffer_load_dwordx4 %[vH1], %[rH], s41 offen offset:1024 lds\n\tbuffer_load_dwordx4 %[vH2], %[rH], s41 offen offset:2048 lds\n\tbuffer_load_dwordx4 %[vH3], %[rH], s41 offen offset:3072 lds\n\ts_add_u32 m0, %[lds0], 77824\n\ts_nop 0\n\tbuffer_load_dwordx4 %[vQ0], %[rQ], s42 offen offset:0 lds\n\tbuffer_load_dwordx4 %[vQ1], %[rQ], s42 offen offset:1024 lds\n\ts_add_u32 s40, s40, 1024\n\ts_add_u32 s41, s41, 229376\n\ts_add_u32 s42, s42, 65536\n\ts_waitcnt lgkmcnt(0)\n\tv_dot2_f32_bf16 %[cs0], v40, %[c10], %[cs0]\n\tv_dot2_f32_bf16 %[cs1], v40, %[c01], %[cs1]\n\tv_dot2_f32_bf16 %[cs2], v41, %[c10], %[cs2]\n\tv_dot2_f32_bf16 %[cs3], v41, %[c01], %[cs3]\n\tv_dot2_f32_bf16 %[cs4], v42, %[c10], %[cs4]\n\tv_dot2_f32_bf16 %[cs5], v42, %[c01], %[cs5]\n\tv_dot2_f32_bf16 %[cs6], v43, %[c10], %[cs6]\n\tv_dot2_f32_bf16 %[cs7], v43, %[c01], %[cs7]\n\tv_dot2_f32_bf16 %[cs0], v44, %[c10], %[cs0]\n\tv_dot2_f32_bf16 %[cs1], v44, %[c01], %[cs1]\n\tv_dot2_f32_bf16 %[cs2], v45, %[c10], %[cs2]\n\tv_dot2_f32_bf16 %[cs3], v45, %[c01], %[cs3]\n\tv_dot2_f32_bf16 %[cs4], v46, %[c10], %[cs4]\n\tv_dot2_f32_bf16 %[cs5], v46, %[c01], %[cs5]\n\tv_dot2_f32_bf16 %[cs6], v47, %[c10], %[cs6]\n\tv_dot2_f32_bf16 %[cs7], v47, %[c01], %[cs7]\n\ts_waitcnt vmcnt(16)\n\ts_barrier\n\tds_read_b128 v[40:43], %[qa5]\n\tds_read_b128 v[44:47], %[qa0]\n\ts_add_u32 m0, %[lds0], 86016\n\ts_nop 0\n\tbuffer_load_dwordx4 %[vA0], %[rA], s40 offen offset:0 lds\n\tbuffer_load_dwordx4 %[vA1], %[rA], s40 offen offset:1024 lds\n\ts_add_u32 m0, %[lds0], 98304\n\ts_nop 0\n\tbuffer_load_dwordx4 %[vH0], %[rH], s41 offen offset:0 lds\n\tbuffer_load_dwordx4 %[vH1], %[rH], s41 offen offset:1024 lds\n\tbuffer_load_dwordx4 %[vH2], %[rH], s41 offen offset:2048 lds\n\tbuffer_load_dwordx4 %[vH3], %[rH], s41 offen offset:3072 lds\n\ts_add_u32 m0, %[lds0], 104448\n\ts_nop 0\n\tbuffer_load_dwordx4 %[vQ0], %[rQ], s42 offen offset:0 lds\n\tbuffer_load_dwordx4 %[vQ1], %[rQ], s42 offen offset:1024 lds\n\ts_add_u32 s40, s40, 1024\n\ts_add_u32 s41, s41, 229376\n\ts_add_u32 s42, s42, 65536\n\ts_add_u32 m0, %[lds0], 112640\n\ts_nop 0\n\tbuffer_load_dwordx4 %[vA0], %[rA], s40 offen offset:0 lds\n\tbuffer_load_dwordx4 %[vA1], %[rA], s40 offen offset:1024 lds\n\ts_add_u32 m0, %[lds0], 124928\n\ts_nop 0\n\tbuffer_load_dwordx4 %[vH0], %[rH], s41 offen offset:0 lds\n\tbuffer_load_dwordx4 %[vH1], %[rH], s41 offen offset:1024 lds\n\tbuffer_load_dwordx4 %[vH2], %[rH], s41 offen offset:2048 lds\n\tbuffer_load_dwordx4 %[vH3], %[rH], s41 offen offset:3072 lds\n\ts_add_u32 m0, %[lds0], 131072\n\ts_nop 0\n\tbuffer_load_dwordx4 %[vQ0], %[rQ], s42 offen offset:0 lds\n\tbuffer_load_dwordx4 %[vQ1], %[rQ], s42 offen offset:1024 lds\n\ts_add_u32 s40, s40, 1024\n\ts_add_u32 s41, s41, 229376\n\ts_add_u32 s42, s42, 65536\n\ts_waitcnt lgkmcnt(0)\n\tv_dot2_f32_bf16 %[cs0], v40, %[c10], %[cs0]\n\tv_dot2_f32_bf16 %[cs1], v40, %[c01], %[cs1]\n\tv_dot2_f32_bf16 %[cs2], v41, %[c10], %[cs2]\n\tv_dot2_f32_bf16 %[cs3], v41, %[c01], %[cs3]\n\tv_dot2_f32_bf16 %[cs4], v42, %[c10], %[cs4]\n\tv_dot2_f32_bf16 %[cs5], v42, %[c01], %[cs5]\n\tv_dot2_f32_bf16 %[cs6], v43, %[c10], %[cs6]\n\tv_dot2_f32_bf16 %[cs7], v43, %[c01], %[cs7]\n\tv_dot2_f32_bf16 %[cs0], v44, %[c10], %[cs0]\n\tv_dot2_f32_bf16 %[cs1], v44, %[c01], %[cs1]\n\tv_dot2_f32_bf16 %[cs2], v45, %[c10], %[cs2]\n\tv_dot2_f32_bf16 %[cs3], v45, %[c01], %[cs3]\n\tv_dot2_f32_bf16 %[cs4], v46, %[c10], %[cs4]\n\tv_dot2_f32_bf16 %[cs5], v46, %[c01], %[cs5]\n\tv_dot2_f32_bf16 %[cs6], v47, %[c10], %[cs6]\n\tv_dot2_f32_bf16 %[cs7], v47, %[c01], %[cs7]\n\ts_waitcnt vmcnt(16)\n\ts_barrier\n\tds_read_b128 v[40:43], %[qa1]\n\tds_read_b128 v[44:47], %[qa2]\n\ts_add_u32 m0, %[lds0], 139264\n\ts_nop 0\n\tbuffer_load_dwordx4 %[vA0], %[rA], s40 offen offset:0 lds\n\tbuffer_load_dwordx4 %[vA1], %[rA], s40 offen offset:1024 lds\n\ts_add_u32 m0, %[lds0], 151552\n\ts_nop 0\n\tbuffer_load_dwordx4 %[vH0], %[rH], s41 offen offset:0 lds\n\tbuffer_load_dwordx4 %[vH1], %[rH], s41 offen offset:1024 lds\n\tbuffer_load_dwordx4 %[vH2], %[rH], s41 offen offset:2048 lds\n\tbuffer_load_dwordx4 %[vH3], %[rH], s41 offen offset:3072 lds\n\ts_add_u32 m0, %[lds0], 157696\n\ts_nop 0\n\tbuffer_load_dwordx4 %[vQ0], %[rQ], s42 offen offset:0 lds\n\tbuffer_load_dwordx4 %[vQ1], %[rQ], s42 offen offset:1024 lds\n\ts_add_u32 s40, s40, 1024\n\ts_add_u32 s41, s41, 229376\n\ts_add_u32 s42, s42, 65536\n\ts_add_u32 m0, %[lds0], 6144\n\ts_nop 0\n\tbuffer_load_dwordx4 %[vA0], %[rA], s40 offen offset:0 lds\n\tbuffer_load_dwordx4 %[vA1], %[rA], s40 offen offset:1024 lds\n\ts_add_u32 m0, %[lds0], 18432\n\ts_nop 0\n\tbuffer_load_dwordx4 %[vH0], %[rH], s41 offen offset:0 lds\n\tbuffer_load_dwordx4 %[vH1], %[rH], s41 offen offset:1024 lds\n\tbuffer_load_dwordx4 %[vH2], %[rH], s41 offen offset:2048 lds\n\tbuffer_load_dwordx4 %[vH3], %[rH], s41 offen offset:3072 lds\n\ts_add_u32 m0, %[lds0], 24576\n\ts_nop 0\n\tbuffer_load_dwordx4 %[vQ0], %[rQ], s42 offen offset:0 lds\n\tbuffer_load_dwordx4 %[vQ1], %[rQ], s42 offen offset:1024 lds\n\ts_add_u32 s40, s40, 1024\n\ts_add_u32 s41, s41, 229376\n\ts_add_u32 s42, s42, 65536\n\ts_waitcnt lgkmcnt(0)\n\tv_dot2_f32_bf16 %[cs0], v40, %[c10], %[cs0]\n\tv_dot2_f32_bf16 %[cs1], v40, %[c01], %[cs1]\n\tv_dot2_f32_bf16 %[cs2], v41, %[c10], %[cs2]\n\tv_dot2_f32_bf16 %[cs3], v41, %[c01], %[cs3]\n\tv_dot2_f32_bf16 %[cs4], v42, %[c10], %[cs4]\n\tv_dot2_f32_bf16 %[cs5], v42, %[c01], %[cs5]\n\tv_dot2_f32_bf16 %[cs6], v43, %[c10], %[cs6]\n\tv_dot2_f32_bf16 %[cs7], v43, %[c01], %[cs7]\n\tv_dot2_f32_bf16 %[cs0], v44, %[c10], %[cs0]\n\tv_dot2_f32_bf16 %[cs1], v44, %[c01], %[cs1]\n\tv_dot2_f32_bf16 %[cs2], v45, %[c10], %[cs2]\n\tv_dot2_f32_bf16 %[cs3], v45, %[c01], %[cs3]\n\tv_dot2_f32_bf16 %[cs4], v46, %[c10], %[cs4]\n\tv_dot2_f32_bf16 %[cs5], v46, %[c01], %[cs5]\n\tv_dot2_f32_bf16 %[cs6], v47, %[c10], %[cs6]\n\tv_dot2_f32_bf16 %[cs7], v47, %[c01], %[cs7]\n\ts_sub_u32 s66, s66, 1\n\ts_cmp_lg_u32 s66, 0\n\ts_cbranch_scc1 L12_l0_%=\n\ts_waitcnt vmcnt(16)\n\ts_barrier\n\tds_read_b128 v[40:43], %[qa3]\n\tds_read_b128 v[44:47], %[qa4]\n\ts_add_u32 m0, %[lds0], 32768\n\ts_nop 0\n\tbuffer_load_dwordx4 %[vA0], %[rA], s40 offen offset:0 lds\n\tbuffer_load_dwordx4 %[vA1], %[rA], s40 offen offset:1024 lds\n\ts_add_u32 m0, %[lds0], 45056\n\ts_nop 0\n\tbuffer_load_dwordx4 %[vH0], %[rH], s41 offen offset:0 lds\n\tbuffer_load_dwordx4 %[vH1], %[rH], s41 offen offset:1024 lds\n\tbuffer_load_dwordx4 %[vH2], %[rH], s41 offen offset:2048 lds\n\tbuffer_load_dwordx4 %[vH3], %[rH], s41 offen offset:3072 lds\n\ts_add_u32 m0, %[lds0], 51200\n\ts_nop 0\n\tbuffer_load_dwordx4 %[vQ0], %[rQ], s42 offen offset:0 lds\n\tbuffer_load_dwordx4 %[vQ1], %[rQ], s42 offen offset:1024 lds\n\ts_add_u32 s40, s40, 1024\n\ts_add_u32 s41, s41, 229376\n\ts_add_u32 s42, s42, 65536\n\ts_waitcnt lgkmcnt(0)\n\tv_dot2_f32_bf16 %[cs0], v40, %[c10], %[cs0]\n\tv_dot2_f32_bf16 %[cs1], v40, %[c01], %[cs1]\n\tv_dot2_f32_bf16 %[cs2], v41, %[c10], %[cs2]\n\tv_dot2_f32_bf16 %[cs3], v41, %[c01], %[cs3]\n\tv_dot2_f32_bf16 %[cs4], v42, %[c10], %[cs4]\n\tv_dot2_f32_bf16 %[cs5], v42, %[c01], %[cs5]\n\tv_dot2_f32_bf16 %[cs6], v43, %[c10], %[cs6]\n\tv_dot2_f32_bf16 %[cs7], v43, %[c01], %[cs7]\n\tv_dot2_f32_bf16 %[cs0], v44, %[c10], %[cs0]\n\tv_dot2_f32_bf16 %[cs1], v44, %[c01], %[cs1]\n\tv_dot2_f32_bf16 %[cs2], v45, %[c10], %[cs2]\n\tv_dot2_f32_bf16 %[cs3], v45, %[c01], %[cs3]\n\tv_dot2_f32_bf16 %[cs4], v46, %[c10], %[cs4]\n\tv_dot2_f32_bf16 %[cs5], v46, %[c01], %[cs5]\n\tv_dot2_f32_bf16 %[cs6], v47, %[c10], %[cs6]\n\tv_dot2_f32_bf16 %[cs7], v47, %[c01], %[cs7]\n\ts_waitcnt vmcnt(8)\n\ts_barrier\n\tds_read_b128 v[40:43], %[qa5]\n\tds_read_b128 v[44:47], %[qa0]\n\ts_waitcnt lgkmcnt(0)\n\tv_dot2_f32_bf16 %[cs0], v40, %[c10], %[cs0]\n\tv_dot2_f32_bf16 %[cs1], v40, %[c01], %[cs1]\n\tv_dot2_f32_bf16 %[cs2], v41, %[c10], %[cs2]\n\tv_dot2_f32_bf16 %[cs3], v41, %[c01], %[cs3]\n\tv_dot2_f32_bf16 %[cs4], v42, %[c10], %[cs4]\n\tv_dot2_f32_bf16 %[cs5], v42, %[c01], %[cs5]\n\tv_dot2_f32_bf16 %[cs6], v43, %[c10], %[cs6]\n\tv_dot2_f32_bf16 %[cs7], v43, %[c01], %[cs7]\n\tv_dot2_f32_bf16 %[cs0], v44, %[c10], %[cs0]\n\tv_dot2_f32_bf16 %[cs1], v44, %[c01], %[cs1]\n\tv_dot2_f32_bf16 %[cs2], v45, %[c10], %[cs2]\n\tv_dot2_f32_bf16 %[cs3], v45, %[c01], %[cs3]\n\tv_dot2_f32_bf16 %[cs4], v46, %[c10], %[cs4]\n\tv_dot2_f32_bf16 %[cs5], v46, %[c01], %[cs5]\n\tv_dot2_f32_bf16 %[cs6], v47, %[c10], %[cs6]\n\tv_dot2_f32_bf16 %[cs7], v47, %[c01], %[cs7]\n\ts_waitcnt vmcnt(0)\n\ts_barrier\n\tds_read_b128 v[40:43], %[qa1]\n\ts_waitcnt lgkmcnt(0)\n\tv_dot2_f32_bf16 %[cs0], v40, %[c10], %[cs0]\n\tv_dot2_f32_bf16 %[cs1], v40, %[c01], %[cs1]\n\tv_dot2_f32_bf16 %[cs2], v41, %[c10], %[cs2]\n\tv_dot2_f32_bf16 %[cs3], v41, %[c01], %[cs3]\n\tv_dot2_f32_bf16 %[cs4], v42, %[c10], %[cs4]\n\tv_dot2_f32_bf16 %[cs5], v42, %[c01], %[cs5]\n\tv_dot2_f32_bf16 %[cs6], v43, %[c10], %[cs6]\n\tv_dot2_f32_bf16 %[cs7], v43, %[c01], %[cs7]\n\ts_nop 3"
#define L1_2_CLOB "s66","s40","s41","s42","v40","v41","v42","v43","v44","v45","v46","v47"
#define L1_3_ASM "s_barrier\n\tds_read_b128 v[40:43], %[qa0]\n\ts_waitcnt lgkmcnt(0)\n\tv_dot2_f32_bf16 %[cs0], v40, %[c10], %[cs0]\n\tv_dot2_f32_bf16 %[cs1], v40, %[c01], %[cs1]\n\tv_dot2_f32_bf16 %[cs2], v41, %[c10], %[cs2]\n\tv_dot2_f32_bf16 %[cs3], v41, %[c01], %[cs3]\n\tv_dot2_f32_bf16 %[cs4], v42, %[c10], %[cs4]\n\tv_dot2_f32_bf16 %[cs5], v42, %[c01], %[cs5]\n\tv_dot2_f32_bf16 %[cs6], v43, %[c10], %[cs6]\n\tv_dot2_f32_bf16 %[cs7], v43, %[c01], %[cs7]\n\ts_barrier\n\tglobal_load_dword v48, %[pf0], off\n\tds_read_b128 v[40:43], %[qa1]\n\tds_read_b128 v[44:47], %[qa2]\n\ts_waitcnt lgkmcnt(0)\n\tv_dot2_f32_bf16 %[cs0], v40, %[c10], %[cs0]\n\tv_dot2_f32_bf16 %[cs1], v40, %[c01], %[cs1]\n\tv_dot2_f32_bf16 %[cs2], v41, %[c10], %[cs2]\n\tv_dot2_f32_bf16 %[cs3], v41, %[c01], %[cs3]\n\tv_dot2_f32_bf16 %[cs4], v42, %[c10], %[cs4]\n\tv_dot2_f32_bf16 %[cs5], v42, %[c01], %[cs5]\n\tv_dot2_f32_bf16 %[cs6], v43, %[c10], %[cs6]\n\tv_dot2_f32_bf16 %[cs7], v43, %[c01], %[cs7]\n\tv_dot2_f32_bf16 %[cs0], v44, %[c10], %[cs0]\n\tv_dot2_f32_bf16 %[cs1], v44, %[c01], %[cs1]\n\tv_dot2_f32_bf16 %[cs2], v45, %[c10], %[cs2]\n\tv_dot2_f32_bf16 %[cs3], v45, %[c01], %[cs3]\n\tv_dot2_f32_bf16 %[cs4], v46, %[c10], %[cs4]\n\tv_dot2_f32_bf16 %[cs5], v46, %[c01], %[cs5]\n\tv_dot2_f32_bf16 %[cs6], v47, %[c10], %[cs6]\n\tv_dot2_f32_bf16 %[cs7], v47, %[c01], %[cs7]\n\ts_barrier\n\tds_read_b128 v[40:43], %[qa3]\n\tds_read_b128 v[44:47], %[qa4]\n\ts_waitcnt lgkmcnt(0)\n\tv_dot2_f32_bf16 %[cs0], v40, %[c10], %[cs0]\n\tv_dot2_f32_bf16 %[cs1], v40, %[c01], %[cs1]\n\tv_dot2_f32_bf16 %[cs2], v41, %[c10], %[cs2]\n\tv_dot2_f32_bf16 %[cs3], v41, %[c01], %[cs3]\n\tv_dot2_f32_bf16 %[cs4], v42, %[c10], %[cs4]\n\tv_dot2_f32_bf16 %[cs5], v42, %[c01], %[cs5]\n\tv_dot2_f32_bf16 %[cs6], v43, %[c10], %[cs6]\n\tv_dot2_f32_bf16 %[cs7], v43, %[c01], %[cs7]\n\tv_dot2_f32_bf16 %[cs0], v44, %[c10], %[cs0]\n\tv_dot2_f32_bf16 %[cs1], v44, %[c01], %[cs1]\n\tv_dot2_f32_bf16 %[cs2], v45, %[c10], %[cs2]\n\tv_dot2_f32_bf16 %[cs3], v45, %[c01], %[cs3]\n\tv_dot2_f32_bf16 %[cs4], v46, %[c10], %[cs4]\n\tv_dot2_f32_bf16 %[cs5], v46, %[c01], %[cs5]\n\tv_dot2_f32_bf16 %[cs6], v47, %[c10], %[cs6]\n\tv_dot2_f32_bf16 %[cs7], v47, %[c01], %[cs7]\n\ts_barrier\n\tglobal_load_dword v49, %[pf1], off\n\tds_read_b128 v[40:43], %[qa5]\n\tds_read_b128 v[44:47], %[qa0]\n\ts_waitcnt lgkmcnt(0)\n\tv_dot2_f32_bf16 %[cs0], v40, %[c10], %[cs0]\n\tv_dot2_f32_bf16 %[cs1], v40, %[c01], %[cs1]\n\tv_dot2_f32_bf16 %[cs2], v41, %[c10], %[cs2]\n\tv_dot2_f32_bf16 %[cs3], v41, %[c01], %[cs3]\n\tv_dot2_f32_bf16 %[cs4], v42, %[c10], %[cs4]\n\tv_dot2_f32_bf16 %[cs5], v42, %[c01], %[cs5]\n\tv_dot2_f32_bf16 %[cs6], v43, %[c10], %[cs6]\n\tv_dot2_f32_bf16 %[cs7], v43, %[c01], %[cs7]\n\tv_dot2_f32_bf16 %[cs0], v44, %[c10], %[cs0]\n\tv_dot2_f32_bf16 %[cs1], v44, %[c01], %[cs1]\n\tv_dot2_f32_bf16 %[cs2], v45, %[c10], %[cs2]\n\tv_dot2_f32_bf16 %[cs3], v45, %[c01], %[cs3]\n\tv_dot2_f32_bf16 %[cs4], v46, %[c10], %[cs4]\n\tv_dot2_f32_bf16 %[cs5], v46, %[c01], %[cs5]\n\tv_dot2_f32_bf16 %[cs6], v47, %[c10], %[cs6]\n\tv_dot2_f32_bf16 %[cs7], v47, %[c01], %[cs7]\n\ts_barrier\n\tds_read_b128 v[40:43], %[qa1]\n\tds_read_b128 v[44:47], %[qa2]\n\ts_waitcnt lgkmcnt(0)\n\tv_dot2_f32_bf16 %[cs0], v40, %[c10], %[cs0]\n\tv_dot2_f32_bf16 %[cs1], v40, %[c01], %[cs1]\n\tv_dot2_f32_bf16 %[cs2], v41, %[c10], %[cs2]\n\tv_dot2_f32_bf16 %[cs3], v41, %[c01], %[cs3]\n\tv_dot2_f32_bf16 %[cs4], v42, %[c10], %[cs4]\n\tv_dot2_f32_bf16 %[cs5], v42, %[c01], %[cs5]\n\tv_dot2_f32_bf16 %[cs6], v43, %[c10], %[cs6]\n\tv_dot2_f32_bf16 %[cs7], v43, %[c01], %[cs7]\n\tv_dot2_f32_bf16 %[cs0], v44, %[c10], %[cs0]\n\tv_dot2_f32_bf16 %[cs1], v44, %[c01], %[cs1]\n\tv_dot2_f32_bf16 %[cs2], v45, %[c10], %[cs2]\n\tv_dot2_f32_bf16 %[cs3], v45, %[c01], %[cs3]\n\tv_dot2_f32_bf16 %[cs4], v46, %[c10], %[cs4]\n\tv_dot2_f32_bf16 %[cs5], v46, %[c01], %[cs5]\n\tv_dot2_f32_bf16 %[cs6], v47, %[c10], %[cs6]\n\tv_dot2_f32_bf16 %[cs7], v47, %[c01], %[cs7]\n\ts_barrier\n\tds_read_b128 v[40:43], %[qa3]\n\tds_read_b128 v[44:47], %[qa4]\n\ts_waitcnt lgkmcnt(0)\n\tv_dot2_f32_bf16 %[cs0], v40, %[c10], %[cs0]\n\tv_dot2_f32_bf16 %[cs1], v40, %[c01], %[cs1]\n\tv_dot2_f32_bf16 %[cs2], v41, %[c10], %[cs2]\n\tv_dot2_f32_bf16 %[cs3], v41, %[c01], %[cs3]\n\tv_dot2_f32_bf16 %[cs4], v42, %[c10], %[cs4]\n\tv_dot2_f32_bf16 %[cs5], v42, %[c01], %[cs5]\n\tv_dot2_f32_bf16 %[cs6], v43, %[c10], %[cs6]\n\tv_dot2_f32_bf16 %[cs7], v43, %[c01], %[cs7]\n\tv_dot2_f32_bf16 %[cs0], v44, %[c10], %[cs0]\n\tv_dot2_f32_bf16 %[cs1], v44, %[c01], %[cs1]\n\tv_dot2_f32_bf16 %[cs2], v45, %[c10], %[cs2]\n\tv_dot2_f32_bf16 %[cs3], v45, %[c01], %[cs3]\n\tv_dot2_f32_bf16 %[cs4], v46, %[c10], %[cs4]\n\tv_dot2_f32_bf16 %[cs5], v46, %[c01], %[cs5]\n\tv_dot2_f32_bf16 %[cs6], v47, %[c10], %[cs6]\n\tv_dot2_f32_bf16 %[cs7], v47, %[c01], %[cs7]\n\ts_barrier\n\tglobal_load_dword v50, %[pf2], off\n\tds_read_b128 v[40:43], %[qa5]\n\tds_read_b128 v[44:47], %[qa0]\n\ts_waitcnt lgkmcnt(0)\n\tv_dot2_f32_bf16 %[cs0], v40, %[c10], %[cs0]\n\tv_dot2_f32_bf16 %[cs1], v40, %[c01], %[cs1]\n\tv_dot2_f32_bf16 %[cs2], v41, %[c10], %[cs2]\n\tv_dot2_f32_bf16 %[cs3], v41, %[c01], %[cs3]\n\tv_dot2_f32_bf16 %[cs4], v42, %[c10], %[cs4]\n\tv_dot2_f32_bf16 %[cs5], v42, %[c01], %[cs5]\n\tv_dot2_f32_bf16 %[cs6], v43, %[c10], %[cs6]\n\tv_dot2_f32_bf16 %[cs7], v43, %[c01], %[cs7]\n\tv_dot2_f32_bf16 %[cs0], v44, %[c10], %[cs0]\n\tv_dot2_f32_bf16 %[cs1], v44, %[c01], %[cs1]\n\tv_dot2_f32_bf16 %[cs2], v45, %[c10], %[cs2]\n\tv_dot2_f32_bf16 %[cs3], v45, %[c01], %[cs3]\n\tv_dot2_f32_bf16 %[cs4], v46, %[c10], %[cs4]\n\tv_dot2_f32_bf16 %[cs5], v46, %[c01], %[cs5]\n\tv_dot2_f32_bf16 %[cs6], v47, %[c10], %[cs6]\n\tv_dot2_f32_bf16 %[cs7], v47, %[c01], %[cs7]\n\ts_barrier\n\tds_read_b128 v[40:43], %[qa1]\n\tds_read_b128 v[44:47], %[qa2]\n\ts_waitcnt lgkmcnt(0)\n\tv_dot2_f32_bf16 %[cs0], v40, %[c10], %[cs0]\n\tv_dot2_f32_bf16 %[cs1], v40, %[c01], %[cs1]\n\tv_dot2_f32_bf16 %[cs2], v41, %[c10], %[cs2]\n\tv_dot2_f32_bf16 %[cs3], v41, %[c01], %[cs3]\n\tv_dot2_f32_bf16 %[cs4], v42, %[c10], %[cs4]\n\tv_dot2_f32_bf16 %[cs5], v42, %[c01], %[cs5]\n\tv_dot2_f32_bf16 %[cs6], v43, %[c10], %[cs6]\n\tv_dot2_f32_bf16 %[cs7], v43, %[c01], %[cs7]\n\tv_dot2_f32_bf16 %[cs0], v44, %[c10], %[cs0]\n\tv_dot2_f32_bf16 %[cs1], v44, %[c01], %[cs1]\n\tv_dot2_f32_bf16 %[cs2], v45, %[c10], %[cs2]\n\tv_dot2_f32_bf16 %[cs3], v45, %[c01], %[cs3]\n\tv_dot2_f32_bf16 %[cs4], v46, %[c10], %[cs4]\n\tv_dot2_f32_bf16 %[cs5], v46, %[c01], %[cs5]\n\tv_dot2_f32_bf16 %[cs6], v47, %[c10], %[cs6]\n\tv_dot2_f32_bf16 %[cs7], v47, %[c01], %[cs7]\n\ts_barrier\n\tds_read_b128 v[40:43], %[qa3]\n\tds_read_b128 v[44:47], %[qa4]\n\ts_waitcnt lgkmcnt(0)\n\tv_dot2_f32_bf16 %[cs0], v40, %[c10], %[cs0]\n\tv_dot2_f32_bf16 %[cs1], v40, %[c01], %[cs1]\n\tv_dot2_f32_bf16 %[cs2], v41, %[c10], %[cs2]\n\tv_dot2_f32_bf16 %[cs3], v41, %[c01], %[cs3]\n\tv_dot2_f32_bf16 %[cs4], v42, %[c10], %[cs4]\n\tv_dot2_f32_bf16 %[cs5], v42, %[c01], %[cs5]\n\tv_dot2_f32_bf16 %[cs6], v43, %[c10], %[cs6]\n\tv_dot2_f32_bf16 %[cs7], v43, %[c01], %[cs7]\n\tv_dot2_f32_bf16 %[cs0], v44, %[c10], %[cs0]\n\tv_dot2_f32_bf16 %[cs1], v44, %[c01], %[cs1]\n\tv_dot2_f32_bf16 %[cs2], v45, %[c10], %[cs2]\n\tv_dot2_f32_bf16 %[cs3], v45, %[c01], %[cs3]\n\tv_dot2_f32_bf16 %[cs4], v46, %[c10], %[cs4]\n\tv_dot2_f32_bf16 %[cs5], v46, %[c01], %[cs5]\n\tv_dot2_f32_bf16 %[cs6], v47, %[c10], %[cs6]\n\tv_dot2_f32_bf16 %[cs7], v47, %[c01], %[cs7]\n\ts_barrier\n\tglobal_load_dword v51, %[pf3], off\n\tds_read_b128 v[40:43], %[qa5]\n\tds_read_b128 v[44:47], %[qa0]\n\ts_waitcnt lgkmcnt(0)\n\tv_dot2_f32_bf16 %[cs0], v40, %[c10], %[cs0]\n\tv_dot2_f32_bf16 %[cs1], v40, %[c01], %[cs1]\n\tv_dot2_f32_bf16 %[cs2], v41, %[c10], %[cs2]\n\tv_dot2_f32_bf16 %[cs3], v41, %[c01], %[cs3]\n\tv_dot2_f32_bf16 %[cs4], v42, %[c10], %[cs4]\n\tv_dot2_f32_bf16 %[cs5], v42, %[c01], %[cs5]\n\tv_dot2_f32_bf16 %[cs6], v43, %[c10], %[cs6]\n\tv_dot2_f32_bf16 %[cs7], v43, %[c01], %[cs7]\n\tv_dot2_f32_bf16 %[cs0], v44, %[c10], %[cs0]\n\tv_dot2_f32_bf16 %[cs1], v44, %[c01], %[cs1]\n\tv_dot2_f32_bf16 %[cs2], v45, %[c10], %[cs2]\n\tv_dot2_f32_bf16 %[cs3], v45, %[c01], %[cs3]\n\tv_dot2_f32_bf16 %[cs4], v46, %[c10], %[cs4]\n\tv_dot2_f32_bf16 %[cs5], v46, %[c01], %[cs5]\n\tv_dot2_f32_bf16 %[cs6], v47, %[c10], %[cs6]\n\tv_dot2_f32_bf16 %[cs7], v47, %[c01], %[cs7]\n\ts_barrier\n\tds_read_b128 v[40:43], %[qa1]\n\tds_read_b128 v[44:47], %[qa2]\n\ts_waitcnt lgkmcnt(0)\n\tv_dot2_f32_bf16 %[cs0], v40, %[c10], %[cs0]\n\tv_dot2_f32_bf16 %[cs1], v40, %[c01], %[cs1]\n\tv_dot2_f32_bf16 %[cs2], v41, %[c10], %[cs2]\n\tv_dot2_f32_bf16 %[cs3], v41, %[c01], %[cs3]\n\tv_dot2_f32_bf16 %[cs4], v42, %[c10], %[cs4]\n\tv_dot2_f32_bf16 %[cs5], v42, %[c01], %[cs5]\n\tv_dot2_f32_bf16 %[cs6], v43, %[c10], %[cs6]\n\tv_dot2_f32_bf16 %[cs7], v43, %[c01], %[cs7]\n\tv_dot2_f32_bf16 %[cs0], v44, %[c10], %[cs0]\n\tv_dot2_f32_bf16 %[cs1], v44, %[c01], %[cs1]\n\tv_dot2_f32_bf16 %[cs2], v45, %[c10], %[cs2]\n\tv_dot2_f32_bf16 %[cs3], v45, %[c01], %[cs3]\n\tv_dot2_f32_bf16 %[cs4], v46, %[c10], %[cs4]\n\tv_dot2_f32_bf16 %[cs5], v46, %[c01], %[cs5]\n\tv_dot2_f32_bf16 %[cs6], v47, %[c10], %[cs6]\n\tv_dot2_f32_bf16 %[cs7], v47, %[c01], %[cs7]\n\ts_barrier\n\tds_read_b128 v[40:43], %[qa3]\n\tds_read_b128 v[44:47], %[qa4]\n\ts_waitcnt lgkmcnt(0)\n\tv_dot2_f32_bf16 %[cs0], v40, %[c10], %[cs0]\n\tv_dot2_f32_bf16 %[cs1], v40, %[c01], %[cs1]\n\tv_dot2_f32_bf16 %[cs2], v41, %[c10], %[cs2]\n\tv_dot2_f32_bf16 %[cs3], v41, %[c01], %[cs3]\n\tv_dot2_f32_bf16 %[cs4], v42, %[c10], %[cs4]\n\tv_dot2_f32_bf16 %[cs5], v42, %[c01], %[cs5]\n\tv_dot2_f32_bf16 %[cs6], v43, %[c10], %[cs6]\n\tv_dot2_f32_bf16 %[cs7], v43, %[c01], %[cs7]\n\tv_dot2_f32_bf16 %[cs0], v44, %[c10], %[cs0]\n\tv_dot2_f32_bf16 %[cs1], v44, %[c01], %[cs1]\n\tv_dot2_f32_bf16 %[cs2], v45, %[c10], %[cs2]\n\tv_dot2_f32_bf16 %[cs3], v45, %[c01], %[cs3]\n\tv_dot2_f32_bf16 %[cs4], v46, %[c10], %[cs4]\n\tv_dot2_f32_bf16 %[cs5], v46, %[c01], %[cs5]\n\tv_dot2_f32_bf16 %[cs6], v47, %[c10], %[cs6]\n\tv_dot2_f32_bf16 %[cs7], v47, %[c01], %[cs7]\n\ts_barrier\n\tds_read_b128 v[40:43], %[qa5]\n\tds_read_b128 v[44:47], %[qa0]\n\ts_waitcnt lgkmcnt(0)\n\tv_dot2_f32_bf16 %[cs0], v40, %[c10], %[cs0]\n\tv_dot2_f32_bf16 %[cs1], v40, %[c01], %[cs1]\n\tv_dot2_f32_bf16 %[cs2], v41, %[c10], %[cs2]\n\tv_dot2_f32_bf16 %[cs3], v41, %[c01], %[cs3]\n\tv_dot2_f32_bf16 %[cs4], v42, %[c10], %[cs4]\n\tv_dot2_f32_bf16 %[cs5], v42, %[c01], %[cs5]\n\tv_dot2_f32_bf16 %[cs6], v43, %[c10], %[cs6]\n\tv_dot2_f32_bf16 %[cs7], v43, %[c01], %[cs7]\n\tv_dot2_f32_bf16 %[cs0], v44, %[c10], %[cs0]\n\tv_dot2_f32_bf16 %[cs1], v44, %[c01], %[cs1]\n\tv_dot2_f32_bf16 %[cs2], v45, %[c10], %[cs2]\n\tv_dot2_f32_bf16 %[cs3], v45, %[c01], %[cs3]\n\tv_dot2_f32_bf16 %[cs4], v46, %[c10], %[cs4]\n\tv_dot2_f32_bf16 %[cs5], v46, %[c01], %[cs5]\n\tv_dot2_f32_bf16 %[cs6], v47, %[c10], %[cs6]\n\tv_dot2_f32_bf16 %[cs7], v47, %[c01], %[cs7]\n\ts_barrier\n\tds_read_b128 v[40:43], %[qa1]\n\tds_read_b128 v[44:47], %[qa2]\n\ts_waitcnt lgkmcnt(0)\n\tv_dot2_f32_bf16 %[cs0], v40, %[c10], %[cs0]\n\tv_dot2_f32_bf16 %[cs1], v40, %[c01], %[cs1]\n\tv_dot2_f32_bf16 %[cs2], v41, %[c10], %[cs2]\n\tv_dot2_f32_bf16 %[cs3], v41, %[c01], %[cs3]\n\tv_dot2_f32_bf16 %[cs4], v42, %[c10], %[cs4]\n\tv_dot2_f32_bf16 %[cs5], v42, %[c01], %[cs5]\n\tv_dot2_f32_bf16 %[cs6], v43, %[c10], %[cs6]\n\tv_dot2_f32_bf16 %[cs7], v43, %[c01], %[cs7]\n\tv_dot2_f32_bf16 %[cs0], v44, %[c10], %[cs0]\n\tv_dot2_f32_bf16 %[cs1], v44, %[c01], %[cs1]\n\tv_dot2_f32_bf16 %[cs2], v45, %[c10], %[cs2]\n\tv_dot2_f32_bf16 %[cs3], v45, %[c01], %[cs3]\n\tv_dot2_f32_bf16 %[cs4], v46, %[c10], %[cs4]\n\tv_dot2_f32_bf16 %[cs5], v46, %[c01], %[cs5]\n\tv_dot2_f32_bf16 %[cs6], v47, %[c10], %[cs6]\n\tv_dot2_f32_bf16 %[cs7], v47, %[c01], %[cs7]\n\ts_barrier\n\tds_read_b128 v[40:43], %[qa3]\n\tds_read_b128 v[44:47], %[qa4]\n\ts_waitcnt lgkmcnt(0)\n\tv_dot2_f32_bf16 %[cs0], v40, %[c10], %[cs0]\n\tv_dot2_f32_bf16 %[cs1], v40, %[c01], %[cs1]\n\tv_dot2_f32_bf16 %[cs2], v41, %[c10], %[cs2]\n\tv_dot2_f32_bf16 %[cs3], v41, %[c01], %[cs3]\n\tv_dot2_f32_bf16 %[cs4], v42, %[c10], %[cs4]\n\tv_dot2_f32_bf16 %[cs5], v42, %[c01], %[cs5]\n\tv_dot2_f32_bf16 %[cs6], v43, %[c10], %[cs6]\n\tv_dot2_f32_bf16 %[cs7], v43, %[c01], %[cs7]\n\tv_dot2_f32_bf16 %[cs0], v44, %[c10], %[cs0]\n\tv_dot2_f32_bf16 %[cs1], v44, %[c01], %[cs1]\n\tv_dot2_f32_bf16 %[cs2], v45, %[c10], %[cs2]\n\tv_dot2_f32_bf16 %[cs3], v45, %[c01], %[cs3]\n\tv_dot2_f32_bf16 %[cs4], v46, %[c10], %[cs4]\n\tv_dot2_f32_bf16 %[cs5], v46, %[c01], %[cs5]\n\tv_dot2_f32_bf16 %[cs6], v47, %[c10], %[cs6]\n\tv_dot2_f32_bf16 %[cs7], v47, %[c01], %[cs7]\n\ts_barrier\n\tds_read_b128 v[40:43], %[qa5]\n\tds_read_b128 v[44:47], %[qa0]\n\ts_waitcnt lgkmcnt(0)\n\tv_dot2_f32_bf16 %[cs0], v40, %[c10], %[cs0]\n\tv_dot2_f32_bf16 %[cs1], v40, %[c01], %[cs1]\n\tv_dot2_f32_bf16 %[cs2], v41, %[c10], %[cs2]\n\tv_dot2_f32_bf16 %[cs3], v41, %[c01], %[cs3]\n\tv_dot2_f32_bf16 %[cs4], v42, %[c10], %[cs4]\n\tv_dot2_f32_bf16 %[cs5], v42, %[c01], %[cs5]\n\tv_dot2_f32_bf16 %[cs6], v43, %[c10], %[cs6]\n\tv_dot2_f32_bf16 %[cs7], v43, %[c01], %[cs7]\n\tv_dot2_f32_bf16 %[cs0], v44, %[c10], %[cs0]\n\tv_dot2_f32_bf16 %[cs1], v44, %[c01], %[cs1]\n\tv_dot2_f32_bf16 %[cs2], v45, %[c10], %[cs2]\n\tv_dot2_f32_bf16 %[cs3], v45, %[c01], %[cs3]\n\tv_dot2_f32_bf16 %[cs4], v46, %[c10], %[cs4]\n\tv_dot2_f32_bf16 %[cs5], v46, %[c01], %[cs5]\n\tv_dot2_f32_bf16 %[cs6], v47, %[c10], %[cs6]\n\tv_dot2_f32_bf16 %[cs7], v47, %[c01], %[cs7]\n\ts_barrier\n\tds_read_b128 v[40:43], %[qa1]\n\ts_waitcnt lgkmcnt(0)\n\tv_dot2_f32_bf16 %[cs0], v40, %[c10], %[cs0]\n\tv_dot2_f32_bf16 %[cs1], v40, %[c01], %[cs1]\n\tv_dot2_f32_bf16 %[cs2], v41, %[c10], %[cs2]\n\tv_dot2_f32_bf16 %[cs3], v41, %[c01], %[cs3]\n\tv_dot2_f32_bf16 %[cs4], v42, %[c10], %[cs4]\n\tv_dot2_f32_bf16 %[cs5], v42, %[c01], %[cs5]\n\tv_dot2_f32_bf16 %[cs6], v43, %[c10], %[cs6]\n\tv_dot2_f32_bf16 %[cs7], v43, %[c01], %[cs7]\n\ts_nop 3\n\ts_waitcnt vmcnt(0)"
#define L1_3_CLOB "s66","v40","v41","v42","v43","v44","v45","v46","v47","v43","v44","v48","v49","v50","v51","s71","vcc"
#define L2_0_ASM "s_mov_b32 s40, %[oA]\n\ts_mov_b32 s41, %[oB]\n\ts_add_u32 m0, %[lds0], 0\n\ts_nop 0\n\tbuffer_load_dwordx4 %[vA0], %[rA], s40 offen offset:0 lds\n\tbuffer_load_dwordx4 %[vA1], %[rA], s40 offen offset:1024 lds\n\tbuffer_load_dwordx4 %[vA2], %[rA], s40 offen offset:2048 lds\n\tbuffer_load_dwordx4 %[vA3], %[rA], s40 offen offset:3072 lds\n\ts_add_u32 m0, %[lds0], 16384\n\ts_nop 0\n\tbuffer_load_dwordx4 %[vB0], %[rB], s41 offen offset:0 lds\n\tbuffer_load_dwordx4 %[vB1], %[rB], s41 offen offset:1024 lds\n\ts_add_u32 s40, s40, 1024\n\ts_add_u32 s41, s41, 65536\n\ts_add_u32 m0, %[lds0], 24576\n\ts_nop 0\n\tbuffer_load_dwordx4 %[vA0], %[rA], s40 offen offset:0 lds\n\tbuffer_load_dwordx4 %[vA1], %[rA], s40 offen offset:1024 lds\n\tbuffer_load_dwordx4 %[vA2], %[rA], s40 offen offset:2048 lds\n\tbuffer_load_dwordx4 %[vA3], %[rA], s40 offen offset:3072 lds\n\ts_add_u32 m0, %[lds0], 40960\n\ts_nop 0\n\tbuffer_load_dwordx4 %[vB0], %[rB], s41 offen offset:0 lds\n\tbuffer_load_dwordx4 %[vB1], %[rB], s41 offen offset:1024 lds\n\ts_add_u32 s40, s40, 1024\n\ts_add_u32 s41, s41, 65536\n\ts_add_u32 m0, %[lds0], 49152\n\ts_nop 0\n\tbuffer_load_dwordx4 %[vA0], %[rA], s40 offen offset:0 lds\n\tbuffer_load_dwordx4 %[vA1], %[rA], s40 offen offset:1024 lds\n\tbuffer_load_dwordx4 %[vA2], %[rA], s40 offen offset:2048 lds\n\tbuffer_load_dwordx4 %[vA3], %[rA], s40 offen offset:3072 lds\n\ts_add_u32 m0, %[lds0], 65536\n\ts_nop 0\n\tbuffer_load_dwordx4 %[vB0], %[rB], s41 offen offset:0 lds\n\tbuffer_load_dwordx4 %[vB1], %[rB], s41 offen offset:1024 lds\n\ts_add_u32 s40, s40, 1024\n\ts_add_u32 s41, s41, 65536\n\ts_add_u32 m0, %[lds0], 73728\n\ts_nop 0\n\tbuffer_load_dwordx4 %[vA0], %[rA], s40 offen offset:0 lds\n\tbuffer_load_dwordx4 %[vA1], %[rA], s40 offen offset:1024 lds\n\tbuffer_load_dwordx4 %[vA2], %[rA], s40 offen offset:2048 lds\n\tbuffer_load_dwordx4 %[vA3], %[rA], s40 offen offset:3072 lds\n\ts_add_u32 m0, %[lds0], 90112\n\ts_nop 0\n\tbuffer_load_dwordx4 %[vB0], %[rB], s41 offen offset:0 lds\n\tbuffer_load_dwordx4 %[vB1], %[rB], s41 offen offset:1024 lds\n\ts_add_u32 s40, s40, 1024\n\ts_add_u32 s41, s41, 65536\n\ts_waitcnt vmcnt(18)\n\ts_barrier\n\tds_read_b128 v[52:55], %[os] offset:0\n\tds_read_b128 v[56:59], %[os] offset:1024\n\tds_read_b128 v[60:63], %[os] offset:2048\n\tds_read_b128 v[64:67], %[os] offset:3072\n\ts_waitcnt lgkmcnt(0)\n\ts_waitcnt vmcnt(6)\n\ts_barrier\n\ts_add_u32 m0, %[lds0], 98304\n\ts_nop 0\n\tbuffer_load_dwordx4 %[vA0], %[rA], s40 offen offset:0 lds\n\tbuffer_load_dwordx4 %[vA1], %[rA], s40 offen offset:1024 lds\n\tbuffer_load_dwordx4 %[vA2], %[rA], s40 offen offset:2048 lds\n\tbuffer_load_dwordx4 %[vA3], %[rA], s40 offen offset:3072 lds\n\ts_add_u32 m0, %[lds0], 114688\n\ts_nop 0\n\tbuffer_load_dwordx4 %[vB0], %[rB], s41 offen offset:0 lds\n\tbuffer_load_dwordx4 %[vB1], %[rB], s41 offen offset:1024 lds\n\ts_add_u32 s40, s40, 1024\n\ts_add_u32 s41, s41, 65536\n\ts_add_u32 m0, %[lds0], 122880\n\ts_nop 0\n\tbuffer_load_dwordx4 %[vA0], %[rA], s40 offen offset:0 lds\n\tbuffer_load_dwordx4 %[vA1], %[rA], s40 offen offset:1024 lds\n\tbuffer_load_dwordx4 %[vA2], %[rA], s40 offen offset:2048 lds\n\tbuffer_load_dwordx4 %[vA3], %[rA], s40 offen offset:3072 lds\n\ts_add_u32 m0, %[lds0], 139264\n\ts_nop 0\n\tbuffer_load_dwordx4 %[vB0], %[rB], s41 offen offset:0 lds\n\tbuffer_load_dwordx4 %[vB1], %[rB], s41 offen offset:1024 lds\n\ts_add_u32 s40, s40, 1024\n\ts_add_u32 s41, s41, 65536\n\ts_mov_b32 s70, 0\n\tbuffer_store_dwordx4 v[52:55], %[ooff], %[ro], s70 offen sc1\n\ts_mov_b32 s70, 512\n\tbuffer_store_dwordx4 v[56:59], %[ooff], %[ro], s70 offen sc1\n\ts_mov_b32 s70, 65536\n\tbuffer_store_dwordx4 v[60:63], %[ooff], %[ro], s70 offen sc1\n\ts_mov_b32 s70, 66048\n\tbuffer_store_dwordx4 v[64:67], %[ooff], %[ro], s70 offen sc1\n\ts_add_u32 m0, %[lds0], 0\n\ts_nop 0\n\tbuffer_load_dwordx4 %[vA0], %[rA], s40 offen offset:0 lds\n\tbuffer_load_dwordx4 %[vA1], %[rA], s40 offen offset:1024 lds\n\tbuffer_load_dwordx4 %[vA2], %[rA], s40 offen offset:2048 lds\n\tbuffer_load_dwordx4 %[vA3], %[rA], s40 offen offset:3072 lds\n\ts_add_u32 m0, %[lds0], 16384\n\ts_nop 0\n\tbuffer_load_dwordx4 %[vB0], %[rB], s41 offen offset:0 lds\n\tbuffer_load_dwordx4 %[vB1], %[rB], s41 offen offset:1024 lds\n\ts_add_u32 s40, s40, 1024\n\ts_add_u32 s41, s41, 65536\n\ts_mov_b32 s66, 3\n\tL20_l0_%=:\n\ts_waitcnt vmcnt(12)\n\ts_barrier\n\ts_add_u32 m0, %[lds0], 24576\n\ts_nop 0\n\tbuffer_load_dwordx4 %[vA0], %[rA], s40 offen offset:0 lds\n\tbuffer_load_dwordx4 %[vA1], %[rA], s40 offen offset:1024 lds\n\tbuffer_load_dwordx4 %[vA2], %[rA], s40 offen offset:2048 lds\n\tbuffer_load_dwordx4 %[vA3], %[rA], s40 offen offset:3072 lds\n\ts_add_u32 m0, %[lds0], 40960\n\ts_nop 0\n\tbuffer_load_dwordx4 %[vB0], %[rB], s41 offen offset:0 lds\n\tbuffer_load_dwordx4 %[vB1], %[rB], s41 offen offset:1024 lds\n\ts_add_u32 s40, s40, 1024\n\ts_add_u32 s41, s41, 65536\n\ts_add_u32 m0, %[lds0], 49152\n\ts_nop 0\n\tbuffer_load_dwordx4 %[vA0], %[rA], s40 offen offset:0 lds\n\tbuffer_load_dwordx4 %[vA1], %[rA], s40 offen offset:1024 lds\n\tbuffer_load_dwordx4 %[vA2], %[rA], s40 offen offset:2048 lds\n\tbuffer_load_dwordx4 %[vA3], %[rA], s40 offen offset:3072 lds\n\ts_add_u32 m0, %[lds0], 65536\n\ts_nop 0\n\tbuffer_load_dwordx4 %[vB0], %[rB], s41 offen offset:0 lds\n\tbuffer_load_dwordx4 %[vB1], %[rB], s41 offen offset:1024 lds\n\ts_add_u32 s40, s40, 1024\n\ts_add_u32 s41, s41, 65536\n\ts_waitcnt vmcnt(12)\n\ts_barrier\n\ts_add_u32 m0, %[lds0], 73728\n\ts_nop 0\n\tbuffer_load_dwordx4 %[vA0], %[rA], s40 offen offset:0 lds\n\tbuffer_load_dwordx4 %[vA1], %[rA], s40 offen offset:1024 lds\n\tbuffer_load_dwordx4 %[vA2], %[rA], s40 offen offset:2048 lds\n\tbuffer_load_dwordx4 %[vA3], %[rA], s40 offen offset:3072 lds\n\ts_add_u32 m0, %[lds0], 90112\n\ts_nop 0\n\tbuffer_load_dwordx4 %[vB0], %[rB], s41 offen offset:0 lds\n\tbuffer_load_dwordx4 %[vB1], %[rB], s41 offen offset:1024 lds\n\ts_add_u32 s40, s40, 1024\n\ts_add_u32 s41, s41, 65536\n\ts_add_u32 m0, %[lds0], 98304\n\ts_nop 0\n\tbuffer_load_dwordx4 %[vA0], %[rA], s40 offen offset:0 lds\n\tbuffer_load_dwordx4 %[vA1], %[rA], s40 offen offset:1024 lds\n\tbuffer_load_dwordx4 %[vA2], %[rA], s40 offen offset:2048 lds\n\tbuffer_load_dwordx4 %[vA3], %[rA], s40 offen offset:3072 lds\n\ts_add_u32 m0, %[lds0], 114688\n\ts_nop 0\n\tbuffer_load_dwordx4 %[vB0], %[rB], s41 offen offset:0 lds\n\tbuffer_load_dwordx4 %[vB1], %[rB], s41 offen offset:1024 lds\n\ts_add_u32 s40, s40, 1024\n\ts_add_u32 s41, s41, 65536\n\ts_waitcnt vmcnt(12)\n\ts_barrier\n\ts_add_u32 m0, %[lds0], 122880\n\ts_nop 0\n\tbuffer_load_dwordx4 %[vA0], %[rA], s40 offen offset:0 lds\n\tbuffer_load_dwordx4 %[vA1], %[rA], s40 offen offset:1024 lds\n\tbuffer_load_dwordx4 %[vA2], %[rA], s40 offen offset:2048 lds\n\tbuffer_load_dwordx4 %[vA3], %[rA], s40 offen offset:3072 lds\n\ts_add_u32 m0, %[lds0], 139264\n\ts_nop 0\n\tbuffer_load_dwordx4 %[vB0], %[rB], s41 offen offset:0 lds\n\tbuffer_load_dwordx4 %[vB1], %[rB], s41 offen offset:1024 lds\n\ts_add_u32 s40, s40, 1024\n\ts_add_u32 s41, s41, 65536\n\ts_add_u32 m0, %[lds0], 0\n\ts_nop 0\n\tbuffer_load_dwordx4 %[vA0], %[rA], s40 offen offset:0 lds\n\tbuffer_load_dwordx4 %[vA1], %[rA], s40 offen offset:1024 lds\n\tbuffer_load_dwordx4 %[vA2], %[rA], s40 offen offset:2048 lds\n\tbuffer_load_dwordx4 %[vA3], %[rA], s40 offen offset:3072 lds\n\ts_add_u32 m0, %[lds0], 16384\n\ts_nop 0\n\tbuffer_load_dwordx4 %[vB0], %[rB], s41 offen offset:0 lds\n\tbuffer_load_dwordx4 %[vB1], %[rB], s41 offen offset:1024 lds\n\ts_add_u32 s40, s40, 1024\n\ts_add_u32 s41, s41, 65536\n\ts_sub_u32 s66, s66, 1\n\ts_cmp_lg_u32 s66, 0\n\ts_cbranch_scc1 L20_l0_%=\n\ts_waitcnt vmcnt(12)\n\ts_barrier\n\ts_add_u32 m0, %[lds0], 24576\n\ts_nop 0\n\tbuffer_load_dwordx4 %[vA0], %[rA], s40 offen offset:0 lds\n\tbuffer_load_dwordx4 %[vA1], %[rA], s40 offen offset:1024 lds\n\tbuffer_load_dwordx4 %[vA2], %[rA], s40 offen offset:2048 lds\n\tbuffer_load_dwordx4 %[vA3], %[rA], s40 offen offset:3072 lds\n\ts_add_u32 m0, %[lds0], 40960\n\ts_nop 0\n\tbuffer_load_dwordx4 %[vB0], %[rB], s41 offen offset:0 lds\n\tbuffer_load_dwordx4 %[vB1], %[rB], s41 offen offset:1024 lds\n\ts_add_u32 s40, s40, 1024\n\ts_add_u32 s41, s41, 65536\n\ts_add_u32 m0, %[lds0], 49152\n\ts_nop 0\n\tbuffer_load_dwordx4 %[vA0], %[rA], s40 offen offset:0 lds\n\tbuffer_load_dwordx4 %[vA1], %[rA], s40 offen offset:1024 lds\n\tbuffer_load_dwordx4 %[vA2], %[rA], s40 offen offset:2048 lds\n\tbuffer_load_dwordx4 %[vA3], %[rA], s40 offen offset:3072 lds\n\ts_add_u32 m0, %[lds0], 65536\n\ts_nop 0\n\tbuffer_load_dwordx4 %[vB0], %[rB], s41 offen offset:0 lds\n\tbuffer_load_dwordx4 %[vB1], %[rB], s41 offen offset:1024 lds\n\ts_add_u32 s40, s40, 1024\n\ts_add_u32 s41, s41, 65536\n\ts_waitcnt vmcnt(12)\n\ts_barrier\n\ts_add_u32 m0, %[lds0], 73728\n\ts_nop 0\n\tbuffer_load_dwordx4 %[vA0], %[rA], s40 offen offset:0 lds\n\tbuffer_load_dwordx4 %[vA1], %[rA], s40 offen offset:1024 lds\n\tbuffer_load_dwordx4 %[vA2], %[rA], s40 offen offset:2048 lds\n\tbuffer_load_dwordx4 %[vA3], %[rA], s40 offen offset:3072 lds\n\ts_add_u32 m0, %[lds0], 90112\n\ts_nop 0\n\tbuffer_load_dwordx4 %[vB0], %[rB], s41 offen offset:0 lds\n\tbuffer_load_dwordx4 %[vB1], %[rB], s41 offen offset:1024 lds\n\ts_add_u32 s40, s40, 1024\n\ts_add_u32 s41, s41, 65536\n\ts_mov_b32 s40, %[oO]\n\ts_mov_b32 s41, %[oW]\n\ts_add_u32 m0, %[lds0], 98304\n\ts_nop 0\n\tbuffer_load_dwordx4 %[vO0], %[rO], s40 offen offset:0 lds\n\tbuffer_load_dwordx4 %[vO1], %[rO], s40 offen offset:1024 lds\n\tbuffer_load_dwordx4 %[vO2], %[rO], s40 offen offset:2048 lds\n\tbuffer_load_dwordx4 %[vO3], %[rO], s40 offen offset:3072 lds\n\ts_add_u32 m0, %[lds0], 114688\n\ts_nop 0\n\tbuffer_load_dwordx4 %[vB0], %[rW], s41 offen offset:0 lds\n\tbuffer_load_dwordx4 %[vB1], %[rW], s41 offen offset:1024 lds\n\ts_add_u32 s40, s40, 1024\n\ts_add_u32 s41, s41, 65536\n\ts_waitcnt vmcnt(12)\n\ts_barrier\n\ts_add_u32 m0, %[lds0], 122880\n\ts_nop 0\n\tbuffer_load_dwordx4 %[vO0], %[rO], s40 offen offset:0 lds\n\tbuffer_load_dwordx4 %[vO1], %[rO], s40 offen offset:1024 lds\n\tbuffer_load_dwordx4 %[vO2], %[rO], s40 offen offset:2048 lds\n\tbuffer_load_dwordx4 %[vO3], %[rO], s40 offen offset:3072 lds\n\ts_add_u32 m0, %[lds0], 139264\n\ts_nop 0\n\tbuffer_load_dwordx4 %[vB0], %[rW], s41 offen offset:0 lds\n\tbuffer_load_dwordx4 %[vB1], %[rW], s41 offen offset:1024 lds\n\ts_add_u32 s40, s40, 1024\n\ts_add_u32 s41, s41, 65536\n\ts_add_u32 m0, %[lds0], 0\n\ts_nop 0\n\tbuffer_load_dwordx4 %[vO0], %[rO], s40 offen offset:0 lds\n\tbuffer_load_dwordx4 %[vO1], %[rO], s40 offen offset:1024 lds\n\tbuffer_load_dwordx4 %[vO2], %[rO], s40 offen offset:2048 lds\n\tbuffer_load_dwordx4 %[vO3], %[rO], s40 offen offset:3072 lds\n\ts_add_u32 m0, %[lds0], 16384\n\ts_nop 0\n\tbuffer_load_dwordx4 %[vB0], %[rW], s41 offen offset:0 lds\n\tbuffer_load_dwordx4 %[vB1], %[rW], s41 offen offset:1024 lds\n\ts_add_u32 s40, s40, 1024\n\ts_add_u32 s41, s41, 65536\n\ts_waitcnt vmcnt(12)\n\ts_barrier\n\ts_add_u32 m0, %[lds0], 24576\n\ts_nop 0\n\tbuffer_load_dwordx4 %[vO0], %[rO], s40 offen offset:0 lds\n\tbuffer_load_dwordx4 %[vO1], %[rO], s40 offen offset:1024 lds\n\tbuffer_load_dwordx4 %[vO2], %[rO], s40 offen offset:2048 lds\n\tbuffer_load_dwordx4 %[vO3], %[rO], s40 offen offset:3072 lds\n\ts_add_u32 m0, %[lds0], 40960\n\ts_nop 0\n\tbuffer_load_dwordx4 %[vB0], %[rW], s41 offen offset:0 lds\n\tbuffer_load_dwordx4 %[vB1], %[rW], s41 offen offset:1024 lds\n\ts_add_u32 s40, s40, 1024\n\ts_add_u32 s41, s41, 65536\n\ts_add_u32 m0, %[lds0], 49152\n\ts_nop 0\n\tbuffer_load_dwordx4 %[vO0], %[rO], s40 offen offset:0 lds\n\tbuffer_load_dwordx4 %[vO1], %[rO], s40 offen offset:1024 lds\n\tbuffer_load_dwordx4 %[vO2], %[rO], s40 offen offset:2048 lds\n\tbuffer_load_dwordx4 %[vO3], %[rO], s40 offen offset:3072 lds\n\ts_add_u32 m0, %[lds0], 65536\n\ts_nop 0\n\tbuffer_load_dwordx4 %[vB0], %[rW], s41 offen offset:0 lds\n\tbuffer_load_dwordx4 %[vB1], %[rW], s41 offen offset:1024 lds\n\ts_add_u32 s40, s40, 1024\n\ts_add_u32 s41, s41, 65536\n\ts_waitcnt vmcnt(12)\n\ts_barrier\n\ts_add_u32 m0, %[lds0], 73728\n\ts_nop 0\n\tbuffer_load_dwordx4 %[vO0], %[rO], s40 offen offset:0 lds\n\tbuffer_load_dwordx4 %[vO1], %[rO], s40 offen offset:1024 lds\n\tbuffer_load_dwordx4 %[vO2], %[rO], s40 offen offset:2048 lds\n\tbuffer_load_dwordx4 %[vO3], %[rO], s40 offen offset:3072 lds\n\ts_add_u32 m0, %[lds0], 90112\n\ts_nop 0\n\tbuffer_load_dwordx4 %[vB0], %[rW], s41 offen offset:0 lds\n\tbuffer_load_dwordx4 %[vB1], %[rW], s41 offen offset:1024 lds\n\ts_add_u32 s40, s40, 1024\n\ts_add_u32 s41, s41, 65536\n\ts_add_u32 m0, %[lds0], 98304\n\ts_nop 0\n\tbuffer_load_dwordx4 %[vO0], %[rO], s40 offen offset:0 lds\n\tbuffer_load_dwordx4 %[vO1], %[rO], s40 offen offset:1024 lds\n\tbuffer_load_dwordx4 %[vO2], %[rO], s40 offen offset:2048 lds\n\tbuffer_load_dwordx4 %[vO3], %[rO], s40 offen offset:3072 lds\n\ts_add_u32 m0, %[lds0], 114688\n\ts_nop 0\n\tbuffer_load_dwordx4 %[vB0], %[rW], s41 offen offset:0 lds\n\tbuffer_load_dwordx4 %[vB1], %[rW], s41 offen offset:1024 lds\n\ts_add_u32 s40, s40, 1024\n\ts_add_u32 s41, s41, 65536\n\ts_waitcnt vmcnt(12)\n\ts_barrier\n\ts_add_u32 m0, %[lds0], 122880\n\ts_nop 0\n\tbuffer_load_dwordx4 %[vO0], %[rO], s40 offen offset:0 lds\n\tbuffer_load_dwordx4 %[vO1], %[rO], s40 offen offset:1024 lds\n\tbuffer_load_dwordx4 %[vO2], %[rO], s40 offen offset:2048 lds\n\tbuffer_load_dwordx4 %[vO3], %[rO], s40 offen offset:3072 lds\n\ts_add_u32 m0, %[lds0], 139264\n\ts_nop 0\n\tbuffer_load_dwordx4 %[vB0], %[rW], s41 offen offset:0 lds\n\tbuffer_load_dwordx4 %[vB1], %[rW], s41 offen offset:1024 lds\n\ts_add_u32 s40, s40, 1024\n\ts_add_u32 s41, s41, 65536\n\ts_waitcnt vmcnt(6)\n\ts_barrier\n\ts_waitcnt vmcnt(0)\n\ts_barrier"
#define L2_0_CLOB "s66","s40","s41","s70","s71","v43","v44","v52","v53","v54","v55","v56","v57","v58","v59","v60","v61","v62","v63","v64","v65","v66","v67"
#define L2_1_ASM "s_mov_b32 s40, %[oA]\n\ts_mov_b32 s41, %[oB]\n\ts_add_u32 m0, %[lds0], 4096\n\ts_nop 0\n\tbuffer_load_dwordx4 %[vA0], %[rA], s40 offen offset:0 lds\n\tbuffer_load_dwordx4 %[vA1], %[rA], s40 offen offset:1024 lds\n\tbuffer_load_dwordx4 %[vA2], %[rA], s40 offen offset:2048 lds\n\tbuffer_load_dwordx4 %[vA3], %[rA], s40 offen offset:3072 lds\n\ts_add_u32 m0, %[lds0], 18432\n\ts_nop 0\n\tbuffer_load_dwordx4 %[vB0], %[rB], s41 offen offset:0 lds\n\tbuffer_load_dwordx4 %[vB1], %[rB], s41 offen offset:1024 lds\n\ts_add_u32 s40, s40, 1024\n\ts_add_u32 s41, s41, 65536\n\ts_add_u32 m0, %[lds0], 28672\n\ts_nop 0\n\tbuffer_load_dwordx4 %[vA0], %[rA], s40 offen offset:0 lds\n\tbuffer_load_dwordx4 %[vA1], %[rA], s40 offen offset:1024 lds\n\tbuffer_load_dwordx4 %[vA2], %[rA], s40 offen offset:2048 lds\n\tbuffer_load_dwordx4 %[vA3], %[rA], s40 offen offset:3072 lds\n\ts_add_u32 m0, %[lds0], 43008\n\ts_nop 0\n\tbuffer_load_dwordx4 %[vB0], %[rB], s41 offen offset:0 lds\n\tbuffer_load_dwordx4 %[vB1], %[rB], s41 offen offset:1024 lds\n\ts_add_u32 s40, s40, 1024\n\ts_add_u32 s41, s41, 65536\n\ts_add_u32 m0, %[lds0], 53248\n\ts_nop 0\n\tbuffer_load_dwordx4 %[vA0], %[rA], s40 offen offset:0 lds\n\tbuffer_load_dwordx4 %[vA1], %[rA], s40 offen offset:1024 lds\n\tbuffer_load_dwordx4 %[vA2], %[rA], s40 offen offset:2048 lds\n\tbuffer_load_dwordx4 %[vA3], %[rA], s40 offen offset:3072 lds\n\ts_add_u32 m0, %[lds0], 67584\n\ts_nop 0\n\tbuffer_load_dwordx4 %[vB0], %[rB], s41 offen offset:0 lds\n\tbuffer_load_dwordx4 %[vB1], %[rB], s41 offen offset:1024 lds\n\ts_add_u32 s40, s40, 1024\n\ts_add_u32 s41, s41, 65536\n\ts_add_u32 m0, %[lds0], 77824\n\ts_nop 0\n\tbuffer_load_dwordx4 %[vA0], %[rA], s40 offen offset:0 lds\n\tbuffer_load_dwordx4 %[vA1], %[rA], s40 offen offset:1024 lds\n\tbuffer_load_dwordx4 %[vA2], %[rA], s40 offen offset:2048 lds\n\tbuffer_load_dwordx4 %[vA3], %[rA], s40 offen offset:3072 lds\n\ts_add_u32 m0, %[lds0], 92160\n\ts_nop 0\n\tbuffer_load_dwordx4 %[vB0], %[rB], s41 offen offset:0 lds\n\tbuffer_load_dwordx4 %[vB1], %[rB], s41 offen offset:1024 lds\n\ts_add_u32 s40, s40, 1024\n\ts_add_u32 s41, s41, 65536\n\ts_waitcnt vmcnt(18)\n\ts_barrier\n\tds_read_b128 v[52:55], %[os] offset:0\n\tds_read_b128 v[56:59], %[os] offset:1024\n\tds_read_b128 v[60:63], %[os] offset:2048\n\tds_read_b128 v[64:67], %[os] offset:3072\n\ts_waitcnt lgkmcnt(0)\n\ts_waitcnt vmcnt(6)\n\ts_barrier\n\ts_add_u32 m0, %[lds0], 102400\n\ts_nop 0\n\tbuffer_load_dwordx4 %[vA0], %[rA], s40 offen offset:0 lds\n\tbuffer_load_dwordx4 %[vA1], %[rA], s40 offen offset:1024 lds\n\tbuffer_load_dwordx4 %[vA2], %[rA], s40 offen offset:2048 lds\n\tbuffer_load_dwordx4 %[vA3], %[rA], s40 offen offset:3072 lds\n\ts_add_u32 m0, %[lds0], 116736\n\ts_nop 0\n\tbuffer_load_dwordx4 %[vB0], %[rB], s41 offen offset:0 lds\n\tbuffer_load_dwordx4 %[vB1], %[rB], s41 offen offset:1024 lds\n\ts_add_u32 s40, s40, 1024\n\ts_add_u32 s41, s41, 65536\n\ts_add_u32 m0, %[lds0], 126976\n\ts_nop 0\n\tbuffer_load_dwordx4 %[vA0], %[rA], s40 offen offset:0 lds\n\tbuffer_load_dwordx4 %[vA1], %[rA], s40 offen offset:1024 lds\n\tbuffer_load_dwordx4 %[vA2], %[rA], s40 offen offset:2048 lds\n\tbuffer_load_dwordx4 %[vA3], %[rA], s40 offen offset:3072 lds\n\ts_add_u32 m0, %[lds0], 141312\n\ts_nop 0\n\tbuffer_load_dwordx4 %[vB0], %[rB], s41 offen offset:0 lds\n\tbuffer_load_dwordx4 %[vB1], %[rB], s41 offen offset:1024 lds\n\ts_add_u32 s40, s40, 1024\n\ts_add_u32 s41, s41, 65536\n\ts_mov_b32 s70, 0\n\tbuffer_store_dwordx4 v[52:55], %[ooff], %[ro], s70 offen sc1\n\ts_mov_b32 s70, 512\n\tbuffer_store_dwordx4 v[56:59], %[ooff], %[ro], s70 offen sc1\n\ts_mov_b32 s70, 65536\n\tbuffer_store_dwordx4 v[60:63], %[ooff], %[ro], s70 offen sc1\n\ts_mov_b32 s70, 66048\n\tbuffer_store_dwordx4 v[64:67], %[ooff], %[ro], s70 offen sc1\n\ts_add_u32 m0, %[lds0], 4096\n\ts_nop 0\n\tbuffer_load_dwordx4 %[vA0], %[rA], s40 offen offset:0 lds\n\tbuffer_load_dwordx4 %[vA1], %[rA], s40 offen offset:1024 lds\n\tbuffer_load_dwordx4 %[vA2], %[rA], s40 offen offset:2048 lds\n\tbuffer_load_dwordx4 %[vA3], %[rA], s40 offen offset:3072 lds\n\ts_add_u32 m0, %[lds0], 18432\n\ts_nop 0\n\tbuffer_load_dwordx4 %[vB0], %[rB], s41 offen offset:0 lds\n\tbuffer_load_dwordx4 %[vB1], %[rB], s41 offen offset:1024 lds\n\ts_add_u32 s40, s40, 1024\n\ts_add_u32 s41, s41, 65536\n\ts_mov_b32 s66, 3\n\tL21_l0_%=:\n\ts_waitcnt vmcnt(12)\n\ts_barrier\n\ts_add_u32 m0, %[lds0], 28672\n\ts_nop 0\n\tbuffer_load_dwordx4 %[vA0], %[rA], s40 offen offset:0 lds\n\tbuffer_load_dwordx4 %[vA1], %[rA], s40 offen offset:1024 lds\n\tbuffer_load_dwordx4 %[vA2], %[rA], s40 offen offset:2048 lds\n\tbuffer_load_dwordx4 %[vA3], %[rA], s40 offen offset:3072 lds\n\ts_add_u32 m0, %[lds0], 43008\n\ts_nop 0\n\tbuffer_load_dwordx4 %[vB0], %[rB], s41 offen offset:0 lds\n\tbuffer_load_dwordx4 %[vB1], %[rB], s41 offen offset:1024 lds\n\ts_add_u32 s40, s40, 1024\n\ts_add_u32 s41, s41, 65536\n\ts_add_u32 m0, %[lds0], 53248\n\ts_nop 0\n\tbuffer_load_dwordx4 %[vA0], %[rA], s40 offen offset:0 lds\n\tbuffer_load_dwordx4 %[vA1], %[rA], s40 offen offset:1024 lds\n\tbuffer_load_dwordx4 %[vA2], %[rA], s40 offen offset:2048 lds\n\tbuffer_load_dwordx4 %[vA3], %[rA], s40 offen offset:3072 lds\n\ts_add_u32 m0, %[lds0], 67584\n\ts_nop 0\n\tbuffer_load_dwordx4 %[vB0], %[rB], s41 offen offset:0 lds\n\tbuffer_load_dwordx4 %[vB1], %[rB], s41 offen offset:1024 lds\n\ts_add_u32 s40, s40, 1024\n\ts_add_u32 s41, s41, 65536\n\ts_waitcnt vmcnt(12)\n\ts_barrier\n\ts_add_u32 m0, %[lds0], 77824\n\ts_nop 0\n\tbuffer_load_dwordx4 %[vA0], %[rA], s40 offen offset:0 lds\n\tbuffer_load_dwordx4 %[vA1], %[rA], s40 offen offset:1024 lds\n\tbuffer_load_dwordx4 %[vA2], %[rA], s40 offen offset:2048 lds\n\tbuffer_load_dwordx4 %[vA3], %[rA], s40 offen offset:3072 lds\n\ts_add_u32 m0, %[lds0], 92160\n\ts_nop 0\n\tbuffer_load_dwordx4 %[vB0], %[rB], s41 offen offset:0 lds\n\tbuffer_load_dwordx4 %[vB1], %[rB], s41 offen offset:1024 lds\n\ts_add_u32 s40, s40, 1024\n\ts_add_u32 s41, s41, 65536\n\ts_add_u32 m0, %[lds0], 102400\n\ts_nop 0\n\tbuffer_load_dwordx4 %[vA0], %[rA], s40 offen offset:0 lds\n\tbuffer_load_dwordx4 %[vA1], %[rA], s40 offen offset:1024 lds\n\tbuffer_load_dwordx4 %[vA2], %[rA], s40 offen offset:2048 lds\n\tbuffer_load_dwordx4 %[vA3], %[rA], s40 offen offset:3072 lds\n\ts_add_u32 m0, %[lds0], 116736\n\ts_nop 0\n\tbuffer_load_dwordx4 %[vB0], %[rB], s41 offen offset:0 lds\n\tbuffer_load_dwordx4 %[vB1], %[rB], s41 offen offset:1024 lds\n\ts_add_u32 s40, s40, 1024\n\ts_add_u32 s41, s41, 65536\n\ts_waitcnt vmcnt(12)\n\ts_barrier\n\ts_add_u32 m0, %[lds0], 126976\n\ts_nop 0\n\tbuffer_load_dwordx4 %[vA0], %[rA], s40 offen offset:0 lds\n\tbuffer_load_dwordx4 %[vA1], %[rA], s40 offen offset:1024 lds\n\tbuffer_load_dwordx4 %[vA2], %[rA], s40 offen offset:2048 lds\n\tbuffer_load_dwordx4 %[vA3], %[rA], s40 offen offset:3072 lds\n\ts_add_u32 m0, %[lds0], 141312\n\ts_nop 0\n\tbuffer_load_dwordx4 %[vB0], %[rB], s41 offen offset:0 lds\n\tbuffer_load_dwordx4 %[vB1], %[rB], s41 offen offset:1024 lds\n\ts_add_u32 s40, s40, 1024\n\ts_add_u32 s41, s41, 65536\n\ts_add_u32 m0, %[lds0], 4096\n\ts_nop 0\n\tbuffer_load_dwordx4 %[vA0], %[rA], s40 offen offset:0 lds\n\tbuffer_load_dwordx4 %[vA1], %[rA], s40 offen offset:1024 lds\n\tbuffer_load_dwordx4 %[vA2], %[rA], s40 offen offset:2048 lds\n\tbuffer_load_dwordx4 %[vA3], %[rA], s40 offen offset:3072 lds\n\ts_add_u32 m0, %[lds0], 18432\n\ts_nop 0\n\tbuffer_load_dwordx4 %[vB0], %[rB], s41 offen offset:0 lds\n\tbuffer_load_dwordx4 %[vB1], %[rB], s41 offen offset:1024 lds\n\ts_add_u32 s40, s40, 1024\n\ts_add_u32 s41, s41, 65536\n\ts_sub_u32 s66, s66, 1\n\ts_cmp_lg_u32 s66, 0\n\ts_cbranch_scc1 L21_l0_%=\n\ts_waitcnt vmcnt(12)\n\ts_barrier\n\ts_add_u32 m0, %[lds0], 28672\n\ts_nop 0\n\tbuffer_load_dwordx4 %[vA0], %[rA], s40 offen offset:0 lds\n\tbuffer_load_dwordx4 %[vA1], %[rA], s40 offen offset:1024 lds\n\tbuffer_load_dwordx4 %[vA2], %[rA], s40 offen offset:2048 lds\n\tbuffer_load_dwordx4 %[vA3], %[rA], s40 offen offset:3072 lds\n\ts_add_u32 m0, %[lds0], 43008\n\ts_nop 0\n\tbuffer_load_dwordx4 %[vB0], %[rB], s41 offen offset:0 lds\n\tbuffer_load_dwordx4 %[vB1], %[rB], s41 offen offset:1024 lds\n\ts_add_u32 s40, s40, 1024\n\ts_add_u32 s41, s41, 65536\n\ts_add_u32 m0, %[lds0], 53248\n\ts_nop 0\n\tbuffer_load_dwordx4 %[vA0], %[rA], s40 offen offset:0 lds\n\tbuffer_load_dwordx4 %[vA1], %[rA], s40 offen offset:1024 lds\n\tbuffer_load_dwordx4 %[vA2], %[rA], s40 offen offset:2048 lds\n\tbuffer_load_dwordx4 %[vA3], %[rA], s40 offen offset:3072 lds\n\ts_add_u32 m0, %[lds0], 67584\n\ts_nop 0\n\tbuffer_load_dwordx4 %[vB0], %[rB], s41 offen offset:0 lds\n\tbuffer_load_dwordx4 %[vB1], %[rB], s41 offen offset:1024 lds\n\ts_add_u32 s40, s40, 1024\n\ts_add_u32 s41, s41, 65536\n\ts_waitcnt vmcnt(12)\n\ts_barrier\n\ts_add_u32 m0, %[lds0], 77824\n\ts_nop 0\n\tbuffer_load_dwordx4 %[vA0], %[rA], s40 offen offset:0 lds\n\tbuffer_load_dwordx4 %[vA1], %[rA], s40 offen offset:1024 lds\n\tbuffer_load_dwordx4 %[vA2], %[rA], s40 offen offset:2048 lds\n\tbuffer_load_dwordx4 %[vA3], %[rA], s40 offen offset:3072 lds\n\ts_add_u32 m0, %[lds0], 92160\n\ts_nop 0\n\tbuffer_load_dwordx4 %[vB0], %[rB], s41 offen offset:0 lds\n\tbuffer_load_dwordx4 %[vB1], %[rB], s41 offen offset:1024 lds\n\ts_add_u32 s40, s40, 1024\n\ts_add_u32 s41, s41, 65536\n\ts_mov_b32 s40, %[oO]\n\ts_mov_b32 s41, %[oW]\n\ts_add_u32 m0, %[lds0], 102400\n\ts_nop 0\n\tbuffer_load_dwordx4 %[vO0], %[rO], s40 offen offset:0 lds\n\tbuffer_load_dwordx4 %[vO1], %[rO], s40 offen offset:1024 lds\n\tbuffer_load_dwordx4 %[vO2], %[rO], s40 offen offset:2048 lds\n\tbuffer_load_dwordx4 %[vO3], %[rO], s40 offen offset:3072 lds\n\ts_add_u32 m0, %[lds0], 116736\n\ts_nop 0\n\tbuffer_load_dwordx4 %[vB0], %[rW], s41 offen offset:0 lds\n\tbuffer_load_dwordx4 %[vB1], %[rW], s41 offen offset:1024 lds\n\ts_add_u32 s40, s40, 1024\n\ts_add_u32 s41, s41, 65536\n\ts_waitcnt vmcnt(12)\n\ts_barrier\n\ts_add_u32 m0, %[lds0], 126976\n\ts_nop 0\n\tbuffer_load_dwordx4 %[vO0], %[rO], s40 offen offset:0 lds\n\tbuffer_load_dwordx4 %[vO1], %[rO], s40 offen offset:1024 lds\n\tbuffer_load_dwordx4 %[vO2], %[rO], s40 offen offset:2048 lds\n\tbuffer_load_dwordx4 %[vO3], %[rO], s40 offen offset:3072 lds\n\ts_add_u32 m0, %[lds0], 141312\n\ts_nop 0\n\tbuffer_load_dwordx4 %[vB0], %[rW], s41 offen offset:0 lds\n\tbuffer_load_dwordx4 %[vB1], %[rW], s41 offen offset:1024 lds\n\ts_add_u32 s40, s40, 1024\n\ts_add_u32 s41, s41, 65536\n\ts_add_u32 m0, %[lds0], 4096\n\ts_nop 0\n\tbuffer_load_dwordx4 %[vO0], %[rO], s40 offen offset:0 lds\n\tbuffer_load_dwordx4 %[vO1], %[rO], s40 offen offset:1024 lds\n\tbuffer_load_dwordx4 %[vO2], %[rO], s40 offen offset:2048 lds\n\tbuffer_load_dwordx4 %[vO3], %[rO], s40 offen offset:3072 lds\n\ts_add_u32 m0, %[lds0], 18432\n\ts_nop 0\n\tbuffer_load_dwordx4 %[vB0], %[rW], s41 offen offset:0 lds\n\tbuffer_load_dwordx4 %[vB1], %[rW], s41 offen offset:1024 lds\n\ts_add_u32 s40, s40, 1024\n\ts_add_u32 s41, s41, 65536\n\ts_waitcnt vmcnt(12)\n\ts_barrier\n\ts_add_u32 m0, %[lds0], 28672\n\ts_nop 0\n\tbuffer_load_dwordx4 %[vO0], %[rO], s40 offen offset:0 lds\n\tbuffer_load_dwordx4 %[vO1], %[rO], s40 offen offset:1024 lds\n\tbuffer_load_dwordx4 %[vO2], %[rO], s40 offen offset:2048 lds\n\tbuffer_load_dwordx4 %[vO3], %[rO], s40 offen offset:3072 lds\n\ts_add_u32 m0, %[lds0], 43008\n\ts_nop 0\n\tbuffer_load_dwordx4 %[vB0], %[rW], s41 offen offset:0 lds\n\tbuffer_load_dwordx4 %[vB1], %[rW], s41 offen offset:1024 lds\n\ts_add_u32 s40, s40, 1024\n\ts_add_u32 s41, s41, 65536\n\ts_add_u32 m0, %[lds0], 53248\n\ts_nop 0\n\tbuffer_load_dwordx4 %[vO0], %[rO], s40 offen offset:0 lds\n\tbuffer_load_dwordx4 %[vO1], %[rO], s40 offen offset:1024 lds\n\tbuffer_load_dwordx4 %[vO2], %[rO], s40 offen offset:2048 lds\n\tbuffer_load_dwordx4 %[vO3], %[rO], s40 offen offset:3072 lds\n\ts_add_u32 m0, %[lds0], 67584\n\ts_nop 0\n\tbuffer_load_dwordx4 %[vB0], %[rW], s41 offen offset:0 lds\n\tbuffer_load_dwordx4 %[vB1], %[rW], s41 offen offset:1024 lds\n\ts_add_u32 s40, s40, 1024\n\ts_add_u32 s41, s41, 65536\n\ts_waitcnt vmcnt(12)\n\ts_barrier\n\ts_add_u32 m0, %[lds0], 77824\n\ts_nop 0\n\tbuffer_load_dwordx4 %[vO0], %[rO], s40 offen offset:0 lds\n\tbuffer_load_dwordx4 %[vO1], %[rO], s40 offen offset:1024 lds\n\tbuffer_load_dwordx4 %[vO2], %[rO], s40 offen offset:2048 lds\n\tbuffer_load_dwordx4 %[vO3], %[rO], s40 offen offset:3072 lds\n\ts_add_u32 m0, %[lds0], 92160\n\ts_nop 0\n\tbuffer_load_dwordx4 %[vB0], %[rW], s41 offen offset:0 lds\n\tbuffer_load_dwordx4 %[vB1], %[rW], s41 offen offset:1024 lds\n\ts_add_u32 s40, s40, 1024\n\ts_add_u32 s41, s41, 65536\n\ts_add_u32 m0, %[lds0], 102400\n\ts_nop 0\n\tbuffer_load_dwordx4 %[vO0], %[rO], s40 offen offset:0 lds\n\tbuffer_load_dwordx4 %[vO1], %[rO], s40 offen offset:1024 lds\n\tbuffer_load_dwordx4 %[vO2], %[rO], s40 offen offset:2048 lds\n\tbuffer_load_dwordx4 %[vO3], %[rO], s40 offen offset:3072 lds\n\ts_add_u32 m0, %[lds0], 116736\n\ts_nop 0\n\tbuffer_load_dwordx4 %[vB0], %[rW], s41 offen offset:0 lds\n\tbuffer_load_dwordx4 %[vB1], %[rW], s41 offen offset:1024 lds\n\ts_add_u32 s40, s40, 1024\n\ts_add_u32 s41, s41, 65536\n\ts_waitcnt vmcnt(12)\n\ts_barrier\n\ts_add_u32 m0, %[lds0], 126976\n\ts_nop 0\n\tbuffer_load_dwordx4 %[vO0], %[rO], s40 offen offset:0 lds\n\tbuffer_load_dwordx4 %[vO1], %[rO], s40 offen offset:1024 lds\n\tbuffer_load_dwordx4 %[vO2], %[rO], s40 offen offset:2048 lds\n\tbuffer_load_dwordx4 %[vO3], %[rO], s40 offen offset:3072 lds\n\ts_add_u32 m0, %[lds0], 141312\n\ts_nop 0\n\tbuffer_load_dwordx4 %[vB0], %[rW], s41 offen offset:0 lds\n\tbuffer_load_dwordx4 %[vB1], %[rW], s41 offen offset:1024 lds\n\ts_add_u32 s40, s40, 1024\n\ts_add_u32 s41, s41, 65536\n\ts_waitcnt vmcnt(6)\n\ts_barrier\n\ts_waitcnt vmcnt(0)\n\ts_barrier"
#define L2_1_CLOB "s66","s40","s41","s70","s71","v43","v44","v52","v53","v54","v55","v56","v57","v58","v59","v60","v61","v62","v63","v64","v65","v66","v67"
#define L2_2_ASM "s_mov_b32 s40, %[oA]\n\ts_mov_b32 s41, %[oB]\n\ts_add_u32 m0, %[lds0], 8192\n\ts_nop 0\n\tbuffer_load_dwordx4 %[vA0], %[rA], s40 offen offset:0 lds\n\tbuffer_load_dwordx4 %[vA1], %[rA], s40 offen offset:1024 lds\n\tbuffer_load_dwordx4 %[vA2], %[rA], s40 offen offset:2048 lds\n\tbuffer_load_dwordx4 %[vA3], %[rA], s40 offen offset:3072 lds\n\ts_add_u32 m0, %[lds0], 20480\n\ts_nop 0\n\tbuffer_load_dwordx4 %[vB0], %[rB], s41 offen offset:0 lds\n\tbuffer_load_dwordx4 %[vB1], %[rB], s41 offen offset:1024 lds\n\ts_add_u32 s40, s40, 1024\n\ts_add_u32 s41, s41, 65536\n\ts_add_u32 m0, %[lds0], 32768\n\ts_nop 0\n\tbuffer_load_dwordx4 %[vA0], %[rA], s40 offen offset:0 lds\n\tbuffer_load_dwordx4 %[vA1], %[rA], s40 offen offset:1024 lds\n\tbuffer_load_dwordx4 %[vA2], %[rA], s40 offen offset:2048 lds\n\tbuffer_load_dwordx4 %[vA3], %[rA], s40 offen offset:3072 lds\n\ts_add_u32 m0, %[lds0], 45056\n\ts_nop 0\n\tbuffer_load_dwordx4 %[vB0], %[rB], s41 offen offset:0 lds\n\tbuffer_load_dwordx4 %[vB1], %[rB], s41 offen offset:1024 lds\n\ts_add_u32 s40, s40, 1024\n\ts_add_u32 s41, s41, 65536\n\ts_add_u32 m0, %[lds0], 57344\n\ts_nop 0\n\tbuffer_load_dwordx4 %[vA0], %[rA], s40 offen offset:0 lds\n\tbuffer_load_dwordx4 %[vA1], %[rA], s40 offen offset:1024 lds\n\tbuffer_load_dwordx4 %[vA2], %[rA], s40 offen offset:2048 lds\n\tbuffer_load_dwordx4 %[vA3], %[rA], s40 offen offset:3072 lds\n\ts_add_u32 m0, %[lds0], 69632\n\ts_nop 0\n\tbuffer_load_dwordx4 %[vB0], %[rB], s41 offen offset:0 lds\n\tbuffer_load_dwordx4 %[vB1], %[rB], s41 offen offset:1024 lds\n\ts_add_u32 s40, s40, 1024\n\ts_add_u32 s41, s41, 65536\n\ts_add_u32 m0, %[lds0], 81920\n\ts_nop 0\n\tbuffer_load_dwordx4 %[vA0], %[rA], s40 offen offset:0 lds\n\tbuffer_load_dwordx4 %[vA1], %[rA], s40 offen offset:1024 lds\n\tbuffer_load_dwordx4 %[vA2], %[rA], s40 offen offset:2048 lds\n\tbuffer_load_dwordx4 %[vA3], %[rA], s40 offen offset:3072 lds\n\ts_add_u32 m0, %[lds0], 94208\n\ts_nop 0\n\tbuffer_load_dwordx4 %[vB0], %[rB], s41 offen offset:0 lds\n\tbuffer_load_dwordx4 %[vB1], %[rB], s41 offen offset:1024 lds\n\ts_add_u32 s40, s40, 1024\n\ts_add_u32 s41, s41, 65536\n\ts_waitcnt vmcnt(18)\n\ts_barrier\n\tds_read_b128 v[52:55], %[os] offset:0\n\tds_read_b128 v[56:59], %[os] offset:1024\n\tds_read_b128 v[60:63], %[os] offset:2048\n\tds_read_b128 v[64:67], %[os] offset:3072\n\ts_waitcnt lgkmcnt(0)\n\ts_waitcnt vmcnt(6)\n\ts_barrier\n\ts_add_u32 m0, %[lds0], 106496\n\ts_nop 0\n\tbuffer_load_dwordx4 %[vA0], %[rA], s40 offen offset:0 lds\n\tbuffer_load_dwordx4 %[vA1], %[rA], s40 offen offset:1024 lds\n\tbuffer_load_dwordx4 %[vA2], %[rA], s40 offen offset:2048 lds\n\tbuffer_load_dwordx4 %[vA3], %[rA], s40 offen offset:3072 lds\n\ts_add_u32 m0, %[lds0], 118784\n\ts_nop 0\n\tbuffer_load_dwordx4 %[vB0], %[rB], s41 offen offset:0 lds\n\tbuffer_load_dwordx4 %[vB1], %[rB], s41 offen offset:1024 lds\n\ts_add_u32 s40, s40, 1024\n\ts_add_u32 s41, s41, 65536\n\ts_add_u32 m0, %[lds0], 131072\n\ts_nop 0\n\tbuffer_load_dwordx4 %[vA0], %[rA], s40 offen offset:0 lds\n\tbuffer_load_dwordx4 %[vA1], %[rA], s40 offen offset:1024 lds\n\tbuffer_load_dwordx4 %[vA2], %[rA], s40 offen offset:2048 lds\n\tbuffer_load_dwordx4 %[vA3], %[rA], s40 offen offset:3072 lds\n\ts_add_u32 m0, %[lds0], 143360\n\ts_nop 0\n\tbuffer_load_dwordx4 %[vB0], %[rB], s41 offen offset:0 lds\n\tbuffer_load_dwordx4 %[vB1], %[rB], s41 offen offset:1024 lds\n\ts_add_u32 s40, s40, 1024\n\ts_add_u32 s41, s41, 65536\n\ts_mov_b32 s70, 0\n\tbuffer_store_dwordx4 v[52:55], %[ooff], %[ro], s70 offen sc1\n\ts_mov_b32 s70, 512\n\tbuffer_store_dwordx4 v[56:59], %[ooff], %[ro], s70 offen sc1\n\ts_mov_b32 s70, 65536\n\tbuffer_store_dwordx4 v[60:63], %[ooff], %[ro], s70 offen sc1\n\ts_mov_b32 s70, 66048\n\tbuffer_store_dwordx4 v[64:67], %[ooff], %[ro], s70 offen sc1\n\ts_add_u32 m0, %[lds0], 8192\n\ts_nop 0\n\tbuffer_load_dwordx4 %[vA0], %[rA], s40 offen offset:0 lds\n\tbuffer_load_dwordx4 %[vA1], %[rA], s40 offen offset:1024 lds\n\tbuffer_load_dwordx4 %[vA2], %[rA], s40 offen offset:2048 lds\n\tbuffer_load_dwordx4 %[vA3], %[rA], s40 offen offset:3072 lds\n\ts_add_u32 m0, %[lds0], 20480\n\ts_nop 0\n\tbuffer_load_dwordx4 %[vB0], %[rB], s41 offen offset:0 lds\n\tbuffer_load_dwordx4 %[vB1], %[rB], s41 offen offset:1024 lds\n\ts_add_u32 s40, s40, 1024\n\ts_add_u32 s41, s41, 65536\n\ts_mov_b32 s66, 3\n\tL22_l0_%=:\n\ts_waitcnt vmcnt(12)\n\ts_barrier\n\ts_add_u32 m0, %[lds0], 32768\n\ts_nop 0\n\tbuffer_load_dwordx4 %[vA0], %[rA], s40 offen offset:0 lds\n\tbuffer_load_dwordx4 %[vA1], %[rA], s40 offen offset:1024 lds\n\tbuffer_load_dwordx4 %[vA2], %[rA], s40 offen offset:2048 lds\n\tbuffer_load_dwordx4 %[vA3], %[rA], s40 offen offset:3072 lds\n\ts_add_u32 m0, %[lds0], 45056\n\ts_nop 0\n\tbuffer_load_dwordx4 %[vB0], %[rB], s41 offen offset:0 lds\n\tbuffer_load_dwordx4 %[vB1], %[rB], s41 offen offset:1024 lds\n\ts_add_u32 s40, s40, 1024\n\ts_add_u32 s41, s41, 65536\n\ts_add_u32 m0, %[lds0], 57344\n\ts_nop 0\n\tbuffer_load_dwordx4 %[vA0], %[rA], s40 offen offset:0 lds\n\tbuffer_load_dwordx4 %[vA1], %[rA], s40 offen offset:1024 lds\n\tbuffer_load_dwordx4 %[vA2], %[rA], s40 offen offset:2048 lds\n\tbuffer_load_dwordx4 %[vA3], %[rA], s40 offen offset:3072 lds\n\ts_add_u32 m0, %[lds0], 69632\n\ts_nop 0\n\tbuffer_load_dwordx4 %[vB0], %[rB], s41 offen offset:0 lds\n\tbuffer_load_dwordx4 %[vB1], %[rB], s41 offen offset:1024 lds\n\ts_add_u32 s40, s40, 1024\n\ts_add_u32 s41, s41, 65536\n\ts_waitcnt vmcnt(12)\n\ts_barrier\n\ts_add_u32 m0, %[lds0], 81920\n\ts_nop 0\n\tbuffer_load_dwordx4 %[vA0], %[rA], s40 offen offset:0 lds\n\tbuffer_load_dwordx4 %[vA1], %[rA], s40 offen offset:1024 lds\n\tbuffer_load_dwordx4 %[vA2], %[rA], s40 offen offset:2048 lds\n\tbuffer_load_dwordx4 %[vA3], %[rA], s40 offen offset:3072 lds\n\ts_add_u32 m0, %[lds0], 94208\n\ts_nop 0\n\tbuffer_load_dwordx4 %[vB0], %[rB], s41 offen offset:0 lds\n\tbuffer_load_dwordx4 %[vB1], %[rB], s41 offen offset:1024 lds\n\ts_add_u32 s40, s40, 1024\n\ts_add_u32 s41, s41, 65536\n\ts_add_u32 m0, %[lds0], 106496\n\ts_nop 0\n\tbuffer_load_dwordx4 %[vA0], %[rA], s40 offen offset:0 lds\n\tbuffer_load_dwordx4 %[vA1], %[rA], s40 offen offset:1024 lds\n\tbuffer_load_dwordx4 %[vA2], %[rA], s40 offen offset:2048 lds\n\tbuffer_load_dwordx4 %[vA3], %[rA], s40 offen offset:3072 lds\n\ts_add_u32 m0, %[lds0], 118784\n\ts_nop 0\n\tbuffer_load_dwordx4 %[vB0], %[rB], s41 offen offset:0 lds\n\tbuffer_load_dwordx4 %[vB1], %[rB], s41 offen offset:1024 lds\n\ts_add_u32 s40, s40, 1024\n\ts_add_u32 s41, s41, 65536\n\ts_waitcnt vmcnt(12)\n\ts_barrier\n\ts_add_u32 m0, %[lds0], 131072\n\ts_nop 0\n\tbuffer_load_dwordx4 %[vA0], %[rA], s40 offen offset:0 lds\n\tbuffer_load_dwordx4 %[vA1], %[rA], s40 offen offset:1024 lds\n\tbuffer_load_dwordx4 %[vA2], %[rA], s40 offen offset:2048 lds\n\tbuffer_load_dwordx4 %[vA3], %[rA], s40 offen offset:3072 lds\n\ts_add_u32 m0, %[lds0], 143360\n\ts_nop 0\n\tbuffer_load_dwordx4 %[vB0], %[rB], s41 offen offset:0 lds\n\tbuffer_load_dwordx4 %[vB1], %[rB], s41 offen offset:1024 lds\n\ts_add_u32 s40, s40, 1024\n\ts_add_u32 s41, s41, 65536\n\ts_add_u32 m0, %[lds0], 8192\n\ts_nop 0\n\tbuffer_load_dwordx4 %[vA0], %[rA], s40 offen offset:0 lds\n\tbuffer_load_dwordx4 %[vA1], %[rA], s40 offen offset:1024 lds\n\tbuffer_load_dwordx4 %[vA2], %[rA], s40 offen offset:2048 lds\n\tbuffer_load_dwordx4 %[vA3], %[rA], s40 offen offset:3072 lds\n\ts_add_u32 m0, %[lds0], 20480\n\ts_nop 0\n\tbuffer_load_dwordx4 %[vB0], %[rB], s41 offen offset:0 lds\n\tbuffer_load_dwordx4 %[vB1], %[rB], s41 offen offset:1024 lds\n\ts_add_u32 s40, s40, 1024\n\ts_add_u32 s41, s41, 65536\n\ts_sub_u32 s66, s66, 1\n\ts_cmp_lg_u32 s66, 0\n\ts_cbranch_scc1 L22_l0_%=\n\ts_waitcnt vmcnt(12)\n\ts_barrier\n\ts_add_u32 m0, %[lds0], 32768\n\ts_nop 0\n\tbuffer_load_dwordx4 %[vA0], %[rA], s40 offen offset:0 lds\n\tbuffer_load_dwordx4 %[vA1], %[rA], s40 offen offset:1024 lds\n\tbuffer_load_dwordx4 %[vA2], %[rA], s40 offen offset:2048 lds\n\tbuffer_load_dwordx4 %[vA3], %[rA], s40 offen offset:3072 lds\n\ts_add_u32 m0, %[lds0], 45056\n\ts_nop 0\n\tbuffer_load_dwordx4 %[vB0], %[rB], s41 offen offset:0 lds\n\tbuffer_load_dwordx4 %[vB1], %[rB], s41 offen offset:1024 lds\n\ts_add_u32 s40, s40, 1024\n\ts_add_u32 s41, s41, 65536\n\ts_add_u32 m0, %[lds0], 57344\n\ts_nop 0\n\tbuffer_load_dwordx4 %[vA0], %[rA], s40 offen offset:0 lds\n\tbuffer_load_dwordx4 %[vA1], %[rA], s40 offen offset:1024 lds\n\tbuffer_load_dwordx4 %[vA2], %[rA], s40 offen offset:2048 lds\n\tbuffer_load_dwordx4 %[vA3], %[rA], s40 offen offset:3072 lds\n\ts_add_u32 m0, %[lds0], 69632\n\ts_nop 0\n\tbuffer_load_dwordx4 %[vB0], %[rB], s41 offen offset:0 lds\n\tbuffer_load_dwordx4 %[vB1], %[rB], s41 offen offset:1024 lds\n\ts_add_u32 s40, s40, 1024\n\ts_add_u32 s41, s41, 65536\n\ts_waitcnt vmcnt(12)\n\ts_barrier\n\ts_add_u32 m0, %[lds0], 81920\n\ts_nop 0\n\tbuffer_load_dwordx4 %[vA0], %[rA], s40 offen offset:0 lds\n\tbuffer_load_dwordx4 %[vA1], %[rA], s40 offen offset:1024 lds\n\tbuffer_load_dwordx4 %[vA2], %[rA], s40 offen offset:2048 lds\n\tbuffer_load_dwordx4 %[vA3], %[rA], s40 offen offset:3072 lds\n\ts_add_u32 m0, %[lds0], 94208\n\ts_nop 0\n\tbuffer_load_dwordx4 %[vB0], %[rB], s41 offen offset:0 lds\n\tbuffer_load_dwordx4 %[vB1], %[rB], s41 offen offset:1024 lds\n\ts_add_u32 s40, s40, 1024\n\ts_add_u32 s41, s41, 65536\n\ts_mov_b32 s40, %[oO]\n\ts_mov_b32 s41, %[oW]\n\ts_add_u32 m0, %[lds0], 106496\n\ts_nop 0\n\tbuffer_load_dwordx4 %[vO0], %[rO], s40 offen offset:0 lds\n\tbuffer_load_dwordx4 %[vO1], %[rO], s40 offen offset:1024 lds\n\tbuffer_load_dwordx4 %[vO2], %[rO], s40 offen offset:2048 lds\n\tbuffer_load_dwordx4 %[vO3], %[rO], s40 offen offset:3072 lds\n\ts_add_u32 m0, %[lds0], 118784\n\ts_nop 0\n\tbuffer_load_dwordx4 %[vB0], %[rW], s41 offen offset:0 lds\n\tbuffer_load_dwordx4 %[vB1], %[rW], s41 offen offset:1024 lds\n\ts_add_u32 s40, s40, 1024\n\ts_add_u32 s41, s41, 65536\n\ts_waitcnt vmcnt(12)\n\ts_barrier\n\ts_add_u32 m0, %[lds0], 131072\n\ts_nop 0\n\tbuffer_load_dwordx4 %[vO0], %[rO], s40 offen offset:0 lds\n\tbuffer_load_dwordx4 %[vO1], %[rO], s40 offen offset:1024 lds\n\tbuffer_load_dwordx4 %[vO2], %[rO], s40 offen offset:2048 lds\n\tbuffer_load_dwordx4 %[vO3], %[rO], s40 offen offset:3072 lds\n\ts_add_u32 m0, %[lds0], 143360\n\ts_nop 0\n\tbuffer_load_dwordx4 %[vB0], %[rW], s41 offen offset:0 lds\n\tbuffer_load_dwordx4 %[vB1], %[rW], s41 offen offset:1024 lds\n\ts_add_u32 s40, s40, 1024\n\ts_add_u32 s41, s41, 65536\n\ts_add_u32 m0, %[lds0], 8192\n\ts_nop 0\n\tbuffer_load_dwordx4 %[vO0], %[rO], s40 offen offset:0 lds\n\tbuffer_load_dwordx4 %[vO1], %[rO], s40 offen offset:1024 lds\n\tbuffer_load_dwordx4 %[vO2], %[rO], s40 offen offset:2048 lds\n\tbuffer_load_dwordx4 %[vO3], %[rO], s40 offen offset:3072 lds\n\ts_add_u32 m0, %[lds0], 20480\n\ts_nop 0\n\tbuffer_load_dwordx4 %[vB0], %[rW], s41 offen offset:0 lds\n\tbuffer_load_dwordx4 %[vB1], %[rW], s41 offen offset:1024 lds\n\ts_add_u32 s40, s40, 1024\n\ts_add_u32 s41, s41, 65536\n\ts_waitcnt vmcnt(12)\n\ts_barrier\n\ts_add_u32 m0, %[lds0], 32768\n\ts_nop 0\n\tbuffer_load_dwordx4 %[vO0], %[rO], s40 offen offset:0 lds\n\tbuffer_load_dwordx4 %[vO1], %[rO], s40 offen offset:1024 lds\n\tbuffer_load_dwordx4 %[vO2], %[rO], s40 offen offset:2048 lds\n\tbuffer_load_dwordx4 %[vO3], %[rO], s40 offen offset:3072 lds\n\ts_add_u32 m0, %[lds0], 45056\n\ts_nop 0\n\tbuffer_load_dwordx4 %[vB0], %[rW], s41 offen offset:0 lds\n\tbuffer_load_dwordx4 %[vB1], %[rW], s41 offen offset:1024 lds\n\ts_add_u32 s40, s40, 1024\n\ts_add_u32 s41, s41, 65536\n\ts_add_u32 m0, %[lds0], 57344\n\ts_nop 0\n\tbuffer_load_dwordx4 %[vO0], %[rO], s40 offen offset:0 lds\n\tbuffer_load_dwordx4 %[vO1], %[rO], s40 offen offset:1024 lds\n\tbuffer_load_dwordx4 %[vO2], %[rO], s40 offen offset:2048 lds\n\tbuffer_load_dwordx4 %[vO3], %[rO], s40 offen offset:3072 lds\n\ts_add_u32 m0, %[lds0], 69632\n\ts_nop 0\n\tbuffer_load_dwordx4 %[vB0], %[rW], s41 offen offset:0 lds\n\tbuffer_load_dwordx4 %[vB1], %[rW], s41 offen offset:1024 lds\n\ts_add_u32 s40, s40, 1024\n\ts_add_u32 s41, s41, 65536\n\ts_waitcnt vmcnt(12)\n\ts_barrier\n\ts_add_u32 m0, %[lds0], 81920\n\ts_nop 0\n\tbuffer_load_dwordx4 %[vO0], %[rO], s40 offen offset:0 lds\n\tbuffer_load_dwordx4 %[vO1], %[rO], s40 offen offset:1024 lds\n\tbuffer_load_dwordx4 %[vO2], %[rO], s40 offen offset:2048 lds\n\tbuffer_load_dwordx4 %[vO3], %[rO], s40 offen offset:3072 lds\n\ts_add_u32 m0, %[lds0], 94208\n\ts_nop 0\n\tbuffer_load_dwordx4 %[vB0], %[rW], s41 offen offset:0 lds\n\tbuffer_load_dwordx4 %[vB1], %[rW], s41 offen offset:1024 lds\n\ts_add_u32 s40, s40, 1024\n\ts_add_u32 s41, s41, 65536\n\ts_add_u32 m0, %[lds0], 106496\n\ts_nop 0\n\tbuffer_load_dwordx4 %[vO0], %[rO], s40 offen offset:0 lds\n\tbuffer_load_dwordx4 %[vO1], %[rO], s40 offen offset:1024 lds\n\tbuffer_load_dwordx4 %[vO2], %[rO], s40 offen offset:2048 lds\n\tbuffer_load_dwordx4 %[vO3], %[rO], s40 offen offset:3072 lds\n\ts_add_u32 m0, %[lds0], 118784\n\ts_nop 0\n\tbuffer_load_dwordx4 %[vB0], %[rW], s41 offen offset:0 lds\n\tbuffer_load_dwordx4 %[vB1], %[rW], s41 offen offset:1024 lds\n\ts_add_u32 s40, s40, 1024\n\ts_add_u32 s41, s41, 65536\n\ts_waitcnt vmcnt(12)\n\ts_barrier\n\ts_add_u32 m0, %[lds0], 131072\n\ts_nop 0\n\tbuffer_load_dwordx4 %[vO0], %[rO], s40 offen offset:0 lds\n\tbuffer_load_dwordx4 %[vO1], %[rO], s40 offen offset:1024 lds\n\tbuffer_load_dwordx4 %[vO2], %[rO], s40 offen offset:2048 lds\n\tbuffer_load_dwordx4 %[vO3], %[rO], s40 offen offset:3072 lds\n\ts_add_u32 m0, %[lds0], 143360\n\ts_nop 0\n\tbuffer_load_dwordx4 %[vB0], %[rW], s41 offen offset:0 lds\n\tbuffer_load_dwordx4 %[vB1], %[rW], s41 offen offset:1024 lds\n\ts_add_u32 s40, s40, 1024\n\ts_add_u32 s41, s41, 65536\n\ts_waitcnt vmcnt(6)\n\ts_barrier\n\ts_waitcnt vmcnt(0)\n\ts_barrier"
#define L2_2_CLOB "s66","s40","s41","s70","s71","v43","v44","v52","v53","v54","v55","v56","v57","v58","v59","v60","v61","v62","v63","v64","v65","v66","v67"
#define L2_3_ASM "s_mov_b32 s40, %[oA]\n\ts_mov_b32 s41, %[oB]\n\ts_add_u32 m0, %[lds0], 12288\n\ts_nop 0\n\tbuffer_load_dwordx4 %[vA0], %[rA], s40 offen offset:0 lds\n\tbuffer_load_dwordx4 %[vA1], %[rA], s40 offen offset:1024 lds\n\tbuffer_load_dwordx4 %[vA2], %[rA], s40 offen offset:2048 lds\n\tbuffer_load_dwordx4 %[vA3], %[rA], s40 offen offset:3072 lds\n\ts_add_u32 m0, %[lds0], 22528\n\ts_nop 0\n\tbuffer_load_dwordx4 %[vB0], %[rB], s41 offen offset:0 lds\n\tbuffer_load_dwordx4 %[vB1], %[rB], s41 offen offset:1024 lds\n\ts_add_u32 s40, s40, 1024\n\ts_add_u32 s41, s41, 65536\n\ts_add_u32 m0, %[lds0], 36864\n\ts_nop 0\n\tbuffer_load_dwordx4 %[vA0], %[rA], s40 offen offset:0 lds\n\tbuffer_load_dwordx4 %[vA1], %[rA], s40 offen offset:1024 lds\n\tbuffer_load_dwordx4 %[vA2], %[rA], s40 offen offset:2048 lds\n\tbuffer_load_dwordx4 %[vA3], %[rA], s40 offen offset:3072 lds\n\ts_add_u32 m0, %[lds0], 47104\n\ts_nop 0\n\tbuffer_load_dwordx4 %[vB0], %[rB], s41 offen offset:0 lds\n\tbuffer_load_dwordx4 %[vB1], %[rB], s41 offen offset:1024 lds\n\ts_add_u32 s40, s40, 1024\n\ts_add_u32 s41, s41, 65536\n\ts_add_u32 m0, %[lds0], 61440\n\ts_nop 0\n\tbuffer_load_dwordx4 %[vA0], %[rA], s40 offen offset:0 lds\n\tbuffer_load_dwordx4 %[vA1], %[rA], s40 offen offset:1024 lds\n\tbuffer_load_dwordx4 %[vA2], %[rA], s40 offen offset:2048 lds\n\tbuffer_load_dwordx4 %[vA3], %[rA], s40 offen offset:3072 lds\n\ts_add_u32 m0, %[lds0], 71680\n\ts_nop 0\n\tbuffer_load_dwordx4 %[vB0], %[rB], s41 offen offset:0 lds\n\tbuffer_load_dwordx4 %[vB1], %[rB], s41 offen offset:1024 lds\n\ts_add_u32 s40, s40, 1024\n\ts_add_u32 s41, s41, 65536\n\ts_add_u32 m0, %[lds0], 86016\n\ts_nop 0\n\tbuffer_load_dwordx4 %[vA0], %[rA], s40 offen offset:0 lds\n\tbuffer_load_dwordx4 %[vA1], %[rA], s40 offen offset:1024 lds\n\tbuffer_load_dwordx4 %[vA2], %[rA], s40 offen offset:2048 lds\n\tbuffer_load_dwordx4 %[vA3], %[rA], s40 offen offset:3072 lds\n\ts_add_u32 m0, %[lds0], 96256\n\ts_nop 0\n\tbuffer_load_dwordx4 %[vB0], %[rB], s41 offen offset:0 lds\n\tbuffer_load_dwordx4 %[vB1], %[rB], s41 offen offset:1024 lds\n\ts_add_u32 s40, s40, 1024\n\ts_add_u32 s41, s41, 65536\n\ts_waitcnt vmcnt(18)\n\ts_barrier\n\tds_read_b128 v[52:55], %[os] offset:0\n\tds_read_b128 v[56:59], %[os] offset:1024\n\tds_read_b128 v[60:63], %[os] offset:2048\n\tds_read_b128 v[64:67], %[os] offset:3072\n\ts_waitcnt lgkmcnt(0)\n\ts_waitcnt vmcnt(6)\n\ts_barrier\n\ts_add_u32 m0, %[lds0], 110592\n\ts_nop 0\n\tbuffer_load_dwordx4 %[vA0], %[rA], s40 offen offset:0 lds\n\tbuffer_load_dwordx4 %[vA1], %[rA], s40 offen offset:1024 lds\n\tbuffer_load_dwordx4 %[vA2], %[rA], s40 offen offset:2048 lds\n\tbuffer_load_dwordx4 %[vA3], %[rA], s40 offen offset:3072 lds\n\ts_add_u32 m0, %[lds0], 120832\n\ts_nop 0\n\tbuffer_load_dwordx4 %[vB0], %[rB], s41 offen offset:0 lds\n\tbuffer_load_dwordx4 %[vB1], %[rB], s41 offen offset:1024 lds\n\ts_add_u32 s40, s40, 1024\n\ts_add_u32 s41, s41, 65536\n\ts_add_u32 m0, %[lds0], 135168\n\ts_nop 0\n\tbuffer_load_dwordx4 %[vA0], %[rA], s40 offen offset:0 lds\n\tbuffer_load_dwordx4 %[vA1], %[rA], s40 offen offset:1024 lds\n\tbuffer_load_dwordx4 %[vA2], %[rA], s40 offen offset:2048 lds\n\tbuffer_load_dwordx4 %[vA3], %[rA], s40 offen offset:3072 lds\n\ts_add_u32 m0, %[lds0], 145408\n\ts_nop 0\n\tbuffer_load_dwordx4 %[vB0], %[rB], s41 offen offset:0 lds\n\tbuffer_load_dwordx4 %[vB1], %[rB], s41 offen offset:1024 lds\n\ts_add_u32 s40, s40, 1024\n\ts_add_u32 s41, s41, 65536\n\ts_mov_b32 s70, 0\n\tbuffer_store_dwordx4 v[52:55], %[ooff], %[ro], s70 offen sc1\n\ts_mov_b32 s70, 512\n\tbuffer_store_dwordx4 v[56:59], %[ooff], %[ro], s70 offen sc1\n\ts_mov_b32 s70, 65536\n\tbuffer_store_dwordx4 v[60:63], %[ooff], %[ro], s70 offen sc1\n\ts_mov_b32 s70, 66048\n\tbuffer_store_dwordx4 v[64:67], %[ooff], %[ro], s70 offen sc1\n\ts_add_u32 m0, %[lds0], 12288\n\ts_nop 0\n\tbuffer_load_dwordx4 %[vA0], %[rA], s40 offen offset:0 lds\n\tbuffer_load_dwordx4 %[vA1], %[rA], s40 offen offset:1024 lds\n\tbuffer_load_dwordx4 %[vA2], %[rA], s40 offen offset:2048 lds\n\tbuffer_load_dwordx4 %[vA3], %[rA], s40 offen offset:3072 lds\n\ts_add_u32 m0, %[lds0], 22528\n\ts_nop 0\n\tbuffer_load_dwordx4 %[vB0], %[rB], s41 offen offset:0 lds\n\tbuffer_load_dwordx4 %[vB1], %[rB], s41 offen offset:1024 lds\n\ts_add_u32 s40, s40, 1024\n\ts_add_u32 s41, s41, 65536\n\ts_waitcnt vmcnt(12)\n\ts_barrier\n\ts_add_u32 m0, %[lds0], 36864\n\ts_nop 0\n\tbuffer_load_dwordx4 %[vA0], %[rA], s40 offen offset:0 lds\n\tbuffer_load_dwordx4 %[vA1], %[rA], s40 offen offset:1024 lds\n\tbuffer_load_dwordx4 %[vA2], %[rA], s40 offen offset:2048 lds\n\tbuffer_load_dwordx4 %[vA3], %[rA], s40 offen offset:3072 lds\n\ts_add_u32 m0, %[lds0], 47104\n\ts_nop 0\n\tbuffer_load_dwordx4 %[vB0], %[rB], s41 offen offset:0 lds\n\tbuffer_load_dwordx4 %[vB1], %[rB], s41 offen offset:1024 lds\n\ts_add_u32 s40, s40, 1024\n\ts_add_u32 s41, s41, 65536\n\ts_add_u32 m0, %[lds0], 61440\n\ts_nop 0\n\tbuffer_load_dwordx4 %[vA0], %[rA], s40 offen offset:0 lds\n\tbuffer_load_dwordx4 %[vA1], %[rA], s40 offen offset:1024 lds\n\tbuffer_load_dwordx4 %[vA2], %[rA], s40 offen offset:2048 lds\n\tbuffer_load_dwordx4 %[vA3], %[rA], s40 offen offset:3072 lds\n\ts_add_u32 m0, %[lds0], 71680\n\ts_nop 0\n\tbuffer_load_dwordx4 %[vB0], %[rB], s41 offen offset:0 lds\n\tbuffer_load_dwordx4 %[vB1], %[rB], s41 offen offset:1024 lds\n\ts_add_u32 s40, s40, 1024\n\ts_add_u32 s41, s41, 65536\n\ts_waitcnt vmcnt(12)\n\ts_barrier\n\ts_add_u32 m0, %[lds0], 86016\n\ts_nop 0\n\tbuffer_load_dwordx4 %[vA0], %[rA], s40 offen offset:0 lds\n\tbuffer_load_dwordx4 %[vA1], %[rA], s40 offen offset:1024 lds\n\tbuffer_load_dwordx4 %[vA2], %[rA], s40 offen offset:2048 lds\n\tbuffer_load_dwordx4 %[vA3], %[rA], s40 offen offset:3072 lds\n\ts_add_u32 m0, %[lds0], 96256\n\ts_nop 0\n\tbuffer_load_dwordx4 %[vB0], %[rB], s41 offen offset:0 lds\n\tbuffer_load_dwordx4 %[vB1], %[rB], s41 offen offset:1024 lds\n\ts_add_u32 s40, s40, 1024\n\ts_add_u32 s41, s41, 65536\n\ts_add_u32 m0, %[lds0], 110592\n\ts_nop 0\n\tbuffer_load_dwordx4 %[vA0], %[rA], s40 offen offset:0 lds\n\tbuffer_load_dwordx4 %[vA1], %[rA], s40 offen offset:1024 lds\n\tbuffer_load_dwordx4 %[vA2], %[rA], s40 offen offset:2048 lds\n\tbuffer_load_dwordx4 %[vA3], %[rA], s40 offen offset:3072 lds\n\ts_add_u32 m0, %[lds0], 120832\n\ts_nop 0\n\tbuffer_load_dwordx4 %[vB0], %[rB], s41 offen offset:0 lds\n\tbuffer_load_dwordx4 %[vB1], %[rB], s41 offen offset:1024 lds\n\ts_add_u32 s40, s40, 1024\n\ts_add_u32 s41, s41, 65536\n\ts_waitcnt vmcnt(12)\n\ts_barrier\n\ts_add_u32 m0, %[lds0], 135168\n\ts_nop 0\n\tbuffer_load_dwordx4 %[vA0], %[rA], s40 offen offset:0 lds\n\tbuffer_load_dwordx4 %[vA1], %[rA], s40 offen offset:1024 lds\n\tbuffer_load_dwordx4 %[vA2], %[rA], s40 offen offset:2048 lds\n\tbuffer_load_dwordx4 %[vA3], %[rA], s40 offen offset:3072 lds\n\ts_add_u32 m0, %[lds0], 145408\n\ts_nop 0\n\tbuffer_load_dwordx4 %[vB0], %[rB], s41 offen offset:0 lds\n\tbuffer_load_dwordx4 %[vB1], %[rB], s41 offen offset:1024 lds\n\ts_add_u32 s40, s40, 1024\n\ts_add_u32 s41, s41, 65536\n\ts_add_u32 m0, %[lds0], 12288\n\ts_nop 0\n\tbuffer_load_dwordx4 %[vA0], %[rA], s40 offen offset:0 lds\n\tbuffer_load_dwordx4 %[vA1], %[rA], s40 offen offset:1024 lds\n\tbuffer_load_dwordx4 %[vA2], %[rA], s40 offen offset:2048 lds\n\tbuffer_load_dwordx4 %[vA3], %[rA], s40 offen offset:3072 lds\n\ts_add_u32 m0, %[lds0], 22528\n\ts_nop 0\n\tbuffer_load_dwordx4 %[vB0], %[rB], s41 offen offset:0 lds\n\tbuffer_load_dwordx4 %[vB1], %[rB], s41 offen offset:1024 lds\n\ts_add_u32 s40, s40, 1024\n\ts_add_u32 s41, s41, 65536\n\ts_waitcnt vmcnt(12)\n\ts_barrier\n\ts_add_u32 m0, %[lds0], 36864\n\ts_nop 0\n\tbuffer_load_dwordx4 %[vA0], %[rA], s40 offen offset:0 lds\n\tbuffer_load_dwordx4 %[vA1], %[rA], s40 offen offset:1024 lds\n\tbuffer_load_dwordx4 %[vA2], %[rA], s40 offen offset:2048 lds\n\tbuffer_load_dwordx4 %[vA3], %[rA], s40 offen offset:3072 lds\n\ts_add_u32 m0, %[lds0], 47104\n\ts_nop 0\n\tbuffer_load_dwordx4 %[vB0], %[rB], s41 offen offset:0 lds\n\tbuffer_load_dwordx4 %[vB1], %[rB], s41 offen offset:1024 lds\n\ts_add_u32 s40, s40, 1024\n\ts_add_u32 s41, s41, 65536\n\ts_add_u32 m0, %[lds0], 61440\n\ts_nop 0\n\tbuffer_load_dwordx4 %[vA0], %[rA], s40 offen offset:0 lds\n\tbuffer_load_dwordx4 %[vA1], %[rA], s40 offen offset:1024 lds\n\tbuffer_load_dwordx4 %[vA2], %[rA], s40 offen offset:2048 lds\n\tbuffer_load_dwordx4 %[vA3], %[rA], s40 offen offset:3072 lds\n\ts_add_u32 m0, %[lds0], 71680\n\ts_nop 0\n\tbuffer_load_dwordx4 %[vB0], %[rB], s41 offen offset:0 lds\n\tbuffer_load_dwordx4 %[vB1], %[rB], s41 offen offset:1024 lds\n\ts_add_u32 s40, s40, 1024\n\ts_add_u32 s41, s41, 65536\n\ts_waitcnt vmcnt(12)\n\ts_barrier\n\tv_mov_b32 v43, 1\n\tv_mov_b32 v44, 0\n\ts_mov_b64 exec, 1\n\tglobal_atomic_add v44, v43, %[oadd]\n\ts_mov_b64 exec, -1\n\ts_add_u32 m0, %[lds0], 86016\n\ts_nop 0\n\tbuffer_load_dwordx4 %[vA0], %[rA], s40 offen offset:0 lds\n\tbuffer_load_dwordx4 %[vA1], %[rA], s40 offen offset:1024 lds\n\tbuffer_load_dwordx4 %[vA2], %[rA], s40 offen offset:2048 lds\n\tbuffer_load_dwordx4 %[vA3], %[rA], s40 offen offset:3072 lds\n\ts_add_u32 m0, %[lds0], 96256\n\ts_nop 0\n\tbuffer_load_dwordx4 %[vB0], %[rB], s41 offen offset:0 lds\n\tbuffer_load_dwordx4 %[vB1], %[rB], s41 offen offset:1024 lds\n\ts_add_u32 s40, s40, 1024\n\ts_add_u32 s41, s41, 65536\n\ts_add_u32 m0, %[lds0], 110592\n\ts_nop 0\n\tbuffer_load_dwordx4 %[vA0], %[rA], s40 offen offset:0 lds\n\tbuffer_load_dwordx4 %[vA1], %[rA], s40 offen offset:1024 lds\n\tbuffer_load_dwordx4 %[vA2], %[rA], s40 offen offset:2048 lds\n\tbuffer_load_dwordx4 %[vA3], %[rA], s40 offen offset:3072 lds\n\ts_add_u32 m0, %[lds0], 120832\n\ts_nop 0\n\tbuffer_load_dwordx4 %[vB0], %[rB], s41 offen offset:0 lds\n\tbuffer_load_dwordx4 %[vB1], %[rB], s41 offen offset:1024 lds\n\ts_add_u32 s40, s40, 1024\n\ts_add_u32 s41, s41, 65536\n\ts_waitcnt vmcnt(12)\n\ts_barrier\n\ts_add_u32 m0, %[lds0], 135168\n\ts_nop 0\n\tbuffer_load_dwordx4 %[vA0], %[rA], s40 offen offset:0 lds\n\tbuffer_load_dwordx4 %[vA1], %[rA], s40 offen offset:1024 lds\n\tbuffer_load_dwordx4 %[vA2], %[rA], s40 offen offset:2048 lds\n\tbuffer_load_dwordx4 %[vA3], %[rA], s40 offen offset:3072 lds\n\ts_add_u32 m0, %[lds0], 145408\n\ts_nop 0\n\tbuffer_load_dwordx4 %[vB0], %[rB], s41 offen offset:0 lds\n\tbuffer_load_dwordx4 %[vB1], %[rB], s41 offen offset:1024 lds\n\ts_add_u32 s40, s40, 1024\n\ts_add_u32 s41, s41, 65536\n\ts_add_u32 m0, %[lds0], 12288\n\ts_nop 0\n\tbuffer_load_dwordx4 %[vA0], %[rA], s40 offen offset:0 lds\n\tbuffer_load_dwordx4 %[vA1], %[rA], s40 offen offset:1024 lds\n\tbuffer_load_dwordx4 %[vA2], %[rA], s40 offen offset:2048 lds\n\tbuffer_load_dwordx4 %[vA3], %[rA], s40 offen offset:3072 lds\n\ts_add_u32 m0, %[lds0], 22528\n\ts_nop 0\n\tbuffer_load_dwordx4 %[vB0], %[rB], s41 offen offset:0 lds\n\tbuffer_load_dwordx4 %[vB1], %[rB], s41 offen offset:1024 lds\n\ts_add_u32 s40, s40, 1024\n\ts_add_u32 s41, s41, 65536\n\ts_waitcnt vmcnt(12)\n\ts_barrier\n\tv_mov_b32 v44, 0\n\tglobal_load_dword v43, v44, %[opoll] sc1\n\ts_add_u32 m0, %[lds0], 36864\n\ts_nop 0\n\tbuffer_load_dwordx4 %[vA0], %[rA], s40 offen offset:0 lds\n\tbuffer_load_dwordx4 %[vA1], %[rA], s40 offen offset:1024 lds\n\tbuffer_load_dwordx4 %[vA2], %[rA], s40 offen offset:2048 lds\n\tbuffer_load_dwordx4 %[vA3], %[rA], s40 offen offset:3072 lds\n\ts_add_u32 m0, %[lds0], 47104\n\ts_nop 0\n\tbuffer_load_dwordx4 %[vB0], %[rB], s41 offen offset:0 lds\n\tbuffer_load_dwordx4 %[vB1], %[rB], s41 offen offset:1024 lds\n\ts_add_u32 s40, s40, 1024\n\ts_add_u32 s41, s41, 65536\n\ts_add_u32 m0, %[lds0], 61440\n\ts_nop 0\n\tbuffer_load_dwordx4 %[vA0], %[rA], s40 offen offset:0 lds\n\tbuffer_load_dwordx4 %[vA1], %[rA], s40 offen offset:1024 lds\n\tbuffer_load_dwordx4 %[vA2], %[rA], s40 offen offset:2048 lds\n\tbuffer_load_dwordx4 %[vA3], %[rA], s40 offen offset:3072 lds\n\ts_add_u32 m0, %[lds0], 71680\n\ts_nop 0\n\tbuffer_load_dwordx4 %[vB0], %[rB], s41 offen offset:0 lds\n\tbuffer_load_dwordx4 %[vB1], %[rB], s41 offen offset:1024 lds\n\ts_add_u32 s40, s40, 1024\n\ts_add_u32 s41, s41, 65536\n\ts_waitcnt vmcnt(12)\n\ts_barrier\n\ts_add_u32 m0, %[lds0], 86016\n\ts_nop 0\n\tbuffer_load_dwordx4 %[vA0], %[rA], s40 offen offset:0 lds\n\tbuffer_load_dwordx4 %[vA1], %[rA], s40 offen offset:1024 lds\n\tbuffer_load_dwordx4 %[vA2], %[rA], s40 offen offset:2048 lds\n\tbuffer_load_dwordx4 %[vA3], %[rA], s40 offen offset:3072 lds\n\ts_add_u32 m0, %[lds0], 96256\n\ts_nop 0\n\tbuffer_load_dwordx4 %[vB0], %[rB], s41 offen offset:0 lds\n\tbuffer_load_dwordx4 %[vB1], %[rB], s41 offen offset:1024 lds\n\ts_add_u32 s40, s40, 1024\n\ts_add_u32 s41, s41, 65536\n\ts_add_u32 m0, %[lds0], 110592\n\ts_nop 0\n\tbuffer_load_dwordx4 %[vA0], %[rA], s40 offen offset:0 lds\n\tbuffer_load_dwordx4 %[vA1], %[rA], s40 offen offset:1024 lds\n\tbuffer_load_dwordx4 %[vA2], %[rA], s40 offen offset:2048 lds\n\tbuffer_load_dwordx4 %[vA3], %[rA], s40 offen offset:3072 lds\n\ts_add_u32 m0, %[lds0], 120832\n\ts_nop 0\n\tbuffer_load_dwordx4 %[vB0], %[rB], s41 offen offset:0 lds\n\tbuffer_load_dwordx4 %[vB1], %[rB], s41 offen offset:1024 lds\n\ts_add_u32 s40, s40, 1024\n\ts_add_u32 s41, s41, 65536\n\ts_waitcnt vmcnt(12)\n\ts_barrier\n\ts_add_u32 m0, %[lds0], 135168\n\ts_nop 0\n\tbuffer_load_dwordx4 %[vA0], %[rA], s40 offen offset:0 lds\n\tbuffer_load_dwordx4 %[vA1], %[rA], s40 offen offset:1024 lds\n\tbuffer_load_dwordx4 %[vA2], %[rA], s40 offen offset:2048 lds\n\tbuffer_load_dwordx4 %[vA3], %[rA], s40 offen offset:3072 lds\n\ts_add_u32 m0, %[lds0], 145408\n\ts_nop 0\n\tbuffer_load_dwordx4 %[vB0], %[rB], s41 offen offset:0 lds\n\tbuffer_load_dwordx4 %[vB1], %[rB], s41 offen offset:1024 lds\n\ts_add_u32 s40, s40, 1024\n\ts_add_u32 s41, s41, 65536\n\ts_add_u32 m0, %[lds0], 12288\n\ts_nop 0\n\tbuffer_load_dwordx4 %[vA0], %[rA], s40 offen offset:0 lds\n\tbuffer_load_dwordx4 %[vA1], %[rA], s40 offen offset:1024 lds\n\tbuffer_load_dwordx4 %[vA2], %[rA], s40 offen offset:2048 lds\n\tbuffer_load_dwordx4 %[vA3], %[rA], s40 offen offset:3072 lds\n\ts_add_u32 m0, %[lds0], 22528\n\ts_nop 0\n\tbuffer_load_dwordx4 %[vB0], %[rB], s41 offen offset:0 lds\n\tbuffer_load_dwordx4 %[vB1], %[rB], s41 offen offset:1024 lds\n\ts_add_u32 s40, s40, 1024\n\ts_add_u32 s41, s41, 65536\n\ts_waitcnt vmcnt(12)\n\ts_barrier\n\tv_readfirstlane_b32 s70, v43\n\ts_mov_b32 s71, 0\n\ts_nop 3\n\ts_cmp_ge_u32 s70, 32\n\ts_cbranch_scc1 L2X_d%=\n\tL2X_p%=:\n\tglobal_load_dword v43, v44, %[opoll] sc1\n\ts_waitcnt vmcnt(0)\n\tv_readfirstlane_b32 s70, v43\n\ts_add_u32 s71, s71, 1\n\ts_nop 3\n\ts_cmp_ge_u32 s70, 32\n\ts_cbranch_scc1 L2X_d%=\n\ts_cmp_gt_u32 s71, 0x10000\n\ts_cbranch_scc1 L2X_d%=\n\ts_sleep 2\n\ts_branch L2X_p%=\n\tL2X_d%=:\n\tbuffer_inv sc1\n\ts_add_u32 m0, %[lds0], 36864\n\ts_nop 0\n\tbuffer_load_dwordx4 %[vA0], %[rA], s40 offen offset:0 lds\n\tbuffer_load_dwordx4 %[vA1], %[rA], s40 offen offset:1024 lds\n\tbuffer_load_dwordx4 %[vA2], %[rA], s40 offen offset:2048 lds\n\tbuffer_load_dwordx4 %[vA3], %[rA], s40 offen offset:3072 lds\n\ts_add_u32 m0, %[lds0], 47104\n\ts_nop 0\n\tbuffer_load_dwordx4 %[vB0], %[rB], s41 offen offset:0 lds\n\tbuffer_load_dwordx4 %[vB1], %[rB], s41 offen offset:1024 lds\n\ts_add_u32 s40, s40, 1024\n\ts_add_u32 s41, s41, 65536\n\ts_add_u32 m0, %[lds0], 61440\n\ts_nop 0\n\tbuffer_load_dwordx4 %[vA0], %[rA], s40 offen offset:0 lds\n\tbuffer_load_dwordx4 %[vA1], %[rA], s40 offen offset:1024 lds\n\tbuffer_load_dwordx4 %[vA2], %[rA], s40 offen offset:2048 lds\n\tbuffer_load_dwordx4 %[vA3], %[rA], s40 offen offset:3072 lds\n\ts_add_u32 m0, %[lds0], 71680\n\ts_nop 0\n\tbuffer_load_dwordx4 %[vB0], %[rB], s41 offen offset:0 lds\n\tbuffer_load_dwordx4 %[vB1], %[rB], s41 offen offset:1024 lds\n\ts_add_u32 s40, s40, 1024\n\ts_add_u32 s41, s41, 65536\n\ts_waitcnt vmcnt(12)\n\ts_barrier\n\ts_add_u32 m0, %[lds0], 86016\n\ts_nop 0\n\tbuffer_load_dwordx4 %[vA0], %[rA], s40 offen offset:0 lds\n\tbuffer_load_dwordx4 %[vA1], %[rA], s40 offen offset:1024 lds\n\tbuffer_load_dwordx4 %[vA2], %[rA], s40 offen offset:2048 lds\n\tbuffer_load_dwordx4 %[vA3], %[rA], s40 offen offset:3072 lds\n\ts_add_u32 m0, %[lds0], 96256\n\ts_nop 0\n\tbuffer_load_dwordx4 %[vB0], %[rB], s41 offen offset:0 lds\n\tbuffer_load_dwordx4 %[vB1], %[rB], s41 offen offset:1024 lds\n\ts_add_u32 s40, s40, 1024\n\ts_add_u32 s41, s41, 65536\n\ts_mov_b32 s40, %[oO]\n\ts_mov_b32 s41, %[oW]\n\ts_add_u32 m0, %[lds0], 110592\n\ts_nop 0\n\tbuffer_load_dwordx4 %[vO0], %[rO], s40 offen offset:0 lds\n\tbuffer_load_dwordx4 %[vO1], %[rO], s40 offen offset:1024 lds\n\tbuffer_load_dwordx4 %[vO2], %[rO], s40 offen offset:2048 lds\n\tbuffer_load_dwordx4 %[vO3], %[rO], s40 offen offset:3072 lds\n\ts_add_u32 m0, %[lds0], 120832\n\ts_nop 0\n\tbuffer_load_dwordx4 %[vB0], %[rW], s41 offen offset:0 lds\n\tbuffer_load_dwordx4 %[vB1], %[rW], s41 offen offset:1024 lds\n\ts_add_u32 s40, s40, 1024\n\ts_add_u32 s41, s41, 65536\n\ts_waitcnt vmcnt(12)\n\ts_barrier\n\ts_add_u32 m0, %[lds0], 135168\n\ts_nop 0\n\tbuffer_load_dwordx4 %[vO0], %[rO], s40 offen offset:0 lds\n\tbuffer_load_dwordx4 %[vO1], %[rO], s40 offen offset:1024 lds\n\tbuffer_load_dwordx4 %[vO2], %[rO], s40 offen offset:2048 lds\n\tbuffer_load_dwordx4 %[vO3], %[rO], s40 offen offset:3072 lds\n\ts_add_u32 m0, %[lds0], 145408\n\ts_nop 0\n\tbuffer_load_dwordx4 %[vB0], %[rW], s41 offen offset:0 lds\n\tbuffer_load_dwordx4 %[vB1], %[rW], s41 offen offset:1024 lds\n\ts_add_u32 s40, s40, 1024\n\ts_add_u32 s41, s41, 65536\n\ts_add_u32 m0, %[lds0], 12288\n\ts_nop 0\n\tbuffer_load_dwordx4 %[vO0], %[rO], s40 offen offset:0 lds\n\tbuffer_load_dwordx4 %[vO1], %[rO], s40 offen offset:1024 lds\n\tbuffer_load_dwordx4 %[vO2], %[rO], s40 offen offset:2048 lds\n\tbuffer_load_dwordx4 %[vO3], %[rO], s40 offen offset:3072 lds\n\ts_add_u32 m0, %[lds0], 22528\n\ts_nop 0\n\tbuffer_load_dwordx4 %[vB0], %[rW], s41 offen offset:0 lds\n\tbuffer_load_dwordx4 %[vB1], %[rW], s41 offen offset:1024 lds\n\ts_add_u32 s40, s40, 1024\n\ts_add_u32 s41, s41, 65536\n\ts_waitcnt vmcnt(12)\n\ts_barrier\n\ts_add_u32 m0, %[lds0], 36864\n\ts_nop 0\n\tbuffer_load_dwordx4 %[vO0], %[rO], s40 offen offset:0 lds\n\tbuffer_load_dwordx4 %[vO1], %[rO], s40 offen offset:1024 lds\n\tbuffer_load_dwordx4 %[vO2], %[rO], s40 offen offset:2048 lds\n\tbuffer_load_dwordx4 %[vO3], %[rO], s40 offen offset:3072 lds\n\ts_add_u32 m0, %[lds0], 47104\n\ts_nop 0\n\tbuffer_load_dwordx4 %[vB0], %[rW], s41 offen offset:0 lds\n\tbuffer_load_dwordx4 %[vB1], %[rW], s41 offen offset:1024 lds\n\ts_add_u32 s40, s40, 1024\n\ts_add_u32 s41, s41, 65536\n\ts_add_u32 m0, %[lds0], 61440\n\ts_nop 0\n\tbuffer_load_dwordx4 %[vO0], %[rO], s40 offen offset:0 lds\n\tbuffer_load_dwordx4 %[vO1], %[rO], s40 offen offset:1024 lds\n\tbuffer_load_dwordx4 %[vO2], %[rO], s40 offen offset:2048 lds\n\tbuffer_load_dwordx4 %[vO3], %[rO], s40 offen offset:3072 lds\n\ts_add_u32 m0, %[lds0], 71680\n\ts_nop 0\n\tbuffer_load_dwordx4 %[vB0], %[rW], s41 offen offset:0 lds\n\tbuffer_load_dwordx4 %[vB1], %[rW], s41 offen offset:1024 lds\n\ts_add_u32 s40, s40, 1024\n\ts_add_u32 s41, s41, 65536\n\ts_waitcnt vmcnt(12)\n\ts_barrier\n\ts_add_u32 m0, %[lds0], 86016\n\ts_nop 0\n\tbuffer_load_dwordx4 %[vO0], %[rO], s40 offen offset:0 lds\n\tbuffer_load_dwordx4 %[vO1], %[rO], s40 offen offset:1024 lds\n\tbuffer_load_dwordx4 %[vO2], %[rO], s40 offen offset:2048 lds\n\tbuffer_load_dwordx4 %[vO3], %[rO], s40 offen offset:3072 lds\n\ts_add_u32 m0, %[lds0], 96256\n\ts_nop 0\n\tbuffer_load_dwordx4 %[vB0], %[rW], s41 offen offset:0 lds\n\tbuffer_load_dwordx4 %[vB1], %[rW], s41 offen offset:1024 lds\n\ts_add_u32 s40, s40, 1024\n\ts_add_u32 s41, s41, 65536\n\ts_add_u32 m0, %[lds0], 110592\n\ts_nop 0\n\tbuffer_load_dwordx4 %[vO0], %[rO], s40 offen offset:0 lds\n\tbuffer_load_dwordx4 %[vO1], %[rO], s40 offen offset:1024 lds\n\tbuffer_load_dwordx4 %[vO2], %[rO], s40 offen offset:2048 lds\n\tbuffer_load_dwordx4 %[vO3], %[rO], s40 offen offset:3072 lds\n\ts_add_u32 m0, %[lds0], 120832\n\ts_nop 0\n\tbuffer_load_dwordx4 %[vB0], %[rW], s41 offen offset:0 lds\n\tbuffer_load_dwordx4 %[vB1], %[rW], s41 offen offset:1024 lds\n\ts_add_u32 s40, s40, 1024\n\ts_add_u32 s41, s41, 65536\n\ts_waitcnt vmcnt(12)\n\ts_barrier\n\ts_add_u32 m0, %[lds0], 135168\n\ts_nop 0\n\tbuffer_load_dwordx4 %[vO0], %[rO], s40 offen offset:0 lds\n\tbuffer_load_dwordx4 %[vO1], %[rO], s40 offen offset:1024 lds\n\tbuffer_load_dwordx4 %[vO2], %[rO], s40 offen offset:2048 lds\n\tbuffer_load_dwordx4 %[vO3], %[rO], s40 offen offset:3072 lds\n\ts_add_u32 m0, %[lds0], 145408\n\ts_nop 0\n\tbuffer_load_dwordx4 %[vB0], %[rW], s41 offen offset:0 lds\n\tbuffer_load_dwordx4 %[vB1], %[rW], s41 offen offset:1024 lds\n\ts_add_u32 s40, s40, 1024\n\ts_add_u32 s41, s41, 65536\n\ts_waitcnt vmcnt(6)\n\ts_barrier\n\ts_waitcnt vmcnt(0)\n\ts_barrier"
#define L2_3_CLOB "s66","s40","s41","s70","s71","v43","v44","v52","v53","v54","v55","v56","v57","v58","v59","v60","v61","v62","v63","v64","v65","v66","v67"
template<int I> DI float rdacc(int tok){ float r; asm("v_accvgpr_read_b32 %0, a%c1":"=v"(r):"i"(I),"s"(tok)); return r; }
template<int I> DI f32x4 rdacc4(int tok){ f32x4 r; r[0]=rdacc<I>(tok); r[1]=rdacc<I+1>(tok); r[2]=rdacc<I+2>(tok); r[3]=rdacc<I+3>(tok); return r; }

constexpr int G1_SLOT=26624, G1_NS=6;
template<int I> DI void wracc(float v){ asm volatile("v_accvgpr_write_b32 a%c0, %1"::"i"(I),"v"(v)); }
template<int I> DI void zacc(){ asm volatile("v_accvgpr_write_b32 a%c0, 0"::"i"(I)); }
template<int I> DI void wracc4(u32x4 v){ wracc<I>(__uint_as_float(v[0])); wracc<I+1>(__uint_as_float(v[1])); wracc<I+2>(__uint_as_float(v[2])); wracc<I+3>(__uint_as_float(v[3])); }
#define G1A(m,n) (((m)*8+(n))*4)
template<int N> DI void g1_zero(){ zacc<G1A(0,N)>(); zacc<G1A(0,N)+1>(); zacc<G1A(0,N)+2>(); zacc<G1A(0,N)+3>(); zacc<G1A(1,N)>(); zacc<G1A(1,N)+1>(); zacc<G1A(1,N)+2>(); zacc<G1A(1,N)+3>();
  zacc<G1A(2,N)>(); zacc<G1A(2,N)+1>(); zacc<G1A(2,N)+2>(); zacc<G1A(2,N)+3>(); zacc<G1A(3,N)>(); zacc<G1A(3,N)+1>(); zacc<G1A(3,N)+2>(); zacc<G1A(3,N)+3>(); }
template<int N> DI void g1_init(const u32x2 b){ const float b0=bflo(b[0]),b1=bfhi(b[0]),b2=bflo(b[1]),b3=bfhi(b[1]);
  wracc<G1A(0,N)>(b0); wracc<G1A(0,N)+1>(b1); wracc<G1A(0,N)+2>(b2); wracc<G1A(0,N)+3>(b3); wracc<G1A(1,N)>(b0); wracc<G1A(1,N)+1>(b1); wracc<G1A(1,N)+2>(b2); wracc<G1A(1,N)+3>(b3);
  wracc<G1A(2,N)>(b0); wracc<G1A(2,N)+1>(b1); wracc<G1A(2,N)+2>(b2); wracc<G1A(2,N)+3>(b3); wracc<G1A(3,N)>(b0); wracc<G1A(3,N)+1>(b1); wracc<G1A(3,N)+2>(b2); wracc<G1A(3,N)+3>(b3); }
template<int NP> DI void gelu_stage(const f32x2 (&v)[NP],unsigned (&o)[NP]){
  const float c0=-2.f*0.7978845608028654f*1.4426950408889634f, c1=c0*0.044715f;
  f32x2 t[NP];
  #pragma unroll
  for(int i=0;i<NP;i++) t[i]=v[i]*v[i];
  SBAR();
  #pragma unroll
  for(int i=0;i<NP;i++) t[i]=t[i]*c1+c0;
  SBAR();
  #pragma unroll
  for(int i=0;i<NP;i++) t[i]=t[i]*v[i];
  SBAR();
  #pragma unroll
  for(int i=0;i<NP;i++){ t[i][0]=__builtin_amdgcn_exp2f(t[i][0]); t[i][1]=__builtin_amdgcn_exp2f(t[i][1]); }
  SBAR();
  #pragma unroll
  for(int i=0;i<NP;i++) t[i]=t[i]+1.f;
  SBAR();
  #pragma unroll
  for(int i=0;i<NP;i++){ t[i][0]=__builtin_amdgcn_rcpf(t[i][0]); t[i][1]=__builtin_amdgcn_rcpf(t[i][1]); }
  SBAR();
  #pragma unroll
  for(int i=0;i<NP;i++) t[i]=t[i]*v[i];
  SBAR();
  #pragma unroll
  for(int i=0;i<NP;i++) o[i]=cvtpk(t[i][0],t[i][1]);
  SBAR();
}
template<int N0,int M0> DI void g1_epi_pair2(LAS char* hs,int tok){
  const f32x4 x0=rdacc4<G1A(M0,N0)>(tok), y0=rdacc4<G1A(M0,N0+1)>(tok), x1=rdacc4<G1A(M0+1,N0)>(tok), y1=rdacc4<G1A(M0+1,N0+1)>(tok);
  const f32x2 v[8]={{x0[0],x0[1]},{x0[2],x0[3]},{y0[0],y0[1]},{y0[2],y0[3]},{x1[0],x1[1]},{x1[2],x1[3]},{y1[0],y1[1]},{y1[2],y1[3]}};
  SBAR(); unsigned o[8]; gelu_stage<8>(v,o);
  *(LAS u32x4*)(hs+M0*1024)=u32x4{o[0],o[1],o[2],o[3]}; *(LAS u32x4*)(hs+(M0+1)*1024)=u32x4{o[4],o[5],o[6],o[7]};
}
template<int N0> DI void g1_epi_pair(LAS char* hs,int tok){ g1_epi_pair2<N0,0>(hs,tok); g1_epi_pair2<N0,2>(hs,tok); }
DI void g1_epi_half(LAS char* hs2,const f32x4 (&x)[4]){
  const f32x2 v[8]={{x[0][0],x[0][1]},{x[0][2],x[0][3]},{x[1][0],x[1][1]},{x[1][2],x[1][3]},{x[2][0],x[2][1]},{x[2][2],x[2][3]},{x[3][0],x[3][1]},{x[3][2],x[3][3]}};
  SBAR(); unsigned o[8]; gelu_stage<8>(v,o);
  #pragma unroll
  for(int m=0;m<4;m++) *(LAS u32x2*)(hs2+m*512)=u32x2{o[2*m],o[2*m+1]};
}
constexpr int AT_SLOT=16384, AT_BASE=98304, CS_BASE=57344, HS_BASE=0, HS2_BASE=49152, OS_BASE=98304;
DI void attn_slow(const bf16x8 q0,const bf16x8 q1,const char* __restrict__ KV,int lane,f32x16& o,float& l){
  float z; asm volatile("v_mov_b32 %0, 0":"=v"(z));
  f32x16 zero; for(int i=0;i<16;i++) zero[i]=z;
  int ln; asm volatile("v_mbcnt_lo_u32_b32 %0, -1, 0\n\tv_mbcnt_hi_u32_b32 %0, -1, %0":"=v"(ln)); lane=ln;
  float m=-INFINITY+z; l=z; o=zero;
  #pragma unroll 1
  for(int t=0;t<64;t++){
    const char* p=KV+(size_t)t*4096+lane*16;
    const bf16x8 k0=*(const bf16x8*)p,k1=*(const bf16x8*)(p+1024),v0=*(const bf16x8*)(p+2048),v1=*(const bf16x8*)(p+3072);
    f32x16 s=MFMA32(k0,q0,zero); s=MFMA32(k1,q1,s);
    float mx=s[0];
    #pragma unroll
    for(int i=1;i<16;i++) mx=fmaxf(mx,s[i]);
    mx=fmaxf(mx,__shfl_xor(mx,32));
    const float mn=fmaxf(m,mx); const float f=__builtin_amdgcn_exp2f(m-mn); m=mn;
    float a=0.f; u32x4 p0,p1;
    #pragma unroll
    for(int i=0;i<16;i++){ s[i]=__builtin_amdgcn_exp2f(s[i]-m); }
    #pragma unroll
    for(int i=0;i<4;i++){ p0[i]=cvtpk(s[2*i],s[2*i+1]); p1[i]=cvtpk(s[8+2*i],s[8+2*i+1]); a+=bflo(p0[i])+bfhi(p0[i])+bflo(p1[i])+bfhi(p1[i]); }
    a+=__shfl_xor(a,32); l=l*f+a;
    #pragma unroll
    for(int i=0;i<16;i++) o[i]*=f;
    o=MFMA32(v0,BF8(p0),o); o=MFMA32(v1,BF8(p1),o);
  }
}
#define ATN_ASM "v_accvgpr_write_b32 a0, 0\n\tv_accvgpr_write_b32 a1, 0\n\tv_accvgpr_write_b32 a2, 0\n\tv_accvgpr_write_b32 a3, 0\n\tv_accvgpr_write_b32 a4, 0\n\tv_accvgpr_write_b32 a5, 0\n\tv_accvgpr_write_b32 a6, 0\n\tv_accvgpr_write_b32 a7, 0\n\tv_accvgpr_write_b32 a8, 0\n\tv_accvgpr_write_b32 a9, 0\n\tv_accvgpr_write_b32 a10, 0\n\tv_accvgpr_write_b32 a11, 0\n\tv_accvgpr_write_b32 a12, 0\n\tv_accvgpr_write_b32 a13, 0\n\tv_accvgpr_write_b32 a14, 0\n\tv_accvgpr_write_b32 a15, 0\n\tv_accvgpr_write_b32 a16, 0\n\tv_accvgpr_write_b32 a17, 0\n\tv_accvgpr_write_b32 a18, 0\n\tv_accvgpr_write_b32 a19, 0\n\tv_accvgpr_write_b32 a20, 0\n\tv_accvgpr_write_b32 a21, 0\n\tv_accvgpr_write_b32 a22, 0\n\tv_accvgpr_write_b32 a23, 0\n\tv_accvgpr_write_b32 a24, 0\n\tv_accvgpr_write_b32 a25, 0\n\tv_accvgpr_write_b32 a26, 0\n\tv_accvgpr_write_b32 a27, 0\n\tv_accvgpr_write_b32 a28, 0\n\tv_accvgpr_write_b32 a29, 0\n\tv_accvgpr_write_b32 a30, 0\n\tv_accvgpr_write_b32 a31, 0\n\tv_accvgpr_write_b32 a32, 0\n\tv_accvgpr_write_b32 a33, 0\n\tv_accvgpr_write_b32 a34, 0\n\tv_accvgpr_write_b32 a35, 0\n\tv_accvgpr_write_b32 a36, 0\n\tv_accvgpr_write_b32 a37, 0\n\tv_accvgpr_write_b32 a38, 0\n\tv_accvgpr_write_b32 a39, 0\n\tv_mov_b32 v120, 0\n\tv_mov_b32 v121, 0\n\tv_mov_b32 v122, 0\n\tv_mov_b32 v123, 0\n\tv_mov_b32 v124, 0\n\tv_mov_b32 v125, 0\n\tv_mov_b32 v126, 0\n\tv_mov_b32 v127, 0\n\tds_read_b128 a[40:43], %[a0] offset:0\n\tds_read_b128 a[44:47], %[a0] offset:1024\n\tds_read_b128 a[48:51], %[a0] offset:2048\n\tds_read_b128 a[52:55], %[a0] offset:2048\n\ts_waitcnt lgkmcnt(2)\n\tv_mfma_f32_32x32x16_bf16 v[16:31], a[40:43], a[56:59], 0\n\tv_mfma_f32_32x32x16_bf16 v[48:63], a[40:43], a[64:67], 0\n\tv_mfma_f32_32x32x16_bf16 v[16:31], a[44:47], a[60:63], v[16:31]\n\tv_mfma_f32_32x32x16_bf16 v[48:63], a[44:47], a[68:71], v[48:63]\n\tds_read_b128 a[40:43], %[a0] offset:4096\n\tds_read_b128 a[44:47], %[a0] offset:5120\n\ts_nop 15\n\ts_nop 7\n\tv_max3_f32 v8, v16, v17, v18\n\tv_max3_f32 v9, v19, v20, v21\n\tv_max3_f32 v8, v8, v22, v23\n\tv_max3_f32 v9, v9, v24, v25\n\tv_max3_f32 v8, v8, v26, v27\n\tv_max3_f32 v9, v9, v28, v29\n\tv_max3_f32 v8, v8, v30, v31\n\tv_max_f32 v8, v8, v9\n\tv_mov_b32 v9, v8\n\ts_nop 1\n\tv_permlane32_swap_b32 v8, v9\n\ts_nop 1\n\tv_max_f32 v8, v8, v9\n\tv_sub_f32 v16, v16, v8\n\tv_sub_f32 v17, v17, v8\n\tv_sub_f32 v18, v18, v8\n\tv_sub_f32 v19, v19, v8\n\tv_sub_f32 v20, v20, v8\n\tv_sub_f32 v21, v21, v8\n\tv_sub_f32 v22, v22, v8\n\tv_sub_f32 v23, v23, v8\n\tv_sub_f32 v24, v24, v8\n\tv_sub_f32 v25, v25, v8\n\tv_sub_f32 v26, v26, v8\n\tv_sub_f32 v27, v27, v8\n\tv_sub_f32 v28, v28, v8\n\tv_sub_f32 v29, v29, v8\n\tv_sub_f32 v30, v30, v8\n\tv_sub_f32 v31, v31, v8\n\tv_sub_f32 v80, 0, v8\n\tv_sub_f32 v81, 0, v8\n\tv_sub_f32 v82, 0, v8\n\tv_sub_f32 v83, 0, v8\n\tv_sub_f32 v84, 0, v8\n\tv_sub_f32 v85, 0, v8\n\tv_sub_f32 v86, 0, v8\n\tv_sub_f32 v87, 0, v8\n\tv_sub_f32 v88, 0, v8\n\tv_sub_f32 v89, 0, v8\n\tv_sub_f32 v90, 0, v8\n\tv_sub_f32 v91, 0, v8\n\tv_sub_f32 v92, 0, v8\n\tv_sub_f32 v93, 0, v8\n\tv_sub_f32 v94, 0, v8\n\tv_sub_f32 v95, 0, v8\n\tv_max3_f32 v10, v48, v49, v50\n\tv_max3_f32 v11, v51, v52, v53\n\tv_max3_f32 v10, v10, v54, v55\n\tv_max3_f32 v11, v11, v56, v57\n\tv_max3_f32 v10, v10, v58, v59\n\tv_max3_f32 v11, v11, v60, v61\n\tv_max3_f32 v10, v10, v62, v63\n\tv_max_f32 v10, v10, v11\n\tv_mov_b32 v11, v10\n\ts_nop 1\n\tv_permlane32_swap_b32 v10, v11\n\ts_nop 1\n\tv_max_f32 v10, v10, v11\n\tv_sub_f32 v48, v48, v10\n\tv_sub_f32 v49, v49, v10\n\tv_sub_f32 v50, v50, v10\n\tv_sub_f32 v51, v51, v10\n\tv_sub_f32 v52, v52, v10\n\tv_sub_f32 v53, v53, v10\n\tv_sub_f32 v54, v54, v10\n\tv_sub_f32 v55, v55, v10\n\tv_sub_f32 v56, v56, v10\n\tv_sub_f32 v57, v57, v10\n\tv_sub_f32 v58, v58, v10\n\tv_sub_f32 v59, v59, v10\n\tv_sub_f32 v60, v60, v10\n\tv_sub_f32 v61, v61, v10\n\tv_sub_f32 v62, v62, v10\n\tv_sub_f32 v63, v63, v10\n\tv_sub_f32 v96, 0, v10\n\tv_sub_f32 v97, 0, v10\n\tv_sub_f32 v98, 0, v10\n\tv_sub_f32 v99, 0, v10\n\tv_sub_f32 v100, 0, v10\n\tv_sub_f32 v101, 0, v10\n\tv_sub_f32 v102, 0, v10\n\tv_sub_f32 v103, 0, v10\n\tv_sub_f32 v104, 0, v10\n\tv_sub_f32 v105, 0, v10\n\tv_sub_f32 v106, 0, v10\n\tv_sub_f32 v107, 0, v10\n\tv_sub_f32 v108, 0, v10\n\tv_sub_f32 v109, 0, v10\n\tv_sub_f32 v110, 0, v10\n\tv_sub_f32 v111, 0, v10\n\tAT_loop%=:\n\ts_waitcnt lgkmcnt(0)\n\tv_mfma_f32_32x32x16_bf16 v[32:47], a[40:43], a[56:59], v[80:95]\n\tv_exp_f32 v16, v16\n\tv_exp_f32 v17, v17\n\tv_exp_f32 v18, v18\n\tv_mfma_f32_32x32x16_bf16 a[0:15], a[52:55], v[120:123], a[0:15]\n\tv_exp_f32 v19, v19\n\tv_exp_f32 v20, v20\n\tv_exp_f32 v21, v21\n\tv_mfma_f32_32x32x16_bf16 v[32:47], a[44:47], a[60:63], v[32:47]\n\tv_exp_f32 v22, v22\n\tv_exp_f32 v23, v23\n\tv_cvt_pk_bf16_f32 v112, v16, v17\n\tv_cvt_pk_bf16_f32 v113, v18, v19\n\tv_mfma_f32_16x16x32_bf16 a[32:35], a[72:75], v[120:123], a[32:35]\n\tv_cvt_pk_bf16_f32 v114, v20, v21\n\tv_cvt_pk_bf16_f32 v115, v22, v23\n\tv_exp_f32 v24, v24\n\tv_exp_f32 v25, v25\n\tv_mfma_f32_32x32x16_bf16 a[0:15], a[48:51], v[112:115], a[0:15]\n\tv_exp_f32 v26, v26\n\tv_exp_f32 v27, v27\n\tv_exp_f32 v28, v28\n\tv_mfma_f32_16x16x32_bf16 a[32:35], a[72:75], v[112:115], a[32:35]\n\tv_exp_f32 v29, v29\n\tv_exp_f32 v30, v30\n\tv_exp_f32 v31, v31\n\tv_cvt_pk_bf16_f32 v120, v24, v25\n\tv_cvt_pk_bf16_f32 v121, v26, v27\n\tv_cvt_pk_bf16_f32 v122, v28, v29\n\tv_cvt_pk_bf16_f32 v123, v30, v31\n\tv_mfma_f32_32x32x16_bf16 v[64:79], a[40:43], a[64:67], v[96:111]\n\tds_read_b128 a[40:43], %[a0] offset:8192\n\tv_exp_f32 v48, v48\n\tv_exp_f32 v49, v49\n\tv_exp_f32 v50, v50\n\tv_mfma_f32_32x32x16_bf16 a[16:31], a[52:55], v[124:127], a[16:31]\n\tds_read_b128 a[52:55], %[a0] offset:3072\n\tv_exp_f32 v51, v51\n\tv_exp_f32 v52, v52\n\tv_exp_f32 v53, v53\n\tv_mfma_f32_32x32x16_bf16 v[64:79], a[44:47], a[68:71], v[64:79]\n\tds_read_b128 a[44:47], %[a0] offset:9216\n\tv_exp_f32 v54, v54\n\tv_exp_f32 v55, v55\n\tv_cvt_pk_bf16_f32 v116, v48, v49\n\tv_cvt_pk_bf16_f32 v117, v50, v51\n\tv_mfma_f32_16x16x32_bf16 a[36:39], a[72:75], v[124:127], a[36:39]\n\tv_cvt_pk_bf16_f32 v118, v52, v53\n\tv_cvt_pk_bf16_f32 v119, v54, v55\n\tv_exp_f32 v56, v56\n\tv_exp_f32 v57, v57\n\tv_mfma_f32_32x32x16_bf16 a[16:31], a[48:51], v[116:119], a[16:31]\n\tds_read_b128 a[48:51], %[a0] offset:6144\n\tv_exp_f32 v58, v58\n\tv_exp_f32 v59, v59\n\tv_exp_f32 v60, v60\n\tv_mfma_f32_16x16x32_bf16 a[36:39], a[72:75], v[116:119], a[36:39]\n\tv_exp_f32 v61, v61\n\tv_exp_f32 v62, v62\n\tv_exp_f32 v63, v63\n\tv_cvt_pk_bf16_f32 v124, v56, v57\n\tv_cvt_pk_bf16_f32 v125, v58, v59\n\tv_cvt_pk_bf16_f32 v126, v60, v61\n\tv_cvt_pk_bf16_f32 v127, v62, v63\n\ts_waitcnt lgkmcnt(0)\n\tv_mfma_f32_32x32x16_bf16 v[16:31], a[40:43], a[56:59], v[80:95]\n\tv_exp_f32 v32, v32\n\tv_exp_f32 v33, v33\n\tv_exp_f32 v34, v34\n\tv_mfma_f32_32x32x16_bf16 a[0:15], a[52:55], v[120:123], a[0:15]\n\tv_exp_f32 v35, v35\n\tv_exp_f32 v36, v36\n\tv_exp_f32 v37, v37\n\tv_mfma_f32_32x32x16_bf16 v[16:31], a[44:47], a[60:63], v[16:31]\n\tv_exp_f32 v38, v38\n\tv_exp_f32 v39, v39\n\tv_cvt_pk_bf16_f32 v112, v32, v33\n\tv_cvt_pk_bf16_f32 v113, v34, v35\n\tv_mfma_f32_16x16x32_bf16 a[32:35], a[72:75], v[120:123], a[32:35]\n\tv_cvt_pk_bf16_f32 v114, v36, v37\n\tv_cvt_pk_bf16_f32 v115, v38, v39\n\tv_exp_f32 v40, v40\n\tv_exp_f32 v41, v41\n\tv_mfma_f32_32x32x16_bf16 a[0:15], a[48:51], v[112:115], a[0:15]\n\tv_exp_f32 v42, v42\n\tv_exp_f32 v43, v43\n\tv_exp_f32 v44, v44\n\tv_mfma_f32_16x16x32_bf16 a[32:35], a[72:75], v[112:115], a[32:35]\n\tv_exp_f32 v45, v45\n\tv_exp_f32 v46, v46\n\tv_exp_f32 v47, v47\n\tv_cvt_pk_bf16_f32 v120, v40, v41\n\tv_cvt_pk_bf16_f32 v121, v42, v43\n\tv_cvt_pk_bf16_f32 v122, v44, v45\n\tv_cvt_pk_bf16_f32 v123, v46, v47\n\tv_mfma_f32_32x32x16_bf16 v[48:63], a[40:43], a[64:67], v[96:111]\n\tds_read_b128 a[40:43], %[a0] offset:12288\n\tv_exp_f32 v64, v64\n\tv_exp_f32 v65, v65\n\tv_exp_f32 v66, v66\n\tv_mfma_f32_32x32x16_bf16 a[16:31], a[52:55], v[124:127], a[16:31]\n\tds_read_b128 a[52:55], %[a0] offset:7168\n\tv_exp_f32 v67, v67\n\tv_exp_f32 v68, v68\n\tv_exp_f32 v69, v69\n\tv_mfma_f32_32x32x16_bf16 v[48:63], a[44:47], a[68:71], v[48:63]\n\tds_read_b128 a[44:47], %[a0] offset:13312\n\tv_exp_f32 v70, v70\n\tv_exp_f32 v71, v71\n\tv_cvt_pk_bf16_f32 v116, v64, v65\n\tv_cvt_pk_bf16_f32 v117, v66, v67\n\tv_mfma_f32_16x16x32_bf16 a[36:39], a[72:75], v[124:127], a[36:39]\n\tv_cvt_pk_bf16_f32 v118, v68, v69\n\tv_cvt_pk_bf16_f32 v119, v70, v71\n\tv_exp_f32 v72, v72\n\tv_exp_f32 v73, v73\n\tv_mfma_f32_32x32x16_bf16 a[16:31], a[48:51], v[116:119], a[16:31]\n\tds_read_b128 a[48:51], %[a0] offset:10240\n\tv_exp_f32 v74, v74\n\tv_exp_f32 v75, v75\n\tv_exp_f32 v76, v76\n\tv_mfma_f32_16x16x32_bf16 a[36:39], a[72:75], v[116:119], a[36:39]\n\tv_exp_f32 v77, v77\n\tv_exp_f32 v78, v78\n\tv_exp_f32 v79, v79\n\tv_cvt_pk_bf16_f32 v124, v72, v73\n\tv_cvt_pk_bf16_f32 v125, v74, v75\n\tv_cvt_pk_bf16_f32 v126, v76, v77\n\tv_cvt_pk_bf16_f32 v127, v78, v79\n\ts_barrier\n\ts_waitcnt lgkmcnt(0)\n\tv_mfma_f32_32x32x16_bf16 v[32:47], a[40:43], a[56:59], v[80:95]\n\tv_exp_f32 v16, v16\n\tv_exp_f32 v17, v17\n\tv_exp_f32 v18, v18\n\tv_mfma_f32_32x32x16_bf16 a[0:15], a[52:55], v[120:123], a[0:15]\n\tv_exp_f32 v19, v19\n\tv_exp_f32 v20, v20\n\tv_exp_f32 v21, v21\n\tv_mfma_f32_32x32x16_bf16 v[32:47], a[44:47], a[60:63], v[32:47]\n\tv_exp_f32 v22, v22\n\tv_exp_f32 v23, v23\n\tv_cvt_pk_bf16_f32 v112, v16, v17\n\tv_cvt_pk_bf16_f32 v113, v18, v19\n\tv_mfma_f32_16x16x32_bf16 a[32:35], a[72:75], v[120:123], a[32:35]\n\tv_cvt_pk_bf16_f32 v114, v20, v21\n\tv_cvt_pk_bf16_f32 v115, v22, v23\n\tv_exp_f32 v24, v24\n\tv_exp_f32 v25, v25\n\tv_mfma_f32_32x32x16_bf16 a[0:15], a[48:51], v[112:115], a[0:15]\n\tv_exp_f32 v26, v26\n\tv_exp_f32 v27, v27\n\tv_exp_f32 v28, v28\n\tv_mfma_f32_16x16x32_bf16 a[32:35], a[72:75], v[112:115], a[32:35]\n\tv_exp_f32 v29, v29\n\tv_exp_f32 v30, v30\n\tv_exp_f32 v31, v31\n\tv_cvt_pk_bf16_f32 v120, v24, v25\n\tv_cvt_pk_bf16_f32 v121, v26, v27\n\tv_cvt_pk_bf16_f32 v122, v28, v29\n\tv_cvt_pk_bf16_f32 v123, v30, v31\n\tv_mfma_f32_32x32x16_bf16 v[64:79], a[40:43], a[64:67], v[96:111]\n\tds_read_b128 a[40:43], %[a1] offset:0\n\tv_exp_f32 v48, v48\n\tv_exp_f32 v49, v49\n\tv_exp_f32 v50, v50\n\tv_mfma_f32_32x32x16_bf16 a[16:31], a[52:55], v[124:127], a[16:31]\n\tds_read_b128 a[52:55], %[a0] offset:11264\n\tv_exp_f32 v51, v51\n\tv_exp_f32 v52, v52\n\tv_exp_f32 v53, v53\n\tv_mfma_f32_32x32x16_bf16 v[64:79], a[44:47], a[68:71], v[64:79]\n\tds_read_b128 a[44:47], %[a1] offset:1024\n\tv_exp_f32 v54, v54\n\tv_exp_f32 v55, v55\n\tv_cvt_pk_bf16_f32 v116, v48, v49\n\tv_cvt_pk_bf16_f32 v117, v50, v51\n\tv_mfma_f32_16x16x32_bf16 a[36:39], a[72:75], v[124:127], a[36:39]\n\tv_cvt_pk_bf16_f32 v118, v52, v53\n\tv_cvt_pk_bf16_f32 v119, v54, v55\n\tv_exp_f32 v56, v56\n\tv_exp_f32 v57, v57\n\tv_mfma_f32_32x32x16_bf16 a[16:31], a[48:51], v[116:119], a[16:31]\n\tds_read_b128 a[48:51], %[a0] offset:14336\n\tv_exp_f32 v58, v58\n\tv_exp_f32 v59, v59\n\tv_exp_f32 v60, v60\n\tv_mfma_f32_16x16x32_bf16 a[36:39], a[72:75], v[116:119], a[36:39]\n\tv_exp_f32 v61, v61\n\tv_exp_f32 v62, v62\n\tv_exp_f32 v63, v63\n\tv_cvt_pk_bf16_f32 v124, v56, v57\n\tv_cvt_pk_bf16_f32 v125, v58, v59\n\tv_cvt_pk_bf16_f32 v126, v60, v61\n\tv_cvt_pk_bf16_f32 v127, v62, v63\n\ts_waitcnt lgkmcnt(0)\n\tv_mfma_f32_32x32x16_bf16 v[16:31], a[40:43], a[56:59], v[80:95]\n\tv_exp_f32 v32, v32\n\tv_exp_f32 v33, v33\n\tv_exp_f32 v34, v34\n\tv_mfma_f32_32x32x16_bf16 a[0:15], a[52:55], v[120:123], a[0:15]\n\tv_exp_f32 v35, v35\n\tv_exp_f32 v36, v36\n\tv_exp_f32 v37, v37\n\tv_mfma_f32_32x32x16_bf16 v[16:31], a[44:47], a[60:63], v[16:31]\n\tv_exp_f32 v38, v38\n\tv_exp_f32 v39, v39\n\tv_cvt_pk_bf16_f32 v112, v32, v33\n\tv_cvt_pk_bf16_f32 v113, v34, v35\n\tv_mfma_f32_16x16x32_bf16 a[32:35], a[72:75], v[120:123], a[32:35]\n\tv_cvt_pk_bf16_f32 v114, v36, v37\n\tv_cvt_pk_bf16_f32 v115, v38, v39\n\tv_exp_f32 v40, v40\n\tv_exp_f32 v41, v41\n\tv_mfma_f32_32x32x16_bf16 a[0:15], a[48:51], v[112:115], a[0:15]\n\tv_exp_f32 v42, v42\n\tv_exp_f32 v43, v43\n\tv_exp_f32 v44, v44\n\tv_mfma_f32_16x16x32_bf16 a[32:35], a[72:75], v[112:115], a[32:35]\n\tv_exp_f32 v45, v45\n\tv_exp_f32 v46, v46\n\tv_exp_f32 v47, v47\n\tv_cvt_pk_bf16_f32 v120, v40, v41\n\tv_cvt_pk_bf16_f32 v121, v42, v43\n\tv_cvt_pk_bf16_f32 v122, v44, v45\n\tv_cvt_pk_bf16_f32 v123, v46, v47\n\tv_mfma_f32_32x32x16_bf16 v[48:63], a[40:43], a[64:67], v[96:111]\n\tds_read_b128 a[40:43], %[a1] offset:4096\n\tv_exp_f32 v64, v64\n\tv_exp_f32 v65, v65\n\tv_exp_f32 v66, v66\n\tv_mfma_f32_32x32x16_bf16 a[16:31], a[52:55], v[124:127], a[16:31]\n\tds_read_b128 a[52:55], %[a0] offset:15360\n\tv_exp_f32 v67, v67\n\tv_exp_f32 v68, v68\n\tv_exp_f32 v69, v69\n\tv_mfma_f32_32x32x16_bf16 v[48:63], a[44:47], a[68:71], v[48:63]\n\tds_read_b128 a[44:47], %[a1] offset:5120\n\tv_exp_f32 v70, v70\n\tv_exp_f32 v71, v71\n\tv_cvt_pk_bf16_f32 v116, v64, v65\n\tv_cvt_pk_bf16_f32 v117, v66, v67\n\tv_mfma_f32_16x16x32_bf16 a[36:39], a[72:75], v[124:127], a[36:39]\n\tv_cvt_pk_bf16_f32 v118, v68, v69\n\tv_cvt_pk_bf16_f32 v119, v70, v71\n\tv_exp_f32 v72, v72\n\tv_exp_f32 v73, v73\n\tv_mfma_f32_32x32x16_bf16 a[16:31], a[48:51], v[116:119], a[16:31]\n\tds_read_b128 a[48:51], %[a1] offset:2048\n\tv_exp_f32 v74, v74\n\tv_exp_f32 v75, v75\n\tv_exp_f32 v76, v76\n\tv_mfma_f32_16x16x32_bf16 a[36:39], a[72:75], v[116:119], a[36:39]\n\tv_exp_f32 v77, v77\n\tv_exp_f32 v78, v78\n\tv_exp_f32 v79, v79\n\tv_cvt_pk_bf16_f32 v124, v72, v73\n\tv_cvt_pk_bf16_f32 v125, v74, v75\n\tv_cvt_pk_bf16_f32 v126, v76, v77\n\tv_cvt_pk_bf16_f32 v127, v78, v79\n\tv_mov_b32 %[a0], %[a1]\n\ts_add_u32 %[slot], %[slot], 1\n\ts_and_b32 %[slot], %[slot], 3\n\ts_cmp_eq_u32 %[slot], 0\n\ts_cselect_b32 %[dlt], %[cW], %[cS]\n\tv_add_u32 %[a1], %[dlt], %[a1]\n\ts_sub_u32 %[cnt], %[cnt], 1\n\ts_cmp_lg_u32 %[cnt], 0\n\ts_cbranch_scc1 AT_loop%=\n\ts_waitcnt lgkmcnt(0)\n\tv_mfma_f32_32x32x16_bf16 v[32:47], a[40:43], a[56:59], v[80:95]\n\tv_exp_f32 v16, v16\n\tv_exp_f32 v17, v17\n\tv_exp_f32 v18, v18\n\tv_mfma_f32_32x32x16_bf16 a[0:15], a[52:55], v[120:123], a[0:15]\n\tv_exp_f32 v19, v19\n\tv_exp_f32 v20, v20\n\tv_exp_f32 v21, v21\n\tv_mfma_f32_32x32x16_bf16 v[32:47], a[44:47], a[60:63], v[32:47]\n\tv_exp_f32 v22, v22\n\tv_exp_f32 v23, v23\n\tv_cvt_pk_bf16_f32 v112, v16, v17\n\tv_cvt_pk_bf16_f32 v113, v18, v19\n\tv_mfma_f32_16x16x32_bf16 a[32:35], a[72:75], v[120:123], a[32:35]\n\tv_cvt_pk_bf16_f32 v114, v20, v21\n\tv_cvt_pk_bf16_f32 v115, v22, v23\n\tv_exp_f32 v24, v24\n\tv_exp_f32 v25, v25\n\tv_mfma_f32_32x32x16_bf16 a[0:15], a[48:51], v[112:115], a[0:15]\n\tv_exp_f32 v26, v26\n\tv_exp_f32 v27, v27\n\tv_exp_f32 v28, v28\n\tv_mfma_f32_16x16x32_bf16 a[32:35], a[72:75], v[112:115], a[32:35]\n\tv_exp_f32 v29, v29\n\tv_exp_f32 v30, v30\n\tv_exp_f32 v31, v31\n\tv_cvt_pk_bf16_f32 v120, v24, v25\n\tv_cvt_pk_bf16_f32 v121, v26, v27\n\tv_cvt_pk_bf16_f32 v122, v28, v29\n\tv_cvt_pk_bf16_f32 v123, v30, v31\n\tv_mfma_f32_32x32x16_bf16 v[64:79], a[40:43], a[64:67], v[96:111]\n\tds_read_b128 a[40:43], %[a0] offset:8192\n\tv_exp_f32 v48, v48\n\tv_exp_f32 v49, v49\n\tv_exp_f32 v50, v50\n\tv_mfma_f32_32x32x16_bf16 a[16:31], a[52:55], v[124:127], a[16:31]\n\tds_read_b128 a[52:55], %[a0] offset:3072\n\tv_exp_f32 v51, v51\n\tv_exp_f32 v52, v52\n\tv_exp_f32 v53, v53\n\tv_mfma_f32_32x32x16_bf16 v[64:79], a[44:47], a[68:71], v[64:79]\n\tds_read_b128 a[44:47], %[a0] offset:9216\n\tv_exp_f32 v54, v54\n\tv_exp_f32 v55, v55\n\tv_cvt_pk_bf16_f32 v116, v48, v49\n\tv_cvt_pk_bf16_f32 v117, v50, v51\n\tv_mfma_f32_16x16x32_bf16 a[36:39], a[72:75], v[124:127], a[36:39]\n\tv_cvt_pk_bf16_f32 v118, v52, v53\n\tv_cvt_pk_bf16_f32 v119, v54, v55\n\tv_exp_f32 v56, v56\n\tv_exp_f32 v57, v57\n\tv_mfma_f32_32x32x16_bf16 a[16:31], a[48:51], v[116:119], a[16:31]\n\tds_read_b128 a[48:51], %[a0] offset:6144\n\tv_exp_f32 v58, v58\n\tv_exp_f32 v59, v59\n\tv_exp_f32 v60, v60\n\tv_mfma_f32_16x16x32_bf16 a[36:39], a[72:75], v[116:119], a[36:39]\n\tv_exp_f32 v61, v61\n\tv_exp_f32 v62, v62\n\tv_exp_f32 v63, v63\n\tv_cvt_pk_bf16_f32 v124, v56, v57\n\tv_cvt_pk_bf16_f32 v125, v58, v59\n\tv_cvt_pk_bf16_f32 v126, v60, v61\n\tv_cvt_pk_bf16_f32 v127, v62, v63\n\ts_waitcnt lgkmcnt(0)\n\tv_mfma_f32_32x32x16_bf16 v[16:31], a[40:43], a[56:59], v[80:95]\n\tv_exp_f32 v32, v32\n\tv_exp_f32 v33, v33\n\tv_exp_f32 v34, v34\n\tv_mfma_f32_32x32x16_bf16 a[0:15], a[52:55], v[120:123], a[0:15]\n\tv_exp_f32 v35, v35\n\tv_exp_f32 v36, v36\n\tv_exp_f32 v37, v37\n\tv_mfma_f32_32x32x16_bf16 v[16:31], a[44:47], a[60:63], v[16:31]\n\tv_exp_f32 v38, v38\n\tv_exp_f32 v39, v39\n\tv_cvt_pk_bf16_f32 v112, v32, v33\n\tv_cvt_pk_bf16_f32 v113, v34, v35\n\tv_mfma_f32_16x16x32_bf16 a[32:35], a[72:75], v[120:123], a[32:35]\n\tv_cvt_pk_bf16_f32 v114, v36, v37\n\tv_cvt_pk_bf16_f32 v115, v38, v39\n\tv_exp_f32 v40, v40\n\tv_exp_f32 v41, v41\n\tv_mfma_f32_32x32x16_bf16 a[0:15], a[48:51], v[112:115], a[0:15]\n\tv_exp_f32 v42, v42\n\tv_exp_f32 v43, v43\n\tv_exp_f32 v44, v44\n\tv_mfma_f32_16x16x32_bf16 a[32:35], a[72:75], v[112:115], a[32:35]\n\tv_exp_f32 v45, v45\n\tv_exp_f32 v46, v46\n\tv_exp_f32 v47, v47\n\tv_cvt_pk_bf16_f32 v120, v40, v41\n\tv_cvt_pk_bf16_f32 v121, v42, v43\n\tv_cvt_pk_bf16_f32 v122, v44, v45\n\tv_cvt_pk_bf16_f32 v123, v46, v47\n\tv_mfma_f32_32x32x16_bf16 v[48:63], a[40:43], a[64:67], v[96:111]\n\tds_read_b128 a[40:43], %[a0] offset:12288\n\tv_exp_f32 v64, v64\n\tv_exp_f32 v65, v65\n\tv_exp_f32 v66, v66\n\tv_mfma_f32_32x32x16_bf16 a[16:31], a[52:55], v[124:127], a[16:31]\n\tds_read_b128 a[52:55], %[a0] offset:7168\n\tv_exp_f32 v67, v67\n\tv_exp_f32 v68, v68\n\tv_exp_f32 v69, v69\n\tv_mfma_f32_32x32x16_bf16 v[48:63], a[44:47], a[68:71], v[48:63]\n\tds_read_b128 a[44:47], %[a0] offset:13312\n\tv_exp_f32 v70, v70\n\tv_exp_f32 v71, v71\n\tv_cvt_pk_bf16_f32 v116, v64, v65\n\tv_cvt_pk_bf16_f32 v117, v66, v67\n\tv_mfma_f32_16x16x32_bf16 a[36:39], a[72:75], v[124:127], a[36:39]\n\tv_cvt_pk_bf16_f32 v118, v68, v69\n\tv_cvt_pk_bf16_f32 v119, v70, v71\n\tv_exp_f32 v72, v72\n\tv_exp_f32 v73, v73\n\tv_mfma_f32_32x32x16_bf16 a[16:31], a[48:51], v[116:119], a[16:31]\n\tds_read_b128 a[48:51], %[a0] offset:10240\n\tv_exp_f32 v74, v74\n\tv_exp_f32 v75, v75\n\tv_exp_f32 v76, v76\n\tv_mfma_f32_16x16x32_bf16 a[36:39], a[72:75], v[116:119], a[36:39]\n\tv_exp_f32 v77, v77\n\tv_exp_f32 v78, v78\n\tv_exp_f32 v79, v79\n\tv_cvt_pk_bf16_f32 v124, v72, v73\n\tv_cvt_pk_bf16_f32 v125, v74, v75\n\tv_cvt_pk_bf16_f32 v126, v76, v77\n\tv_cvt_pk_bf16_f32 v127, v78, v79\n\ts_waitcnt lgkmcnt(0)\n\tv_mfma_f32_32x32x16_bf16 v[32:47], a[40:43], a[56:59], v[80:95]\n\tv_exp_f32 v16, v16\n\tv_exp_f32 v17, v17\n\tv_exp_f32 v18, v18\n\tv_mfma_f32_32x32x16_bf16 a[0:15], a[52:55], v[120:123], a[0:15]\n\tv_exp_f32 v19, v19\n\tv_exp_f32 v20, v20\n\tv_exp_f32 v21, v21\n\tv_mfma_f32_32x32x16_bf16 v[32:47], a[44:47], a[60:63], v[32:47]\n\tv_exp_f32 v22, v22\n\tv_exp_f32 v23, v23\n\tv_cvt_pk_bf16_f32 v112, v16, v17\n\tv_cvt_pk_bf16_f32 v113, v18, v19\n\tv_mfma_f32_16x16x32_bf16 a[32:35], a[72:75], v[120:123], a[32:35]\n\tv_cvt_pk_bf16_f32 v114, v20, v21\n\tv_cvt_pk_bf16_f32 v115, v22, v23\n\tv_exp_f32 v24, v24\n\tv_exp_f32 v25, v25\n\tv_mfma_f32_32x32x16_bf16 a[0:15], a[48:51], v[112:115], a[0:15]\n\tv_exp_f32 v26, v26\n\tv_exp_f32 v27, v27\n\tv_exp_f32 v28, v28\n\tv_mfma_f32_16x16x32_bf16 a[32:35], a[72:75], v[112:115], a[32:35]\n\tv_exp_f32 v29, v29\n\tv_exp_f32 v30, v30\n\tv_exp_f32 v31, v31\n\tv_cvt_pk_bf16_f32 v120, v24, v25\n\tv_cvt_pk_bf16_f32 v121, v26, v27\n\tv_cvt_pk_bf16_f32 v122, v28, v29\n\tv_cvt_pk_bf16_f32 v123, v30, v31\n\tv_mfma_f32_32x32x16_bf16 v[64:79], a[40:43], a[64:67], v[96:111]\n\tv_exp_f32 v48, v48\n\tv_exp_f32 v49, v49\n\tv_exp_f32 v50, v50\n\tv_mfma_f32_32x32x16_bf16 a[16:31], a[52:55], v[124:127], a[16:31]\n\tds_read_b128 a[52:55], %[a0] offset:11264\n\tv_exp_f32 v51, v51\n\tv_exp_f32 v52, v52\n\tv_exp_f32 v53, v53\n\tv_mfma_f32_32x32x16_bf16 v[64:79], a[44:47], a[68:71], v[64:79]\n\tv_exp_f32 v54, v54\n\tv_exp_f32 v55, v55\n\tv_cvt_pk_bf16_f32 v116, v48, v49\n\tv_cvt_pk_bf16_f32 v117, v50, v51\n\tv_mfma_f32_16x16x32_bf16 a[36:39], a[72:75], v[124:127], a[36:39]\n\tv_cvt_pk_bf16_f32 v118, v52, v53\n\tv_cvt_pk_bf16_f32 v119, v54, v55\n\tv_exp_f32 v56, v56\n\tv_exp_f32 v57, v57\n\tv_mfma_f32_32x32x16_bf16 a[16:31], a[48:51], v[116:119], a[16:31]\n\tds_read_b128 a[48:51], %[a0] offset:14336\n\tv_exp_f32 v58, v58\n\tv_exp_f32 v59, v59\n\tv_exp_f32 v60, v60\n\tv_mfma_f32_16x16x32_bf16 a[36:39], a[72:75], v[116:119], a[36:39]\n\tv_exp_f32 v61, v61\n\tv_exp_f32 v62, v62\n\tv_exp_f32 v63, v63\n\tv_cvt_pk_bf16_f32 v124, v56, v57\n\tv_cvt_pk_bf16_f32 v125, v58, v59\n\tv_cvt_pk_bf16_f32 v126, v60, v61\n\tv_cvt_pk_bf16_f32 v127, v62, v63\n\ts_waitcnt lgkmcnt(0)\n\tv_exp_f32 v32, v32\n\tv_exp_f32 v33, v33\n\tv_exp_f32 v34, v34\n\tv_mfma_f32_32x32x16_bf16 a[0:15], a[52:55], v[120:123], a[0:15]\n\tv_exp_f32 v35, v35\n\tv_exp_f32 v36, v36\n\tv_exp_f32 v37, v37\n\tv_exp_f32 v38, v38\n\tv_exp_f32 v39, v39\n\tv_cvt_pk_bf16_f32 v112, v32, v33\n\tv_cvt_pk_bf16_f32 v113, v34, v35\n\tv_mfma_f32_16x16x32_bf16 a[32:35], a[72:75], v[120:123], a[32:35]\n\tv_cvt_pk_bf16_f32 v114, v36, v37\n\tv_cvt_pk_bf16_f32 v115, v38, v39\n\tv_exp_f32 v40, v40\n\tv_exp_f32 v41, v41\n\tv_mfma_f32_32x32x16_bf16 a[0:15], a[48:51], v[112:115], a[0:15]\n\tv_exp_f32 v42, v42\n\tv_exp_f32 v43, v43\n\tv_exp_f32 v44, v44\n\tv_mfma_f32_16x16x32_bf16 a[32:35], a[72:75], v[112:115], a[32:35]\n\tv_exp_f32 v45, v45\n\tv_exp_f32 v46, v46\n\tv_exp_f32 v47, v47\n\tv_cvt_pk_bf16_f32 v120, v40, v41\n\tv_cvt_pk_bf16_f32 v121, v42, v43\n\tv_cvt_pk_bf16_f32 v122, v44, v45\n\tv_cvt_pk_bf16_f32 v123, v46, v47\n\tv_exp_f32 v64, v64\n\tv_exp_f32 v65, v65\n\tv_exp_f32 v66, v66\n\tv_mfma_f32_32x32x16_bf16 a[16:31], a[52:55], v[124:127], a[16:31]\n\tds_read_b128 a[52:55], %[a0] offset:15360\n\tv_exp_f32 v67, v67\n\tv_exp_f32 v68, v68\n\tv_exp_f32 v69, v69\n\tv_exp_f32 v70, v70\n\tv_exp_f32 v71, v71\n\tv_cvt_pk_bf16_f32 v116, v64, v65\n\tv_cvt_pk_bf16_f32 v117, v66, v67\n\tv_mfma_f32_16x16x32_bf16 a[36:39], a[72:75], v[124:127], a[36:39]\n\tv_cvt_pk_bf16_f32 v118, v68, v69\n\tv_cvt_pk_bf16_f32 v119, v70, v71\n\tv_exp_f32 v72, v72\n\tv_exp_f32 v73, v73\n\tv_mfma_f32_32x32x16_bf16 a[16:31], a[48:51], v[116:119], a[16:31]\n\tv_exp_f32 v74, v74\n\tv_exp_f32 v75, v75\n\tv_exp_f32 v76, v76\n\tv_mfma_f32_16x16x32_bf16 a[36:39], a[72:75], v[116:119], a[36:39]\n\tv_exp_f32 v77, v77\n\tv_exp_f32 v78, v78\n\tv_exp_f32 v79, v79\n\tv_cvt_pk_bf16_f32 v124, v72, v73\n\tv_cvt_pk_bf16_f32 v125, v74, v75\n\tv_cvt_pk_bf16_f32 v126, v76, v77\n\tv_cvt_pk_bf16_f32 v127, v78, v79\n\ts_waitcnt lgkmcnt(0)\n\ts_nop 1\n\tv_mfma_f32_32x32x16_bf16 a[0:15], a[52:55], v[120:123], a[0:15]\n\tv_mfma_f32_16x16x32_bf16 a[32:35], a[72:75], v[120:123], a[32:35]\n\ts_nop 1\n\tv_mfma_f32_32x32x16_bf16 a[16:31], a[52:55], v[124:127], a[16:31]\n\tv_mfma_f32_16x16x32_bf16 a[36:39], a[72:75], v[124:127], a[36:39]\n\ts_nop 15\n\ts_nop 7"
#define ATN_CLOB "v8","v9","v10","v11","v12","v13","v14","v15","v16","v17","v18","v19","v20","v21","v22","v23","v24","v25","v26","v27","v28","v29","v30","v31","v32","v33","v34","v35","v36","v37","v38","v39","v40","v41","v42","v43","v44","v45","v46","v47","v48","v49","v50","v51","v52","v53","v54","v55","v56","v57","v58","v59","v60","v61","v62","v63","v64","v65","v66","v67","v68","v69","v70","v71","v72","v73","v74","v75","v76","v77","v78","v79","v80","v81","v82","v83","v84","v85","v86","v87","v88","v89","v90","v91","v92","v93","v94","v95","v96","v97","v98","v99","v100","v101","v102","v103","v104","v105","v106","v107","v108","v109","v110","v111","v112","v113","v114","v115","v116","v117","v118","v119","v120","v121","v122","v123","v124","v125","v126","v127","a0","a1","a2","a3","a4","a5","a6","a7","a8","a9","a10","a11","a12","a13","a14","a15","a16","a17","a18","a19","a20","a21","a22","a23","a24","a25","a26","a27","a28","a29","a30","a31","a32","a33","a34","a35","a36","a37","a38","a39","a40","a41","a42","a43","a44","a45","a46","a47","a48","a49","a50","a51","a52","a53","a54","a55","a56","a57","a58","a59","a60","a61","a62","a63","a64","a65","a66","a67","a68","a69","a70","a71","a72","a73","a74","a75","a76","a77","a78","a79","a80","a81","a82","a83","a84","a85","a86","a87","a88","a89","a90","a91","a92","a93","a94","a95","a96","a97","a98","a99","a100","a101","a102","a103","a104","a105","a106","a107","a108","a109","a110","a111","a112","a113","a114","a115","a116","a117","a118","a119","a120","a121","a122","a123","a124","a125","a126","a127"
#define ATL_0_ASM "s_mov_b64 s[40:41], %[pK]\n\ts_mov_b32 s60, %[lds0]\n\ts_mov_b32 s61, 0\n\ts_add_u32 m0, s60, 0\n\ts_nop 0\n\tglobal_load_lds_dwordx4 %[vA], s[40:41] offset:0\n\tglobal_load_lds_dwordx4 %[vA], s[40:41] offset:1024\n\tglobal_load_lds_dwordx4 %[vA], s[40:41] offset:2048\n\tglobal_load_lds_dwordx4 %[vA], s[40:41] offset:3072\n\ts_add_u32 s40, s40, 16384\n\ts_addc_u32 s41, s41, 0\n\ts_add_u32 s61, s61, 1\n\ts_and_b32 s61, s61, 3\n\ts_lshl_b32 s62, s61, 14\n\ts_add_u32 s60, %[lds0], s62\n\ts_add_u32 m0, s60, 0\n\ts_nop 0\n\tglobal_load_lds_dwordx4 %[vA], s[40:41] offset:0\n\tglobal_load_lds_dwordx4 %[vA], s[40:41] offset:1024\n\tglobal_load_lds_dwordx4 %[vA], s[40:41] offset:2048\n\tglobal_load_lds_dwordx4 %[vA], s[40:41] offset:3072\n\ts_add_u32 s40, s40, 16384\n\ts_addc_u32 s41, s41, 0\n\ts_add_u32 s61, s61, 1\n\ts_and_b32 s61, s61, 3\n\ts_lshl_b32 s62, s61, 14\n\ts_add_u32 s60, %[lds0], s62\n\ts_add_u32 m0, s60, 0\n\ts_nop 0\n\tglobal_load_lds_dwordx4 %[vA], s[40:41] offset:0\n\tglobal_load_lds_dwordx4 %[vA], s[40:41] offset:1024\n\tglobal_load_lds_dwordx4 %[vA], s[40:41] offset:2048\n\tglobal_load_lds_dwordx4 %[vA], s[40:41] offset:3072\n\ts_add_u32 s40, s40, 16384\n\ts_addc_u32 s41, s41, 0\n\ts_add_u32 s61, s61, 1\n\ts_and_b32 s61, s61, 3\n\ts_lshl_b32 s62, s61, 14\n\ts_add_u32 s60, %[lds0], s62\n\ts_waitcnt vmcnt(8)\n\ts_barrier\n\ts_waitcnt vmcnt(4)\n\ts_barrier\n\ts_add_u32 m0, s60, 0\n\ts_nop 0\n\tglobal_load_lds_dwordx4 %[vA], s[40:41] offset:0\n\tglobal_load_lds_dwordx4 %[vA], s[40:41] offset:1024\n\tglobal_load_lds_dwordx4 %[vA], s[40:41] offset:2048\n\tglobal_load_lds_dwordx4 %[vA], s[40:41] offset:3072\n\ts_add_u32 s40, s40, 16384\n\ts_addc_u32 s41, s41, 0\n\ts_add_u32 s61, s61, 1\n\ts_and_b32 s61, s61, 3\n\ts_lshl_b32 s62, s61, 14\n\ts_add_u32 s60, %[lds0], s62\n\tds_read_b128 v[48:51], %[hs] offset:0\n\tds_read_b128 v[52:55], %[hs] offset:1024\n\tds_read_b128 v[56:59], %[hs] offset:2048\n\ts_waitcnt lgkmcnt(0)\n\ts_mov_b32 s70, 0\n\tbuffer_store_dwordx4 v[48:51], %[hoff], %[rh], s70 offen sc1\n\ts_mov_b32 s70, 114688\n\tbuffer_store_dwordx4 v[52:55], %[hoff], %[rh], s70 offen sc1\n\ts_mov_b32 s70, 229376\n\tbuffer_store_dwordx4 v[56:59], %[hoff], %[rh], s70 offen sc1\n\ts_waitcnt vmcnt(7)\n\ts_barrier\n\ts_add_u32 m0, s60, 0\n\ts_nop 0\n\tglobal_load_lds_dwordx4 %[vA], s[40:41] offset:0\n\tglobal_load_lds_dwordx4 %[vA], s[40:41] offset:1024\n\tglobal_load_lds_dwordx4 %[vA], s[40:41] offset:2048\n\tglobal_load_lds_dwordx4 %[vA], s[40:41] offset:3072\n\ts_add_u32 s40, s40, 16384\n\ts_addc_u32 s41, s41, 0\n\ts_add_u32 s61, s61, 1\n\ts_and_b32 s61, s61, 3\n\ts_lshl_b32 s62, s61, 14\n\ts_add_u32 s60, %[lds0], s62\n\tds_read_b128 v[48:51], %[hs] offset:3072\n\tds_read_b128 v[52:55], %[hs] offset:4096\n\tds_read_b128 v[56:59], %[hs] offset:5120\n\ts_waitcnt lgkmcnt(0)\n\ts_mov_b32 s70, 344064\n\tbuffer_store_dwordx4 v[48:51], %[hoff], %[rh], s70 offen sc1\n\ts_mov_b32 s70, 1024\n\tbuffer_store_dwordx4 v[52:55], %[hoff], %[rh], s70 offen sc1\n\ts_mov_b32 s70, 115712\n\tbuffer_store_dwordx4 v[56:59], %[hoff], %[rh], s70 offen sc1\n\ts_waitcnt vmcnt(10)\n\ts_barrier\n\ts_add_u32 m0, s60, 0\n\ts_nop 0\n\tglobal_load_lds_dwordx4 %[vA], s[40:41] offset:0\n\tglobal_load_lds_dwordx4 %[vA], s[40:41] offset:1024\n\tglobal_load_lds_dwordx4 %[vA], s[40:41] offset:2048\n\tglobal_load_lds_dwordx4 %[vA], s[40:41] offset:3072\n\ts_add_u32 s40, s40, 16384\n\ts_addc_u32 s41, s41, 0\n\ts_add_u32 s61, s61, 1\n\ts_and_b32 s61, s61, 3\n\ts_lshl_b32 s62, s61, 14\n\ts_add_u32 s60, %[lds0], s62\n\tds_read_b128 v[48:51], %[hs] offset:6144\n\tds_read_b128 v[52:55], %[hs] offset:7168\n\tds_read_b128 v[56:59], %[hs] offset:8192\n\ts_waitcnt lgkmcnt(0)\n\ts_mov_b32 s70, 230400\n\tbuffer_store_dwordx4 v[48:51], %[hoff], %[rh], s70 offen sc1\n\ts_mov_b32 s70, 345088\n\tbuffer_store_dwordx4 v[52:55], %[hoff], %[rh], s70 offen sc1\n\ts_mov_b32 s70, 2048\n\tbuffer_store_dwordx4 v[56:59], %[hoff], %[rh], s70 offen sc1\n\ts_waitcnt vmcnt(10)\n\ts_barrier\n\ts_add_u32 m0, s60, 0\n\ts_nop 0\n\tglobal_load_lds_dwordx4 %[vA], s[40:41] offset:0\n\tglobal_load_lds_dwordx4 %[vA], s[40:41] offset:1024\n\tglobal_load_lds_dwordx4 %[vA], s[40:41] offset:2048\n\tglobal_load_lds_dwordx4 %[vA], s[40:41] offset:3072\n\ts_add_u32 s40, s40, 16384\n\ts_addc_u32 s41, s41, 0\n\ts_add_u32 s61, s61, 1\n\ts_and_b32 s61, s61, 3\n\ts_lshl_b32 s62, s61, 14\n\ts_add_u32 s60, %[lds0], s62\n\tds_read_b128 v[48:51], %[hs] offset:9216\n\tds_read_b128 v[52:55], %[hs] offset:10240\n\tds_read_b128 v[56:59], %[hs] offset:11264\n\ts_waitcnt lgkmcnt(0)\n\ts_mov_b32 s70, 116736\n\tbuffer_store_dwordx4 v[48:51], %[hoff], %[rh], s70 offen sc1\n\ts_mov_b32 s70, 231424\n\tbuffer_store_dwordx4 v[52:55], %[hoff], %[rh], s70 offen sc1\n\ts_mov_b32 s70, 346112\n\tbuffer_store_dwordx4 v[56:59], %[hoff], %[rh], s70 offen sc1\n\ts_waitcnt vmcnt(10)\n\ts_barrier\n\ts_add_u32 m0, s60, 0\n\ts_nop 0\n\tglobal_load_lds_dwordx4 %[vA], s[40:41] offset:0\n\tglobal_load_lds_dwordx4 %[vA], s[40:41] offset:1024\n\tglobal_load_lds_dwordx4 %[vA], s[40:41] offset:2048\n\tglobal_load_lds_dwordx4 %[vA], s[40:41] offset:3072\n\ts_add_u32 s40, s40, 16384\n\ts_addc_u32 s41, s41, 0\n\ts_add_u32 s61, s61, 1\n\ts_and_b32 s61, s61, 3\n\ts_lshl_b32 s62, s61, 14\n\ts_add_u32 s60, %[lds0], s62\n\tds_read_b64 v[48:49], %[hs2] offset:0\n\tds_read_b64 v[52:53], %[hs2] offset:512\n\tds_read_b64 v[56:57], %[hs2] offset:1024\n\ts_waitcnt lgkmcnt(0)\n\ts_mov_b32 s70, 0\n\tbuffer_store_dwordx2 v[48:49], %[hoff2], %[rh], s70 offen sc1\n\ts_mov_b32 s70, 114688\n\tbuffer_store_dwordx2 v[52:53], %[hoff2], %[rh], s70 offen sc1\n\ts_mov_b32 s70, 229376\n\tbuffer_store_dwordx2 v[56:57], %[hoff2], %[rh], s70 offen sc1\n\ts_waitcnt vmcnt(10)\n\ts_barrier\n\ts_add_u32 m0, s60, 0\n\ts_nop 0\n\tglobal_load_lds_dwordx4 %[vA], s[40:41] offset:0\n\tglobal_load_lds_dwordx4 %[vA], s[40:41] offset:1024\n\tglobal_load_lds_dwordx4 %[vA], s[40:41] offset:2048\n\tglobal_load_lds_dwordx4 %[vA], s[40:41] offset:3072\n\ts_add_u32 s40, s40, 16384\n\ts_addc_u32 s41, s41, 0\n\ts_add_u32 s61, s61, 1\n\ts_and_b32 s61, s61, 3\n\ts_lshl_b32 s62, s61, 14\n\ts_add_u32 s60, %[lds0], s62\n\tds_read_b64 v[48:49], %[hs2] offset:1536\n\ts_waitcnt lgkmcnt(0)\n\ts_mov_b32 s70, 344064\n\tbuffer_store_dwordx2 v[48:49], %[hoff2], %[rh], s70 offen sc1\n\ts_waitcnt vmcnt(8)\n\ts_barrier\n\ts_add_u32 m0, s60, 0\n\ts_nop 0\n\tglobal_load_lds_dwordx4 %[vA], s[40:41] offset:0\n\tglobal_load_lds_dwordx4 %[vA], s[40:41] offset:1024\n\tglobal_load_lds_dwordx4 %[vA], s[40:41] offset:2048\n\tglobal_load_lds_dwordx4 %[vA], s[40:41] offset:3072\n\ts_add_u32 s40, s40, 16384\n\ts_addc_u32 s41, s41, 0\n\ts_add_u32 s61, s61, 1\n\ts_and_b32 s61, s61, 3\n\ts_lshl_b32 s62, s61, 14\n\ts_add_u32 s60, %[lds0], s62\n\tglobal_load_dword v40, %[p0], off\n\tglobal_load_dword v41, %[p1], off\n\tglobal_load_dword v42, %[p2], off\n\ts_waitcnt vmcnt(8)\n\ts_barrier\n\ts_add_u32 m0, s60, 0\n\ts_nop 0\n\tglobal_load_lds_dwordx4 %[vA], s[40:41] offset:0\n\tglobal_load_lds_dwordx4 %[vA], s[40:41] offset:1024\n\tglobal_load_lds_dwordx4 %[vA], s[40:41] offset:2048\n\tglobal_load_lds_dwordx4 %[vA], s[40:41] offset:3072\n\ts_add_u32 s40, s40, 16384\n\ts_addc_u32 s41, s41, 0\n\ts_add_u32 s61, s61, 1\n\ts_and_b32 s61, s61, 3\n\ts_lshl_b32 s62, s61, 14\n\ts_add_u32 s60, %[lds0], s62\n\ts_waitcnt vmcnt(7)\n\ts_barrier\n\ts_add_u32 m0, s60, 0\n\ts_nop 0\n\tglobal_load_lds_dwordx4 %[vA], s[40:41] offset:0\n\tglobal_load_lds_dwordx4 %[vA], s[40:41] offset:1024\n\tglobal_load_lds_dwordx4 %[vA], s[40:41] offset:2048\n\tglobal_load_lds_dwordx4 %[vA], s[40:41] offset:3072\n\ts_add_u32 s40, s40, 16384\n\ts_addc_u32 s41, s41, 0\n\ts_add_u32 s61, s61, 1\n\ts_and_b32 s61, s61, 3\n\ts_lshl_b32 s62, s61, 14\n\ts_add_u32 s60, %[lds0], s62\n\ts_waitcnt vmcnt(4)\n\ts_barrier\n\ts_add_u32 m0, s60, 0\n\ts_nop 0\n\tglobal_load_lds_dwordx4 %[vA], s[40:41] offset:0\n\tglobal_load_lds_dwordx4 %[vA], s[40:41] offset:1024\n\tglobal_load_lds_dwordx4 %[vA], s[40:41] offset:2048\n\tglobal_load_lds_dwordx4 %[vA], s[40:41] offset:3072\n\ts_add_u32 s40, s40, 16384\n\ts_addc_u32 s41, s41, 0\n\ts_add_u32 s61, s61, 1\n\ts_and_b32 s61, s61, 3\n\ts_lshl_b32 s62, s61, 14\n\ts_add_u32 s60, %[lds0], s62\n\ts_waitcnt vmcnt(4)\n\ts_barrier\n\ts_add_u32 m0, s60, 0\n\ts_nop 0\n\tglobal_load_lds_dwordx4 %[vA], s[40:41] offset:0\n\tglobal_load_lds_dwordx4 %[vA], s[40:41] offset:1024\n\tglobal_load_lds_dwordx4 %[vA], s[40:41] offset:2048\n\tglobal_load_lds_dwordx4 %[vA], s[40:41] offset:3072\n\ts_add_u32 s40, s40, 16384\n\ts_addc_u32 s41, s41, 0\n\ts_add_u32 s61, s61, 1\n\ts_and_b32 s61, s61, 3\n\ts_lshl_b32 s62, s61, 14\n\ts_add_u32 s60, %[lds0], s62\n\ts_waitcnt vmcnt(4)\n\ts_barrier\n\ts_add_u32 m0, s60, 0\n\ts_nop 0\n\tglobal_load_lds_dwordx4 %[vA], s[40:41] offset:0\n\tglobal_load_lds_dwordx4 %[vA], s[40:41] offset:1024\n\tglobal_load_lds_dwordx4 %[vA], s[40:41] offset:2048\n\tglobal_load_lds_dwordx4 %[vA], s[40:41] offset:3072\n\ts_add_u32 s40, s40, 16384\n\ts_addc_u32 s41, s41, 0\n\ts_add_u32 s61, s61, 1\n\ts_and_b32 s61, s61, 3\n\ts_lshl_b32 s62, s61, 14\n\ts_add_u32 s60, %[lds0], s62\n\ts_waitcnt vmcnt(4)\n\ts_barrier\n\ts_add_u32 m0, s60, 0\n\ts_nop 0\n\tglobal_load_lds_dwordx4 %[vA], s[40:41] offset:0\n\tglobal_load_lds_dwordx4 %[vA], s[40:41] offset:1024\n\tglobal_load_lds_dwordx4 %[vA], s[40:41] offset:2048\n\tglobal_load_lds_dwordx4 %[vA], s[40:41] offset:3072\n\ts_add_u32 s40, s40, 16384\n\ts_addc_u32 s41, s41, 0\n\ts_add_u32 s61, s61, 1\n\ts_and_b32 s61, s61, 3\n\ts_lshl_b32 s62, s61, 14\n\ts_add_u32 s60, %[lds0], s62\n\ts_waitcnt vmcnt(4)\n\ts_barrier\n\ts_waitcnt vmcnt(0)\n\ts_barrier"
#define ATL_0_CLOB "s40","s41","s60","s61","s62","s66","s70","s71","v40","v41","v42","v43","v44","v48","v49","v50","v51","v52","v53","v54","v55","v56","v57","v58","v59"
#define ATL_1_ASM "s_mov_b64 s[40:41], %[pK]\n\ts_mov_b32 s60, %[lds0]\n\ts_mov_b32 s61, 0\n\ts_add_u32 m0, s60, 4096\n\ts_nop 0\n\tglobal_load_lds_dwordx4 %[vA], s[40:41] offset:0\n\tglobal_load_lds_dwordx4 %[vA], s[40:41] offset:1024\n\tglobal_load_lds_dwordx4 %[vA], s[40:41] offset:2048\n\tglobal_load_lds_dwordx4 %[vA], s[40:41] offset:3072\n\ts_add_u32 s40, s40, 16384\n\ts_addc_u32 s41, s41, 0\n\ts_add_u32 s61, s61, 1\n\ts_and_b32 s61, s61, 3\n\ts_lshl_b32 s62, s61, 14\n\ts_add_u32 s60, %[lds0], s62\n\ts_add_u32 m0, s60, 4096\n\ts_nop 0\n\tglobal_load_lds_dwordx4 %[vA], s[40:41] offset:0\n\tglobal_load_lds_dwordx4 %[vA], s[40:41] offset:1024\n\tglobal_load_lds_dwordx4 %[vA], s[40:41] offset:2048\n\tglobal_load_lds_dwordx4 %[vA], s[40:41] offset:3072\n\ts_add_u32 s40, s40, 16384\n\ts_addc_u32 s41, s41, 0\n\ts_add_u32 s61, s61, 1\n\ts_and_b32 s61, s61, 3\n\ts_lshl_b32 s62, s61, 14\n\ts_add_u32 s60, %[lds0], s62\n\ts_add_u32 m0, s60, 4096\n\ts_nop 0\n\tglobal_load_lds_dwordx4 %[vA], s[40:41] offset:0\n\tglobal_load_lds_dwordx4 %[vA], s[40:41] offset:1024\n\tglobal_load_lds_dwordx4 %[vA], s[40:41] offset:2048\n\tglobal_load_lds_dwordx4 %[vA], s[40:41] offset:3072\n\ts_add_u32 s40, s40, 16384\n\ts_addc_u32 s41, s41, 0\n\ts_add_u32 s61, s61, 1\n\ts_and_b32 s61, s61, 3\n\ts_lshl_b32 s62, s61, 14\n\ts_add_u32 s60, %[lds0], s62\n\ts_waitcnt vmcnt(8)\n\ts_barrier\n\ts_waitcnt vmcnt(4)\n\ts_barrier\n\ts_add_u32 m0, s60, 4096\n\ts_nop 0\n\tglobal_load_lds_dwordx4 %[vA], s[40:41] offset:0\n\tglobal_load_lds_dwordx4 %[vA], s[40:41] offset:1024\n\tglobal_load_lds_dwordx4 %[vA], s[40:41] offset:2048\n\tglobal_load_lds_dwordx4 %[vA], s[40:41] offset:3072\n\ts_add_u32 s40, s40, 16384\n\ts_addc_u32 s41, s41, 0\n\ts_add_u32 s61, s61, 1\n\ts_and_b32 s61, s61, 3\n\ts_lshl_b32 s62, s61, 14\n\ts_add_u32 s60, %[lds0], s62\n\tds_read_b128 v[48:51], %[hs] offset:0\n\tds_read_b128 v[52:55], %[hs] offset:1024\n\tds_read_b128 v[56:59], %[hs] offset:2048\n\ts_waitcnt lgkmcnt(0)\n\ts_mov_b32 s70, 0\n\tbuffer_store_dwordx4 v[48:51], %[hoff], %[rh], s70 offen sc1\n\ts_mov_b32 s70, 114688\n\tbuffer_store_dwordx4 v[52:55], %[hoff], %[rh], s70 offen sc1\n\ts_mov_b32 s70, 229376\n\tbuffer_store_dwordx4 v[56:59], %[hoff], %[rh], s70 offen sc1\n\ts_waitcnt vmcnt(7)\n\ts_barrier\n\ts_add_u32 m0, s60, 4096\n\ts_nop 0\n\tglobal_load_lds_dwordx4 %[vA], s[40:41] offset:0\n\tglobal_load_lds_dwordx4 %[vA], s[40:41] offset:1024\n\tglobal_load_lds_dwordx4 %[vA], s[40:41] offset:2048\n\tglobal_load_lds_dwordx4 %[vA], s[40:41] offset:3072\n\ts_add_u32 s40, s40, 16384\n\ts_addc_u32 s41, s41, 0\n\ts_add_u32 s61, s61, 1\n\ts_and_b32 s61, s61, 3\n\ts_lshl_b32 s62, s61, 14\n\ts_add_u32 s60, %[lds0], s62\n\tds_read_b128 v[48:51], %[hs] offset:3072\n\tds_read_b128 v[52:55], %[hs] offset:4096\n\tds_read_b128 v[56:59], %[hs] offset:5120\n\ts_waitcnt lgkmcnt(0)\n\ts_mov_b32 s70, 344064\n\tbuffer_store_dwordx4 v[48:51], %[hoff], %[rh], s70 offen sc1\n\ts_mov_b32 s70, 1024\n\tbuffer_store_dwordx4 v[52:55], %[hoff], %[rh], s70 offen sc1\n\ts_mov_b32 s70, 115712\n\tbuffer_store_dwordx4 v[56:59], %[hoff], %[rh], s70 offen sc1\n\ts_waitcnt vmcnt(10)\n\ts_barrier\n\ts_add_u32 m0, s60, 4096\n\ts_nop 0\n\tglobal_load_lds_dwordx4 %[vA], s[40:41] offset:0\n\tglobal_load_lds_dwordx4 %[vA], s[40:41] offset:1024\n\tglobal_load_lds_dwordx4 %[vA], s[40:41] offset:2048\n\tglobal_load_lds_dwordx4 %[vA], s[40:41] offset:3072\n\ts_add_u32 s40, s40, 16384\n\ts_addc_u32 s41, s41, 0\n\ts_add_u32 s61, s61, 1\n\ts_and_b32 s61, s61, 3\n\ts_lshl_b32 s62, s61, 14\n\ts_add_u32 s60, %[lds0], s62\n\tds_read_b128 v[48:51], %[hs] offset:6144\n\tds_read_b128 v[52:55], %[hs] offset:7168\n\tds_read_b128 v[56:59], %[hs] offset:8192\n\ts_waitcnt lgkmcnt(0)\n\ts_mov_b32 s70, 230400\n\tbuffer_store_dwordx4 v[48:51], %[hoff], %[rh], s70 offen sc1\n\ts_mov_b32 s70, 345088\n\tbuffer_store_dwordx4 v[52:55], %[hoff], %[rh], s70 offen sc1\n\ts_mov_b32 s70, 2048\n\tbuffer_store_dwordx4 v[56:59], %[hoff], %[rh], s70 offen sc1\n\ts_waitcnt vmcnt(10)\n\ts_barrier\n\ts_add_u32 m0, s60, 4096\n\ts_nop 0\n\tglobal_load_lds_dwordx4 %[vA], s[40:41] offset:0\n\tglobal_load_lds_dwordx4 %[vA], s[40:41] offset:1024\n\tglobal_load_lds_dwordx4 %[vA], s[40:41] offset:2048\n\tglobal_load_lds_dwordx4 %[vA], s[40:41] offset:3072\n\ts_add_u32 s40, s40, 16384\n\ts_addc_u32 s41, s41, 0\n\ts_add_u32 s61, s61, 1\n\ts_and_b32 s61, s61, 3\n\ts_lshl_b32 s62, s61, 14\n\ts_add_u32 s60, %[lds0], s62\n\tds_read_b128 v[48:51], %[hs] offset:9216\n\tds_read_b128 v[52:55], %[hs] offset:10240\n\tds_read_b128 v[56:59], %[hs] offset:11264\n\ts_waitcnt lgkmcnt(0)\n\ts_mov_b32 s70, 116736\n\tbuffer_store_dwordx4 v[48:51], %[hoff], %[rh], s70 offen sc1\n\ts_mov_b32 s70, 231424\n\tbuffer_store_dwordx4 v[52:55], %[hoff], %[rh], s70 offen sc1\n\ts_mov_b32 s70, 346112\n\tbuffer_store_dwordx4 v[56:59], %[hoff], %[rh], s70 offen sc1\n\ts_waitcnt vmcnt(10)\n\ts_barrier\n\ts_add_u32 m0, s60, 4096\n\ts_nop 0\n\tglobal_load_lds_dwordx4 %[vA], s[40:41] offset:0\n\tglobal_load_lds_dwordx4 %[vA], s[40:41] offset:1024\n\tglobal_load_lds_dwordx4 %[vA], s[40:41] offset:2048\n\tglobal_load_lds_dwordx4 %[vA], s[40:41] offset:3072\n\ts_add_u32 s40, s40, 16384\n\ts_addc_u32 s41, s41, 0\n\ts_add_u32 s61, s61, 1\n\ts_and_b32 s61, s61, 3\n\ts_lshl_b32 s62, s61, 14\n\ts_add_u32 s60, %[lds0], s62\n\tds_read_b64 v[48:49], %[hs2] offset:0\n\tds_read_b64 v[52:53], %[hs2] offset:512\n\tds_read_b64 v[56:57], %[hs2] offset:1024\n\ts_waitcnt lgkmcnt(0)\n\ts_mov_b32 s70, 0\n\tbuffer_store_dwordx2 v[48:49], %[hoff2], %[rh], s70 offen sc1\n\ts_mov_b32 s70, 114688\n\tbuffer_store_dwordx2 v[52:53], %[hoff2], %[rh], s70 offen sc1\n\ts_mov_b32 s70, 229376\n\tbuffer_store_dwordx2 v[56:57], %[hoff2], %[rh], s70 offen sc1\n\ts_waitcnt vmcnt(10)\n\ts_barrier\n\ts_add_u32 m0, s60, 4096\n\ts_nop 0\n\tglobal_load_lds_dwordx4 %[vA], s[40:41] offset:0\n\tglobal_load_lds_dwordx4 %[vA], s[40:41] offset:1024\n\tglobal_load_lds_dwordx4 %[vA], s[40:41] offset:2048\n\tglobal_load_lds_dwordx4 %[vA], s[40:41] offset:3072\n\ts_add_u32 s40, s40, 16384\n\ts_addc_u32 s41, s41, 0\n\ts_add_u32 s61, s61, 1\n\ts_and_b32 s61, s61, 3\n\ts_lshl_b32 s62, s61, 14\n\ts_add_u32 s60, %[lds0], s62\n\tds_read_b64 v[48:49], %[hs2] offset:1536\n\ts_waitcnt lgkmcnt(0)\n\ts_mov_b32 s70, 344064\n\tbuffer_store_dwordx2 v[48:49], %[hoff2], %[rh], s70 offen sc1\n\ts_waitcnt vmcnt(8)\n\ts_barrier\n\ts_add_u32 m0, s60, 4096\n\ts_nop 0\n\tglobal_load_lds_dwordx4 %[vA], s[40:41] offset:0\n\tglobal_load_lds_dwordx4 %[vA], s[40:41] offset:1024\n\tglobal_load_lds_dwordx4 %[vA], s[40:41] offset:2048\n\tglobal_load_lds_dwordx4 %[vA], s[40:41] offset:3072\n\ts_add_u32 s40, s40, 16384\n\ts_addc_u32 s41, s41, 0\n\ts_add_u32 s61, s61, 1\n\ts_and_b32 s61, s61, 3\n\ts_lshl_b32 s62, s61, 14\n\ts_add_u32 s60, %[lds0], s62\n\tglobal_load_dword v40, %[p0], off\n\tglobal_load_dword v41, %[p1], off\n\ts_waitcnt vmcnt(7)\n\ts_barrier\n\ts_add_u32 m0, s60, 4096\n\ts_nop 0\n\tglobal_load_lds_dwordx4 %[vA], s[40:41] offset:0\n\tglobal_load_lds_dwordx4 %[vA], s[40:41] offset:1024\n\tglobal_load_lds_dwordx4 %[vA], s[40:41] offset:2048\n\tglobal_load_lds_dwordx4 %[vA], s[40:41] offset:3072\n\ts_add_u32 s40, s40, 16384\n\ts_addc_u32 s41, s41, 0\n\ts_add_u32 s61, s61, 1\n\ts_and_b32 s61, s61, 3\n\ts_lshl_b32 s62, s61, 14\n\ts_add_u32 s60, %[lds0], s62\n\ts_waitcnt vmcnt(6)\n\ts_barrier\n\ts_add_u32 m0, s60, 4096\n\ts_nop 0\n\tglobal_load_lds_dwordx4 %[vA], s[40:41] offset:0\n\tglobal_load_lds_dwordx4 %[vA], s[40:41] offset:1024\n\tglobal_load_lds_dwordx4 %[vA], s[40:41] offset:2048\n\tglobal_load_lds_dwordx4 %[vA], s[40:41] offset:3072\n\ts_add_u32 s40, s40, 16384\n\ts_addc_u32 s41, s41, 0\n\ts_add_u32 s61, s61, 1\n\ts_and_b32 s61, s61, 3\n\ts_lshl_b32 s62, s61, 14\n\ts_add_u32 s60, %[lds0], s62\n\ts_waitcnt vmcnt(4)\n\ts_barrier\n\ts_add_u32 m0, s60, 4096\n\ts_nop 0\n\tglobal_load_lds_dwordx4 %[vA], s[40:41] offset:0\n\tglobal_load_lds_dwordx4 %[vA], s[40:41] offset:1024\n\tglobal_load_lds_dwordx4 %[vA], s[40:41] offset:2048\n\tglobal_load_lds_dwordx4 %[vA], s[40:41] offset:3072\n\ts_add_u32 s40, s40, 16384\n\ts_addc_u32 s41, s41, 0\n\ts_add_u32 s61, s61, 1\n\ts_and_b32 s61, s61, 3\n\ts_lshl_b32 s62, s61, 14\n\ts_add_u32 s60, %[lds0], s62\n\ts_waitcnt vmcnt(4)\n\ts_barrier\n\ts_add_u32 m0, s60, 4096\n\ts_nop 0\n\tglobal_load_lds_dwordx4 %[vA], s[40:41] offset:0\n\tglobal_load_lds_dwordx4 %[vA], s[40:41] offset:1024\n\tglobal_load_lds_dwordx4 %[vA], s[40:41] offset:2048\n\tglobal_load_lds_dwordx4 %[vA], s[40:41] offset:3072\n\ts_add_u32 s40, s40, 16384\n\ts_addc_u32 s41, s41, 0\n\ts_add_u32 s61, s61, 1\n\ts_and_b32 s61, s61, 3\n\ts_lshl_b32 s62, s61, 14\n\ts_add_u32 s60, %[lds0], s62\n\ts_waitcnt vmcnt(4)\n\ts_barrier\n\ts_add_u32 m0, s60, 4096\n\ts_nop 0\n\tglobal_load_lds_dwordx4 %[vA], s[40:41] offset:0\n\tglobal_load_lds_dwordx4 %[vA], s[40:41] offset:1024\n\tglobal_load_lds_dwordx4 %[vA], s[40:41] offset:2048\n\tglobal_load_lds_dwordx4 %[vA], s[40:41] offset:3072\n\ts_add_u32 s40, s40, 16384\n\ts_addc_u32 s41, s41, 0\n\ts_add_u32 s61, s61, 1\n\ts_and_b32 s61, s61, 3\n\ts_lshl_b32 s62, s61, 14\n\ts_add_u32 s60, %[lds0], s62\n\ts_waitcnt vmcnt(4)\n\ts_barrier\n\ts_add_u32 m0, s60, 4096\n\ts_nop 0\n\tglobal_load_lds_dwordx4 %[vA], s[40:41] offset:0\n\tglobal_load_lds_dwordx4 %[vA], s[40:41] offset:1024\n\tglobal_load_lds_dwordx4 %[vA], s[40:41] offset:2048\n\tglobal_load_lds_dwordx4 %[vA], s[40:41] offset:3072\n\ts_add_u32 s40, s40, 16384\n\ts_addc_u32 s41, s41, 0\n\ts_add_u32 s61, s61, 1\n\ts_and_b32 s61, s61, 3\n\ts_lshl_b32 s62, s61, 14\n\ts_add_u32 s60, %[lds0], s62\n\ts_waitcnt vmcnt(4)\n\ts_barrier\n\ts_waitcnt vmcnt(0)\n\ts_barrier"
#define ATL_1_CLOB "s40","s41","s60","s61","s62","s66","s70","s71","v40","v41","v42","v43","v44","v48","v49","v50","v51","v52","v53","v54","v55","v56","v57","v58","v59"
#define ATL_2_ASM "s_mov_b64 s[40:41], %[pK]\n\ts_mov_b32 s60, %[lds0]\n\ts_mov_b32 s61, 0\n\ts_add_u32 m0, s60, 8192\n\ts_nop 0\n\tglobal_load_lds_dwordx4 %[vA], s[40:41] offset:0\n\tglobal_load_lds_dwordx4 %[vA], s[40:41] offset:1024\n\tglobal_load_lds_dwordx4 %[vA], s[40:41] offset:2048\n\tglobal_load_lds_dwordx4 %[vA], s[40:41] offset:3072\n\ts_add_u32 s40, s40, 16384\n\ts_addc_u32 s41, s41, 0\n\ts_add_u32 s61, s61, 1\n\ts_and_b32 s61, s61, 3\n\ts_lshl_b32 s62, s61, 14\n\ts_add_u32 s60, %[lds0], s62\n\ts_add_u32 m0, s60, 8192\n\ts_nop 0\n\tglobal_load_lds_dwordx4 %[vA], s[40:41] offset:0\n\tglobal_load_lds_dwordx4 %[vA], s[40:41] offset:1024\n\tglobal_load_lds_dwordx4 %[vA], s[40:41] offset:2048\n\tglobal_load_lds_dwordx4 %[vA], s[40:41] offset:3072\n\ts_add_u32 s40, s40, 16384\n\ts_addc_u32 s41, s41, 0\n\ts_add_u32 s61, s61, 1\n\ts_and_b32 s61, s61, 3\n\ts_lshl_b32 s62, s61, 14\n\ts_add_u32 s60, %[lds0], s62\n\ts_add_u32 m0, s60, 8192\n\ts_nop 0\n\tglobal_load_lds_dwordx4 %[vA], s[40:41] offset:0\n\tglobal_load_lds_dwordx4 %[vA], s[40:41] offset:1024\n\tglobal_load_lds_dwordx4 %[vA], s[40:41] offset:2048\n\tglobal_load_lds_dwordx4 %[vA], s[40:41] offset:3072\n\ts_add_u32 s40, s40, 16384\n\ts_addc_u32 s41, s41, 0\n\ts_add_u32 s61, s61, 1\n\ts_and_b32 s61, s61, 3\n\ts_lshl_b32 s62, s61, 14\n\ts_add_u32 s60, %[lds0], s62\n\ts_waitcnt vmcnt(8)\n\ts_barrier\n\ts_waitcnt vmcnt(4)\n\ts_barrier\n\ts_add_u32 m0, s60, 8192\n\ts_nop 0\n\tglobal_load_lds_dwordx4 %[vA], s[40:41] offset:0\n\tglobal_load_lds_dwordx4 %[vA], s[40:41] offset:1024\n\tglobal_load_lds_dwordx4 %[vA], s[40:41] offset:2048\n\tglobal_load_lds_dwordx4 %[vA], s[40:41] offset:3072\n\ts_add_u32 s40, s40, 16384\n\ts_addc_u32 s41, s41, 0\n\ts_add_u32 s61, s61, 1\n\ts_and_b32 s61, s61, 3\n\ts_lshl_b32 s62, s61, 14\n\ts_add_u32 s60, %[lds0], s62\n\tds_read_b128 v[48:51], %[hs] offset:0\n\tds_read_b128 v[52:55], %[hs] offset:1024\n\tds_read_b128 v[56:59], %[hs] offset:2048\n\ts_waitcnt lgkmcnt(0)\n\ts_mov_b32 s70, 0\n\tbuffer_store_dwordx4 v[48:51], %[hoff], %[rh], s70 offen sc1\n\ts_mov_b32 s70, 114688\n\tbuffer_store_dwordx4 v[52:55], %[hoff], %[rh], s70 offen sc1\n\ts_mov_b32 s70, 229376\n\tbuffer_store_dwordx4 v[56:59], %[hoff], %[rh], s70 offen sc1\n\ts_waitcnt vmcnt(7)\n\ts_barrier\n\ts_add_u32 m0, s60, 8192\n\ts_nop 0\n\tglobal_load_lds_dwordx4 %[vA], s[40:41] offset:0\n\tglobal_load_lds_dwordx4 %[vA], s[40:41] offset:1024\n\tglobal_load_lds_dwordx4 %[vA], s[40:41] offset:2048\n\tglobal_load_lds_dwordx4 %[vA], s[40:41] offset:3072\n\ts_add_u32 s40, s40, 16384\n\ts_addc_u32 s41, s41, 0\n\ts_add_u32 s61, s61, 1\n\ts_and_b32 s61, s61, 3\n\ts_lshl_b32 s62, s61, 14\n\ts_add_u32 s60, %[lds0], s62\n\tds_read_b128 v[48:51], %[hs] offset:3072\n\tds_read_b128 v[52:55], %[hs] offset:4096\n\tds_read_b128 v[56:59], %[hs] offset:5120\n\ts_waitcnt lgkmcnt(0)\n\ts_mov_b32 s70, 344064\n\tbuffer_store_dwordx4 v[48:51], %[hoff], %[rh], s70 offen sc1\n\ts_mov_b32 s70, 1024\n\tbuffer_store_dwordx4 v[52:55], %[hoff], %[rh], s70 offen sc1\n\ts_mov_b32 s70, 115712\n\tbuffer_store_dwordx4 v[56:59], %[hoff], %[rh], s70 offen sc1\n\ts_waitcnt vmcnt(10)\n\ts_barrier\n\ts_add_u32 m0, s60, 8192\n\ts_nop 0\n\tglobal_load_lds_dwordx4 %[vA], s[40:41] offset:0\n\tglobal_load_lds_dwordx4 %[vA], s[40:41] offset:1024\n\tglobal_load_lds_dwordx4 %[vA], s[40:41] offset:2048\n\tglobal_load_lds_dwordx4 %[vA], s[40:41] offset:3072\n\ts_add_u32 s40, s40, 16384\n\ts_addc_u32 s41, s41, 0\n\ts_add_u32 s61, s61, 1\n\ts_and_b32 s61, s61, 3\n\ts_lshl_b32 s62, s61, 14\n\ts_add_u32 s60, %[lds0], s62\n\tds_read_b128 v[48:51], %[hs] offset:6144\n\tds_read_b128 v[52:55], %[hs] offset:7168\n\tds_read_b128 v[56:59], %[hs] offset:8192\n\ts_waitcnt lgkmcnt(0)\n\ts_mov_b32 s70, 230400\n\tbuffer_store_dwordx4 v[48:51], %[hoff], %[rh], s70 offen sc1\n\ts_mov_b32 s70, 345088\n\tbuffer_store_dwordx4 v[52:55], %[hoff], %[rh], s70 offen sc1\n\ts_mov_b32 s70, 2048\n\tbuffer_store_dwordx4 v[56:59], %[hoff], %[rh], s70 offen sc1\n\ts_waitcnt vmcnt(10)\n\ts_barrier\n\ts_add_u32 m0, s60, 8192\n\ts_nop 0\n\tglobal_load_lds_dwordx4 %[vA], s[40:41] offset:0\n\tglobal_load_lds_dwordx4 %[vA], s[40:41] offset:1024\n\tglobal_load_lds_dwordx4 %[vA], s[40:41] offset:2048\n\tglobal_load_lds_dwordx4 %[vA], s[40:41] offset:3072\n\ts_add_u32 s40, s40, 16384\n\ts_addc_u32 s41, s41, 0\n\ts_add_u32 s61, s61, 1\n\ts_and_b32 s61, s61, 3\n\ts_lshl_b32 s62, s61, 14\n\ts_add_u32 s60, %[lds0], s62\n\tds_read_b128 v[48:51], %[hs] offset:9216\n\tds_read_b128 v[52:55], %[hs] offset:10240\n\tds_read_b128 v[56:59], %[hs] offset:11264\n\ts_waitcnt lgkmcnt(0)\n\ts_mov_b32 s70, 116736\n\tbuffer_store_dwordx4 v[48:51], %[hoff], %[rh], s70 offen sc1\n\ts_mov_b32 s70, 231424\n\tbuffer_store_dwordx4 v[52:55], %[hoff], %[rh], s70 offen sc1\n\ts_mov_b32 s70, 346112\n\tbuffer_store_dwordx4 v[56:59], %[hoff], %[rh], s70 offen sc1\n\ts_waitcnt vmcnt(10)\n\ts_barrier\n\ts_add_u32 m0, s60, 8192\n\ts_nop 0\n\tglobal_load_lds_dwordx4 %[vA], s[40:41] offset:0\n\tglobal_load_lds_dwordx4 %[vA], s[40:41] offset:1024\n\tglobal_load_lds_dwordx4 %[vA], s[40:41] offset:2048\n\tglobal_load_lds_dwordx4 %[vA], s[40:41] offset:3072\n\ts_add_u32 s40, s40, 16384\n\ts_addc_u32 s41, s41, 0\n\ts_add_u32 s61, s61, 1\n\ts_and_b32 s61, s61, 3\n\ts_lshl_b32 s62, s61, 14\n\ts_add_u32 s60, %[lds0], s62\n\tds_read_b64 v[48:49], %[hs2] offset:0\n\tds_read_b64 v[52:53], %[hs2] offset:512\n\tds_read_b64 v[56:57], %[hs2] offset:1024\n\ts_waitcnt lgkmcnt(0)\n\ts_mov_b32 s70, 0\n\tbuffer_store_dwordx2 v[48:49], %[hoff2], %[rh], s70 offen sc1\n\ts_mov_b32 s70, 114688\n\tbuffer_store_dwordx2 v[52:53], %[hoff2], %[rh], s70 offen sc1\n\ts_mov_b32 s70, 229376\n\tbuffer_store_dwordx2 v[56:57], %[hoff2], %[rh], s70 offen sc1\n\ts_waitcnt vmcnt(10)\n\ts_barrier\n\ts_add_u32 m0, s60, 8192\n\ts_nop 0\n\tglobal_load_lds_dwordx4 %[vA], s[40:41] offset:0\n\tglobal_load_lds_dwordx4 %[vA], s[40:41] offset:1024\n\tglobal_load_lds_dwordx4 %[vA], s[40:41] offset:2048\n\tglobal_load_lds_dwordx4 %[vA], s[40:41] offset:3072\n\ts_add_u32 s40, s40, 16384\n\ts_addc_u32 s41, s41, 0\n\ts_add_u32 s61, s61, 1\n\ts_and_b32 s61, s61, 3\n\ts_lshl_b32 s62, s61, 14\n\ts_add_u32 s60, %[lds0], s62\n\tds_read_b64 v[48:49], %[hs2] offset:1536\n\ts_waitcnt lgkmcnt(0)\n\ts_mov_b32 s70, 344064\n\tbuffer_store_dwordx2 v[48:49], %[hoff2], %[rh], s70 offen sc1\n\ts_waitcnt vmcnt(8)\n\ts_barrier\n\ts_add_u32 m0, s60, 8192\n\ts_nop 0\n\tglobal_load_lds_dwordx4 %[vA], s[40:41] offset:0\n\tglobal_load_lds_dwordx4 %[vA], s[40:41] offset:1024\n\tglobal_load_lds_dwordx4 %[vA], s[40:41] offset:2048\n\tglobal_load_lds_dwordx4 %[vA], s[40:41] offset:3072\n\ts_add_u32 s40, s40, 16384\n\ts_addc_u32 s41, s41, 0\n\ts_add_u32 s61, s61, 1\n\ts_and_b32 s61, s61, 3\n\ts_lshl_b32 s62, s61, 14\n\ts_add_u32 s60, %[lds0], s62\n\tglobal_load_dword v40, %[p0], off\n\tglobal_load_dword v41, %[p1], off\n\ts_waitcnt vmcnt(7)\n\ts_barrier\n\ts_add_u32 m0, s60, 8192\n\ts_nop 0\n\tglobal_load_lds_dwordx4 %[vA], s[40:41] offset:0\n\tglobal_load_lds_dwordx4 %[vA], s[40:41] offset:1024\n\tglobal_load_lds_dwordx4 %[vA], s[40:41] offset:2048\n\tglobal_load_lds_dwordx4 %[vA], s[40:41] offset:3072\n\ts_add_u32 s40, s40, 16384\n\ts_addc_u32 s41, s41, 0\n\ts_add_u32 s61, s61, 1\n\ts_and_b32 s61, s61, 3\n\ts_lshl_b32 s62, s61, 14\n\ts_add_u32 s60, %[lds0], s62\n\ts_waitcnt vmcnt(6)\n\ts_barrier\n\ts_add_u32 m0, s60, 8192\n\ts_nop 0\n\tglobal_load_lds_dwordx4 %[vA], s[40:41] offset:0\n\tglobal_load_lds_dwordx4 %[vA], s[40:41] offset:1024\n\tglobal_load_lds_dwordx4 %[vA], s[40:41] offset:2048\n\tglobal_load_lds_dwordx4 %[vA], s[40:41] offset:3072\n\ts_add_u32 s40, s40, 16384\n\ts_addc_u32 s41, s41, 0\n\ts_add_u32 s61, s61, 1\n\ts_and_b32 s61, s61, 3\n\ts_lshl_b32 s62, s61, 14\n\ts_add_u32 s60, %[lds0], s62\n\ts_waitcnt vmcnt(4)\n\ts_barrier\n\ts_add_u32 m0, s60, 8192\n\ts_nop 0\n\tglobal_load_lds_dwordx4 %[vA], s[40:41] offset:0\n\tglobal_load_lds_dwordx4 %[vA], s[40:41] offset:1024\n\tglobal_load_lds_dwordx4 %[vA], s[40:41] offset:2048\n\tglobal_load_lds_dwordx4 %[vA], s[40:41] offset:3072\n\ts_add_u32 s40, s40, 16384\n\ts_addc_u32 s41, s41, 0\n\ts_add_u32 s61, s61, 1\n\ts_and_b32 s61, s61, 3\n\ts_lshl_b32 s62, s61, 14\n\ts_add_u32 s60, %[lds0], s62\n\ts_waitcnt vmcnt(4)\n\ts_barrier\n\ts_add_u32 m0, s60, 8192\n\ts_nop 0\n\tglobal_load_lds_dwordx4 %[vA], s[40:41] offset:0\n\tglobal_load_lds_dwordx4 %[vA], s[40:41] offset:1024\n\tglobal_load_lds_dwordx4 %[vA], s[40:41] offset:2048\n\tglobal_load_lds_dwordx4 %[vA], s[40:41] offset:3072\n\ts_add_u32 s40, s40, 16384\n\ts_addc_u32 s41, s41, 0\n\ts_add_u32 s61, s61, 1\n\ts_and_b32 s61, s61, 3\n\ts_lshl_b32 s62, s61, 14\n\ts_add_u32 s60, %[lds0], s62\n\ts_waitcnt vmcnt(4)\n\ts_barrier\n\ts_add_u32 m0, s60, 8192\n\ts_nop 0\n\tglobal_load_lds_dwordx4 %[vA], s[40:41] offset:0\n\tglobal_load_lds_dwordx4 %[vA], s[40:41] offset:1024\n\tglobal_load_lds_dwordx4 %[vA], s[40:41] offset:2048\n\tglobal_load_lds_dwordx4 %[vA], s[40:41] offset:3072\n\ts_add_u32 s40, s40, 16384\n\ts_addc_u32 s41, s41, 0\n\ts_add_u32 s61, s61, 1\n\ts_and_b32 s61, s61, 3\n\ts_lshl_b32 s62, s61, 14\n\ts_add_u32 s60, %[lds0], s62\n\ts_waitcnt vmcnt(4)\n\ts_barrier\n\ts_add_u32 m0, s60, 8192\n\ts_nop 0\n\tglobal_load_lds_dwordx4 %[vA], s[40:41] offset:0\n\tglobal_load_lds_dwordx4 %[vA], s[40:41] offset:1024\n\tglobal_load_lds_dwordx4 %[vA], s[40:41] offset:2048\n\tglobal_load_lds_dwordx4 %[vA], s[40:41] offset:3072\n\ts_add_u32 s40, s40, 16384\n\ts_addc_u32 s41, s41, 0\n\ts_add_u32 s61, s61, 1\n\ts_and_b32 s61, s61, 3\n\ts_lshl_b32 s62, s61, 14\n\ts_add_u32 s60, %[lds0], s62\n\ts_waitcnt vmcnt(4)\n\ts_barrier\n\ts_waitcnt vmcnt(0)\n\ts_barrier"
#define ATL_2_CLOB "s40","s41","s60","s61","s62","s66","s70","s71","v40","v41","v42","v43","v44","v48","v49","v50","v51","v52","v53","v54","v55","v56","v57","v58","v59"
#define ATL_3_ASM "s_mov_b64 s[40:41], %[pK]\n\ts_mov_b32 s60, %[lds0]\n\ts_mov_b32 s61, 0\n\ts_add_u32 m0, s60, 12288\n\ts_nop 0\n\tglobal_load_lds_dwordx4 %[vA], s[40:41] offset:0\n\tglobal_load_lds_dwordx4 %[vA], s[40:41] offset:1024\n\tglobal_load_lds_dwordx4 %[vA], s[40:41] offset:2048\n\tglobal_load_lds_dwordx4 %[vA], s[40:41] offset:3072\n\ts_add_u32 s40, s40, 16384\n\ts_addc_u32 s41, s41, 0\n\ts_add_u32 s61, s61, 1\n\ts_and_b32 s61, s61, 3\n\ts_lshl_b32 s62, s61, 14\n\ts_add_u32 s60, %[lds0], s62\n\ts_add_u32 m0, s60, 12288\n\ts_nop 0\n\tglobal_load_lds_dwordx4 %[vA], s[40:41] offset:0\n\tglobal_load_lds_dwordx4 %[vA], s[40:41] offset:1024\n\tglobal_load_lds_dwordx4 %[vA], s[40:41] offset:2048\n\tglobal_load_lds_dwordx4 %[vA], s[40:41] offset:3072\n\ts_add_u32 s40, s40, 16384\n\ts_addc_u32 s41, s41, 0\n\ts_add_u32 s61, s61, 1\n\ts_and_b32 s61, s61, 3\n\ts_lshl_b32 s62, s61, 14\n\ts_add_u32 s60, %[lds0], s62\n\ts_add_u32 m0, s60, 12288\n\ts_nop 0\n\tglobal_load_lds_dwordx4 %[vA], s[40:41] offset:0\n\tglobal_load_lds_dwordx4 %[vA], s[40:41] offset:1024\n\tglobal_load_lds_dwordx4 %[vA], s[40:41] offset:2048\n\tglobal_load_lds_dwordx4 %[vA], s[40:41] offset:3072\n\ts_add_u32 s40, s40, 16384\n\ts_addc_u32 s41, s41, 0\n\ts_add_u32 s61, s61, 1\n\ts_and_b32 s61, s61, 3\n\ts_lshl_b32 s62, s61, 14\n\ts_add_u32 s60, %[lds0], s62\n\ts_waitcnt vmcnt(8)\n\ts_barrier\n\ts_waitcnt vmcnt(4)\n\ts_barrier\n\ts_add_u32 m0, s60, 12288\n\ts_nop 0\n\tglobal_load_lds_dwordx4 %[vA], s[40:41] offset:0\n\tglobal_load_lds_dwordx4 %[vA], s[40:41] offset:1024\n\tglobal_load_lds_dwordx4 %[vA], s[40:41] offset:2048\n\tglobal_load_lds_dwordx4 %[vA], s[40:41] offset:3072\n\ts_add_u32 s40, s40, 16384\n\ts_addc_u32 s41, s41, 0\n\ts_add_u32 s61, s61, 1\n\ts_and_b32 s61, s61, 3\n\ts_lshl_b32 s62, s61, 14\n\ts_add_u32 s60, %[lds0], s62\n\tds_read_b128 v[48:51], %[hs] offset:0\n\tds_read_b128 v[52:55], %[hs] offset:1024\n\tds_read_b128 v[56:59], %[hs] offset:2048\n\ts_waitcnt lgkmcnt(0)\n\ts_mov_b32 s70, 0\n\tbuffer_store_dwordx4 v[48:51], %[hoff], %[rh], s70 offen sc1\n\ts_mov_b32 s70, 114688\n\tbuffer_store_dwordx4 v[52:55], %[hoff], %[rh], s70 offen sc1\n\ts_mov_b32 s70, 229376\n\tbuffer_store_dwordx4 v[56:59], %[hoff], %[rh], s70 offen sc1\n\ts_waitcnt vmcnt(7)\n\ts_barrier\n\ts_add_u32 m0, s60, 12288\n\ts_nop 0\n\tglobal_load_lds_dwordx4 %[vA], s[40:41] offset:0\n\tglobal_load_lds_dwordx4 %[vA], s[40:41] offset:1024\n\tglobal_load_lds_dwordx4 %[vA], s[40:41] offset:2048\n\tglobal_load_lds_dwordx4 %[vA], s[40:41] offset:3072\n\ts_add_u32 s40, s40, 16384\n\ts_addc_u32 s41, s41, 0\n\ts_add_u32 s61, s61, 1\n\ts_and_b32 s61, s61, 3\n\ts_lshl_b32 s62, s61, 14\n\ts_add_u32 s60, %[lds0], s62\n\tds_read_b128 v[48:51], %[hs] offset:3072\n\tds_read_b128 v[52:55], %[hs] offset:4096\n\tds_read_b128 v[56:59], %[hs] offset:5120\n\ts_waitcnt lgkmcnt(0)\n\ts_mov_b32 s70, 344064\n\tbuffer_store_dwordx4 v[48:51], %[hoff], %[rh], s70 offen sc1\n\ts_mov_b32 s70, 1024\n\tbuffer_store_dwordx4 v[52:55], %[hoff], %[rh], s70 offen sc1\n\ts_mov_b32 s70, 115712\n\tbuffer_store_dwordx4 v[56:59], %[hoff], %[rh], s70 offen sc1\n\ts_waitcnt vmcnt(10)\n\ts_barrier\n\ts_add_u32 m0, s60, 12288\n\ts_nop 0\n\tglobal_load_lds_dwordx4 %[vA], s[40:41] offset:0\n\tglobal_load_lds_dwordx4 %[vA], s[40:41] offset:1024\n\tglobal_load_lds_dwordx4 %[vA], s[40:41] offset:2048\n\tglobal_load_lds_dwordx4 %[vA], s[40:41] offset:3072\n\ts_add_u32 s40, s40, 16384\n\ts_addc_u32 s41, s41, 0\n\ts_add_u32 s61, s61, 1\n\ts_and_b32 s61, s61, 3\n\ts_lshl_b32 s62, s61, 14\n\ts_add_u32 s60, %[lds0], s62\n\tds_read_b128 v[48:51], %[hs] offset:6144\n\tds_read_b128 v[52:55], %[hs] offset:7168\n\tds_read_b128 v[56:59], %[hs] offset:8192\n\ts_waitcnt lgkmcnt(0)\n\ts_mov_b32 s70, 230400\n\tbuffer_store_dwordx4 v[48:51], %[hoff], %[rh], s70 offen sc1\n\ts_mov_b32 s70, 345088\n\tbuffer_store_dwordx4 v[52:55], %[hoff], %[rh], s70 offen sc1\n\ts_mov_b32 s70, 2048\n\tbuffer_store_dwordx4 v[56:59], %[hoff], %[rh], s70 offen sc1\n\ts_waitcnt vmcnt(10)\n\ts_barrier\n\ts_add_u32 m0, s60, 12288\n\ts_nop 0\n\tglobal_load_lds_dwordx4 %[vA], s[40:41] offset:0\n\tglobal_load_lds_dwordx4 %[vA], s[40:41] offset:1024\n\tglobal_load_lds_dwordx4 %[vA], s[40:41] offset:2048\n\tglobal_load_lds_dwordx4 %[vA], s[40:41] offset:3072\n\ts_add_u32 s40, s40, 16384\n\ts_addc_u32 s41, s41, 0\n\ts_add_u32 s61, s61, 1\n\ts_and_b32 s61, s61, 3\n\ts_lshl_b32 s62, s61, 14\n\ts_add_u32 s60, %[lds0], s62\n\tds_read_b128 v[48:51], %[hs] offset:9216\n\tds_read_b128 v[52:55], %[hs] offset:10240\n\tds_read_b128 v[56:59], %[hs] offset:11264\n\ts_waitcnt lgkmcnt(0)\n\ts_mov_b32 s70, 116736\n\tbuffer_store_dwordx4 v[48:51], %[hoff], %[rh], s70 offen sc1\n\ts_mov_b32 s70, 231424\n\tbuffer_store_dwordx4 v[52:55], %[hoff], %[rh], s70 offen sc1\n\ts_mov_b32 s70, 346112\n\tbuffer_store_dwordx4 v[56:59], %[hoff], %[rh], s70 offen sc1\n\ts_waitcnt vmcnt(10)\n\ts_barrier\n\ts_add_u32 m0, s60, 12288\n\ts_nop 0\n\tglobal_load_lds_dwordx4 %[vA], s[40:41] offset:0\n\tglobal_load_lds_dwordx4 %[vA], s[40:41] offset:1024\n\tglobal_load_lds_dwordx4 %[vA], s[40:41] offset:2048\n\tglobal_load_lds_dwordx4 %[vA], s[40:41] offset:3072\n\ts_add_u32 s40, s40, 16384\n\ts_addc_u32 s41, s41, 0\n\ts_add_u32 s61, s61, 1\n\ts_and_b32 s61, s61, 3\n\ts_lshl_b32 s62, s61, 14\n\ts_add_u32 s60, %[lds0], s62\n\tds_read_b64 v[48:49], %[hs2] offset:0\n\tds_read_b64 v[52:53], %[hs2] offset:512\n\tds_read_b64 v[56:57], %[hs2] offset:1024\n\ts_waitcnt lgkmcnt(0)\n\ts_mov_b32 s70, 0\n\tbuffer_store_dwordx2 v[48:49], %[hoff2], %[rh], s70 offen sc1\n\ts_mov_b32 s70, 114688\n\tbuffer_store_dwordx2 v[52:53], %[hoff2], %[rh], s70 offen sc1\n\ts_mov_b32 s70, 229376\n\tbuffer_store_dwordx2 v[56:57], %[hoff2], %[rh], s70 offen sc1\n\ts_waitcnt vmcnt(10)\n\ts_barrier\n\ts_add_u32 m0, s60, 12288\n\ts_nop 0\n\tglobal_load_lds_dwordx4 %[vA], s[40:41] offset:0\n\tglobal_load_lds_dwordx4 %[vA], s[40:41] offset:1024\n\tglobal_load_lds_dwordx4 %[vA], s[40:41] offset:2048\n\tglobal_load_lds_dwordx4 %[vA], s[40:41] offset:3072\n\ts_add_u32 s40, s40, 16384\n\ts_addc_u32 s41, s41, 0\n\ts_add_u32 s61, s61, 1\n\ts_and_b32 s61, s61, 3\n\ts_lshl_b32 s62, s61, 14\n\ts_add_u32 s60, %[lds0], s62\n\tds_read_b64 v[48:49], %[hs2] offset:1536\n\ts_waitcnt lgkmcnt(0)\n\ts_mov_b32 s70, 344064\n\tbuffer_store_dwordx2 v[48:49], %[hoff2], %[rh], s70 offen sc1\n\ts_waitcnt vmcnt(8)\n\ts_barrier\n\ts_add_u32 m0, s60, 12288\n\ts_nop 0\n\tglobal_load_lds_dwordx4 %[vA], s[40:41] offset:0\n\tglobal_load_lds_dwordx4 %[vA], s[40:41] offset:1024\n\tglobal_load_lds_dwordx4 %[vA], s[40:41] offset:2048\n\tglobal_load_lds_dwordx4 %[vA], s[40:41] offset:3072\n\ts_add_u32 s40, s40, 16384\n\ts_addc_u32 s41, s41, 0\n\ts_add_u32 s61, s61, 1\n\ts_and_b32 s61, s61, 3\n\ts_lshl_b32 s62, s61, 14\n\ts_add_u32 s60, %[lds0], s62\n\tglobal_load_dword v40, %[p0], off\n\tglobal_load_dword v41, %[p1], off\n\ts_waitcnt vmcnt(7)\n\ts_barrier\n\ts_add_u32 m0, s60, 12288\n\ts_nop 0\n\tglobal_load_lds_dwordx4 %[vA], s[40:41] offset:0\n\tglobal_load_lds_dwordx4 %[vA], s[40:41] offset:1024\n\tglobal_load_lds_dwordx4 %[vA], s[40:41] offset:2048\n\tglobal_load_lds_dwordx4 %[vA], s[40:41] offset:3072\n\ts_add_u32 s40, s40, 16384\n\ts_addc_u32 s41, s41, 0\n\ts_add_u32 s61, s61, 1\n\ts_and_b32 s61, s61, 3\n\ts_lshl_b32 s62, s61, 14\n\ts_add_u32 s60, %[lds0], s62\n\ts_waitcnt vmcnt(6)\n\ts_barrier\n\ts_add_u32 m0, s60, 12288\n\ts_nop 0\n\tglobal_load_lds_dwordx4 %[vA], s[40:41] offset:0\n\tglobal_load_lds_dwordx4 %[vA], s[40:41] offset:1024\n\tglobal_load_lds_dwordx4 %[vA], s[40:41] offset:2048\n\tglobal_load_lds_dwordx4 %[vA], s[40:41] offset:3072\n\ts_add_u32 s40, s40, 16384\n\ts_addc_u32 s41, s41, 0\n\ts_add_u32 s61, s61, 1\n\ts_and_b32 s61, s61, 3\n\ts_lshl_b32 s62, s61, 14\n\ts_add_u32 s60, %[lds0], s62\n\tv_mov_b32 v43, 1\n\tv_mov_b32 v44, 0\n\ts_mov_b64 exec, 1\n\tglobal_atomic_add v44, v43, %[hadd]\n\ts_mov_b64 exec, -1\n\ts_waitcnt vmcnt(5)\n\ts_barrier\n\ts_add_u32 m0, s60, 12288\n\ts_nop 0\n\tglobal_load_lds_dwordx4 %[vA], s[40:41] offset:0\n\tglobal_load_lds_dwordx4 %[vA], s[40:41] offset:1024\n\tglobal_load_lds_dwordx4 %[vA], s[40:41] offset:2048\n\tglobal_load_lds_dwordx4 %[vA], s[40:41] offset:3072\n\ts_add_u32 s40, s40, 16384\n\ts_addc_u32 s41, s41, 0\n\ts_add_u32 s61, s61, 1\n\ts_and_b32 s61, s61, 3\n\ts_lshl_b32 s62, s61, 14\n\ts_add_u32 s60, %[lds0], s62\n\ts_waitcnt vmcnt(5)\n\ts_barrier\n\tv_mov_b32 v44, 0\n\tglobal_load_dword v43, v44, %[hpoll] sc1\n\ts_add_u32 m0, s60, 12288\n\ts_nop 0\n\tglobal_load_lds_dwordx4 %[vA], s[40:41] offset:0\n\tglobal_load_lds_dwordx4 %[vA], s[40:41] offset:1024\n\tglobal_load_lds_dwordx4 %[vA], s[40:41] offset:2048\n\tglobal_load_lds_dwordx4 %[vA], s[40:41] offset:3072\n\ts_add_u32 s40, s40, 16384\n\ts_addc_u32 s41, s41, 0\n\ts_add_u32 s61, s61, 1\n\ts_and_b32 s61, s61, 3\n\ts_lshl_b32 s62, s61, 14\n\ts_add_u32 s60, %[lds0], s62\n\ts_waitcnt vmcnt(5)\n\ts_barrier\n\ts_add_u32 m0, s60, 12288\n\ts_nop 0\n\tglobal_load_lds_dwordx4 %[vA], s[40:41] offset:0\n\tglobal_load_lds_dwordx4 %[vA], s[40:41] offset:1024\n\tglobal_load_lds_dwordx4 %[vA], s[40:41] offset:2048\n\tglobal_load_lds_dwordx4 %[vA], s[40:41] offset:3072\n\ts_add_u32 s40, s40, 16384\n\ts_addc_u32 s41, s41, 0\n\ts_add_u32 s61, s61, 1\n\ts_and_b32 s61, s61, 3\n\ts_lshl_b32 s62, s61, 14\n\ts_add_u32 s60, %[lds0], s62\n\ts_waitcnt vmcnt(4)\n\ts_barrier\n\ts_add_u32 m0, s60, 12288\n\ts_nop 0\n\tglobal_load_lds_dwordx4 %[vA], s[40:41] offset:0\n\tglobal_load_lds_dwordx4 %[vA], s[40:41] offset:1024\n\tglobal_load_lds_dwordx4 %[vA], s[40:41] offset:2048\n\tglobal_load_lds_dwordx4 %[vA], s[40:41] offset:3072\n\ts_add_u32 s40, s40, 16384\n\ts_addc_u32 s41, s41, 0\n\ts_add_u32 s61, s61, 1\n\ts_and_b32 s61, s61, 3\n\ts_lshl_b32 s62, s61, 14\n\ts_add_u32 s60, %[lds0], s62\n\tv_readfirstlane_b32 s70, v43\n\ts_mov_b32 s71, 0\n\ts_nop 3\n\ts_cmp_ge_u32 s70, 32\n\ts_cbranch_scc1 ATH_d%=\n\tATH_p%=:\n\tglobal_load_dword v43, v44, %[hpoll] sc1\n\ts_waitcnt vmcnt(0)\n\tv_readfirstlane_b32 s70, v43\n\ts_add_u32 s71, s71, 1\n\ts_nop 3\n\ts_cmp_ge_u32 s70, 32\n\ts_cbranch_scc1 ATH_d%=\n\ts_cmp_gt_u32 s71, 0x10000\n\ts_cbranch_scc1 ATH_d%=\n\ts_sleep 2\n\ts_branch ATH_p%=\n\tATH_d%=:\n\tbuffer_inv sc1\n\ts_waitcnt vmcnt(5)\n\ts_barrier\n\ts_waitcnt vmcnt(0)\n\ts_barrier"
#define ATL_3_CLOB "s40","s41","s60","s61","s62","s66","s70","s71","v40","v41","v42","v43","v44","v48","v49","v50","v51","v52","v53","v54","v55","v56","v57","v58","v59"

template<int Q> DI f32x16 at_rd(int tok){ f32x16 o; const f32x4 a=rdacc4<16*Q>(tok),b=rdacc4<16*Q+4>(tok),c=rdacc4<16*Q+8>(tok),d=rdacc4<16*Q+12>(tok);
  #pragma unroll
  for(int i=0;i<4;i++){ o[i]=a[i]; o[4+i]=b[i]; o[8+i]=c[i]; o[12+i]=d[i]; }
  return o; }
DI float at_rowsum(float l0,float l1){
  auto a=__builtin_amdgcn_permlane16_swap(__float_as_uint(l0),__float_as_uint(l1),false,false);
  auto b=__builtin_amdgcn_permlane32_swap(a[0],a[0],false,false); return __uint_as_float(b[0]); }
DI bool at_bad(const f32x16& o,float l){
  f32x2 t[8];
  #pragma unroll
  for(int i=0;i<8;i++) t[i]=f32x2{o[2*i],o[2*i+1]}*0.f;
  t[0]+=t[4]; t[1]+=t[5]; t[2]+=t[6]; t[3]+=t[7]; t[0]+=t[2]; t[1]+=t[3]; t[0]+=t[1];
  return !(l+t[0][0]+t[0][1]<1.0e12f); }
DI void at_store(LAS char* os,const f32x16& o,float l){
  const float inv=__builtin_amdgcn_rcpf(l);
  u32x4 wa,wb;
  wa[0]=cvtpk(o[0]*inv,o[1]*inv); wa[1]=cvtpk(o[2]*inv,o[3]*inv); wa[2]=cvtpk(o[8]*inv,o[9]*inv); wa[3]=cvtpk(o[10]*inv,o[11]*inv);
  wb[0]=cvtpk(o[4]*inv,o[5]*inv); wb[1]=cvtpk(o[6]*inv,o[7]*inv); wb[2]=cvtpk(o[12]*inv,o[13]*inv); wb[3]=cvtpk(o[14]*inv,o[15]*inv);
  *(LAS u32x4*)os=wa; *(LAS u32x4*)(os+1024)=wb;
}
template<int I> DI float rdacc_v(){ float r; asm volatile("v_accvgpr_read_b32 %0, a%c1":"=v"(r):"i"(I)); return r; }
template<int I> DI bf16x8 rdq(){ f32x4 r; r[0]=rdacc_v<I>(); r[1]=rdacc_v<I+1>(); r[2]=rdacc_v<I+2>(); r[3]=rdacc_v<I+3>(); return __builtin_bit_cast(bf16x8,r); }
DI int lane_id(){ int l; asm volatile("v_mbcnt_lo_u32_b32 %0, -1, 0\n\tv_mbcnt_hi_u32_b32 %0, -1, %0":"=v"(l)); return l; }

constexpr int G2_SLOT=24576, G2_NS=6;
template<int L> DI void g2_loader(u16* Ht,u16* Ot,const char* W2,const char* Wo,int R,int C,int q,LAS char* lds,unsigned* flags,int rbk,int cbk){
  const int lane=lane_id(); const unsigned lds0=(unsigned)(uintptr_t)lds;
  const unsigned rH=112*1024, rO=32*1024;
  const rsrc_t rA=mkrsrc(Ht,2048*3584*2), rOt=mkrsrc(Ot,2048*1024*2), rB=mkrsrc(W2,3584*1024*2), rW=mkrsrc(Wo,1024*1024*2);
  const unsigned oA=(R*16+4*L)*rH+q*28*1024, oO=(R*16+4*L)*rO+q*8*1024, oB=C*256+q*28*32*2048, oW=C*256+q*8*32*2048;
  unsigned vA[4],vO[4],vB[2];
  #pragma unroll
  for(int i=0;i<4;i++){ vA[i]=lane*16+i*rH-i*1024; vO[i]=lane*16+i*rO-i*1024; }
  #pragma unroll
  for(int i=0;i<2;i++){ const int r=4*(2*L+i)+(lane>>4), pos=lane&15; vB[i]=r*2048+(pos^((r&7)<<1))*16-i*1024; }
  const unsigned* oadd=flags+4096+256+(rbk>>1)*16; const unsigned* opoll=flags+4096+256+R*16;
  const int r=lane&31,h=lane>>5;
  const unsigned ooff=(((4*rbk+2*(L>>1))*2+(r>>4))*32+2*cbk+(L&1))*1024+((r&15)+16*h)*16, os=lds0+OS_BASE+L*4096+lane*16;
  #define L2_CALL(N) asm volatile(L2_##N##_ASM :: [rA]"s"(rA),[rO]"s"(rOt),[rB]"s"(rB),[rW]"s"(rW),[oA]"s"(oA),[oO]"s"(oO),[oB]"s"(oB),[oW]"s"(oW),[lds0]"s"(lds0), \
     [vA0]"v"(vA[0]),[vA1]"v"(vA[1]),[vA2]"v"(vA[2]),[vA3]"v"(vA[3]),[vO0]"v"(vO[0]),[vO1]"v"(vO[1]),[vO2]"v"(vO[2]),[vO3]"v"(vO[3]),[vB0]"v"(vB[0]),[vB1]"v"(vB[1]), \
     [oadd]"s"(oadd),[opoll]"s"(opoll),[ro]"s"(rOt),[ooff]"v"(ooff),[os]"v"(os) : "memory","scc",L2_##N##_CLOB)
  if(L==0) L2_CALL(0); else if(L==1) L2_CALL(1); else if(L==2) L2_CALL(2); else L2_CALL(3);
  #undef L2_CALL
}
#define G2A(m,n) (((m)*4+(n))*4)
template<int M> DI void g2_send(rsrc_t rp,unsigned off,int tok){
  #define PK2(m,n0) { const f32x4 u=rdacc4<G2A(m,n0)>(tok), v=rdacc4<G2A(m,n0+1)>(tok); const u32x4 w={cvtpk(u[0],u[1]),cvtpk(u[2],u[3]),cvtpk(v[0],v[1]),cvtpk(v[2],v[3])}; BST16(w,rp,off+(((m)-M)*2+(n0)/2)*1024,16); }
  PK2(M,0) PK2(M,2) PK2(M+1,0) PK2(M+1,2)
  #undef PK2
}
template<int M> DI void g2_own(f32x4 (&v)[8],int tok){
  v[0]=rdacc4<G2A(M,0)>(tok); v[1]=rdacc4<G2A(M,1)>(tok); v[2]=rdacc4<G2A(M,2)>(tok); v[3]=rdacc4<G2A(M,3)>(tok);
  v[4]=rdacc4<G2A(M+1,0)>(tok); v[5]=rdacc4<G2A(M+1,1)>(tok); v[6]=rdacc4<G2A(M+1,2)>(tok); v[7]=rdacc4<G2A(M+1,3)>(tok); }
DI void g2_compute(int wr,int wc,int R,int C,int q,unsigned lds0,char* part,unsigned* flags,const u16* x,const u16* b2,u16* out){
  int cnt;
  { const int lane=lane_id(); const int g=lane>>4,qq=(lane&15)>>2,p=lane&3;
    int aA=(int)lds0+wr*8192+lane*16; const int rho=4*(g&1)+qq; const int rowb=(int)lds0+16384+(4*g+qq)*256+8*p;
    int b0=rowb+32*((4*wc+0)^rho), b1=rowb+32*((4*wc+1)^rho), b2_=rowb+32*((4*wc+2)^rho), b3=rowb+32*((4*wc+3)^rho);
    cnt=17; int slot=0, dlt;
    asm volatile(G2_ASM : [aA]"+v"(aA),[b0]"+v"(b0),[b1]"+v"(b1),[b2]"+v"(b2_),[b3]"+v"(b3),[cnt]"+s"(cnt),[slot]"+s"(slot),[dlt]"=&s"(dlt) : [cW]"s"(-(G2_NS-1)*G2_SLOT),[cS]"s"(G2_SLOT) : "memory","scc",G2_CLOB);
  }
  const int lane=lane_id(), wv=wr*2+wc; const int g=lane>>4,c=lane&15;
  const int tile=R*8+C;
  u32x2 xx[8],bb[4];
  { const int col=C*128+wc*64+4*g; const int row0=R*256+wr*128+q*32+c;
    #pragma unroll
    for(int i=0;i<8;i++) xx[i]=*(const u32x2*)(x+(size_t)(row0+16*(i>>2))*1024+col+16*(i&3));
    #pragma unroll
    for(int n=0;n<4;n++) bb[n]=*(const u32x2*)(b2+col+16*n); }
  const rsrc_t rp=mkrsrc(part,64*4*4*4*4096);
  { const unsigned base=(unsigned)(((tile*4)*4+q)*4+wv)*4096+lane*16;
    if(q!=0) g2_send<0>(rp,base+0*65536,cnt); if(q!=1) g2_send<2>(rp,base+1*65536,cnt); if(q!=2) g2_send<4>(rp,base+2*65536,cnt); if(q!=3) g2_send<6>(rp,base+3*65536,cnt); }
  f32x4 v[8];
  if(q==0) g2_own<0>(v,cnt); else if(q==1) g2_own<2>(v,cnt); else if(q==2) g2_own<4>(v,cnt); else g2_own<6>(v,cnt);
  #pragma unroll
  for(int i=0;i<8;i++){ const u32x2 xv=xx[i], bv=bb[i&3]; v[i]+=f32x4{bflo(xv[0])+bflo(bv[0]),bfhi(xv[0])+bfhi(bv[0]),bflo(xv[1])+bflo(bv[1]),bfhi(xv[1])+bfhi(bv[1])}; PIN(v[i]); }
  asm volatile("s_waitcnt vmcnt(0)":::"memory");
  if(lane<4 && lane!=q) __hip_atomic_fetch_add(flags+(tile*4+lane)*16,1u,__ATOMIC_RELAXED,__HIP_MEMORY_SCOPE_AGENT);
  if(wv==0){ unsigned spins=0; while(__builtin_amdgcn_readfirstlane(__hip_atomic_load(flags+(tile*4+q)*16,__ATOMIC_RELAXED,__HIP_MEMORY_SCOPE_AGENT))<12u){ __builtin_amdgcn_s_sleep(1); if(++spins>(1u<<17)) break; } }
  asm volatile("s_barrier":::"memory");
  u32x4 r[3][4];
  { const char* s0=part+(size_t)(((tile*4+q)*4+((q+1)&3))*4+wv)*4096+lane*16; const char* s1=part+(size_t)(((tile*4+q)*4+((q+2)&3))*4+wv)*4096+lane*16; const char* s2=part+(size_t)(((tile*4+q)*4+((q+3)&3))*4+wv)*4096+lane*16;
    asm volatile("global_load_dwordx4 %0, %12, off sc1\n\tglobal_load_dwordx4 %1, %12, off offset:1024 sc1\n\tglobal_load_dwordx4 %2, %12, off offset:2048 sc1\n\tglobal_load_dwordx4 %3, %12, off offset:3072 sc1\n\t"
                 "global_load_dwordx4 %4, %13, off sc1\n\tglobal_load_dwordx4 %5, %13, off offset:1024 sc1\n\tglobal_load_dwordx4 %6, %13, off offset:2048 sc1\n\tglobal_load_dwordx4 %7, %13, off offset:3072 sc1\n\t"
                 "global_load_dwordx4 %8, %14, off sc1\n\tglobal_load_dwordx4 %9, %14, off offset:1024 sc1\n\tglobal_load_dwordx4 %10, %14, off offset:2048 sc1\n\tglobal_load_dwordx4 %11, %14, off offset:3072 sc1\n\ts_waitcnt vmcnt(0)"
                 :"=&v"(r[0][0]),"=&v"(r[0][1]),"=&v"(r[0][2]),"=&v"(r[0][3]),"=&v"(r[1][0]),"=&v"(r[1][1]),"=&v"(r[1][2]),"=&v"(r[1][3]),"=&v"(r[2][0]),"=&v"(r[2][1]),"=&v"(r[2][2]),"=&v"(r[2][3]):"v"(s0),"v"(s1),"v"(s2):"memory"); }
  u16* op=out+(size_t)(R*256+wr*128+q*32+c)*1024+C*128+wc*64+(g&1)*16+(g>>1)*8;
  #pragma unroll
  for(int p=0;p<4;p++){
    #pragma unroll
    for(int s=0;s<3;s++){ const u32x4 t=r[s][p]; v[2*p]+=f32x4{bflo(t[0]),bfhi(t[0]),bflo(t[1]),bfhi(t[1])}; v[2*p+1]+=f32x4{bflo(t[2]),bfhi(t[2]),bflo(t[3]),bfhi(t[3])}; }
    auto s0=__builtin_amdgcn_permlane16_swap(cvtpk(v[2*p][0],v[2*p][1]),cvtpk(v[2*p+1][0],v[2*p+1][1]),false,false);
    auto s1=__builtin_amdgcn_permlane16_swap(cvtpk(v[2*p][2],v[2*p][3]),cvtpk(v[2*p+1][2],v[2*p+1][3]),false,false);
    const u32x4 o={s0[0],s1[0],s0[1],s1[1]};
    *(u32x4*)(op+(size_t)(p>>1)*16*1024+32*(p&1))=o; }
}
template<int L> DI void k23_loader(const char* Yt,const char* W1,const char* Wq,const char* KV,const char* w2,const char* wo,u16* Ht,int rbk,int cbk,LAS char* lds,unsigned* flags,int rbk2){
  const int lane=lane_id();
  const unsigned lds0=(unsigned)(uintptr_t)lds;
  { float cs0=0.f,cs1=0.f,cs2=0.f,cs3=0.f,cs4=0.f,cs5=0.f,cs6=0.f,cs7=0.f;
    unsigned qa; { const int r=8*L+(lane>>3),pos=lane&7; const int sw=((r>>1)&3)<<1; qa=lds0+22528+r*128+(pos^sw)*16; }
    #define CS_OPS [cs0]"+v"(cs0),[cs1]"+v"(cs1),[cs2]"+v"(cs2),[cs3]"+v"(cs3),[cs4]"+v"(cs4),[cs5]"+v"(cs5),[cs6]"+v"(cs6),[cs7]"+v"(cs7)
    #define CS_INS [qa0]"v"(qa),[qa1]"v"(qa+G1_SLOT),[qa2]"v"(qa+2*G1_SLOT),[qa3]"v"(qa+3*G1_SLOT),[qa4]"v"(qa+4*G1_SLOT),[qa5]"v"(qa+5*G1_SLOT),[c10]"s"(0x00003f80u),[c01]"s"(0x3f800000u)
    if(L<3){
      constexpr int F0=3*L, H0=5*L, Q0=L;
      const rsrc_t rA=mkrsrc(Yt,2048*1024*2), rH=mkrsrc(W1,1024*3584*2), rQ=mkrsrc(Wq,1024*1024*2);
      const unsigned oA=(rbk*8)*32768, oH=cbk*448, oQ=cbk*128;
      unsigned vA[3],vH[5],vQ[2];
      #pragma unroll
      for(int i=0;i<3;i++) vA[i]=lane*16+((F0+i)&7)*32768-i*1024;
      #pragma unroll
      for(int i=0;i<5;i++){ const int e=64*((H0+i)<14?(H0+i):13)+lane; const int r=e/28,pos=e%28; const int ch=pos^(2*((r>>2)&1)); vH[i]=r*7168+ch*16-(i&3)*1024; }
      #pragma unroll
      for(int i=0;i<2;i++){ const int r=8*((Q0+i)&3)+(lane>>3),pos=lane&7; const int sw=((r>>1)&3)<<1; vQ[i]=r*2048+(pos^sw)*16-i*1024; }
      #define L1_CALL(N) asm volatile(L1_##N##_ASM : CS_OPS : CS_INS,[rA]"s"(rA),[rH]"s"(rH),[rQ]"s"(rQ),[oA]"s"(oA),[oH]"s"(oH),[oQ]"s"(oQ),[lds0]"s"(lds0), \
         [vA0]"v"(vA[0]),[vA1]"v"(vA[1]),[vA2]"v"(vA[2]),[vH0]"v"(vH[0]),[vH1]"v"(vH[1]),[vH2]"v"(vH[2]),[vH3]"v"(vH[3]),[vH4]"v"(vH[4]),[vQ0]"v"(vQ[0]),[vQ1]"v"(vQ[1]) : "memory","scc",L1_##N##_CLOB)
      if(L==0) L1_CALL(0); else if(L==1) L1_CALL(1); else L1_CALL(2);
      #undef L1_CALL
    } else {
      const char* pf[4];
      #pragma unroll
      for(int i=0;i<4;i++){ const int t=i*64+lane, id=(t<260?t:259)*16+rbk; const int row=192+id/5, u=id%5;
        pf[i]=(u<4)? W1+(size_t)row*7168+(((cbk*448)&~127)+u*128) : Wq+(size_t)row*2048+cbk*128; }
      asm volatile(L1_3_ASM : CS_OPS : CS_INS,[pf0]"v"(pf[0]),[pf1]"v"(pf[1]),[pf2]"v"(pf[2]),[pf3]"v"(pf[3]) : "memory","scc",L1_3_CLOB);
    }
    #undef CS_OPS
    #undef CS_INS
    asm volatile("s_barrier":::"memory");
    float cs[8]={cs0,cs1,cs2,cs3,cs4,cs5,cs6,cs7};
    #pragma unroll
    for(int i=0;i<8;i++){ float v=cs[i]; v+=__shfl_xor(v,8); v+=__shfl_xor(v,16); v+=__shfl_xor(v,32); cs[i]=v; }
    if(lane<8){ LAS float* o=(LAS float*)(lds+CS_BASE)+L*64+lane*8;
      *(LAS f32x4*)o=f32x4{cs[0],cs[1],cs[2],cs[3]}; *(LAS f32x4*)(o+4)=f32x4{cs[4],cs[5],cs[6],cs[7]}; }
    asm volatile("s_waitcnt lgkmcnt(0)":::"memory"); }
  { const int t=L*64+lane; const int g0=blockIdx.x*576+t, g1=g0+256, g2=blockIdx.x*576+512+lane;
    const char* p0=(g0<114688)? w2+(size_t)g0*64 : wo+(size_t)(g0-114688)*64; const char* p1=(g1<114688)? w2+(size_t)g1*64 : wo+(size_t)(g1-114688)*64;
    const char* p2=(g2<114688)? w2+(size_t)g2*64 : wo+(size_t)(g2-114688)*64;
    const char* pK=KV+L*4096; const unsigned vA=lane*16, ldsk=lds0+AT_BASE; const unsigned* hadd=flags+4096+(rbk>>1)*16; const unsigned* hpoll=flags+4096+rbk2*16;
    const rsrc_t rh=mkrsrc(Ht,2048*3584*2); const unsigned ho=(((rbk*8+(L>>1)*4)*112+cbk*7)*64+lane)*16, hoff=ho+4*(L&1)*1024, hoff2=ho+3*1024+8*(L&1);
    const unsigned hs=lds0+HS_BASE+L*12288+lane*16, hs2=lds0+HS2_BASE+L*2048+lane*8;
    #define ATL_CALL(N) asm volatile(ATL_##N##_ASM :: [pK]"s"(pK),[lds0]"s"(ldsk),[vA]"v"(vA),[p0]"v"(p0),[p1]"v"(p1),[p2]"v"(p2),[hadd]"s"(hadd),[hpoll]"s"(hpoll),[rh]"s"(rh),[hoff]"v"(hoff),[hoff2]"v"(hoff2),[hs]"v"(hs),[hs2]"v"(hs2) : "memory","scc",ATL_##N##_CLOB)
    if(L==0) ATL_CALL(0); else if(L==1) ATL_CALL(1); else if(L==2) ATL_CALL(2); else ATL_CALL(3);
    #undef ATL_CALL
  }
}
DI void k23_compute(int wr,int wc,int rbk,int cbk,const u16* b1,const float* rowstat,const char* KV,LAS char* lds,u16* Ht,u16* Ot){
  int cnt; f32x4 hv[4];
  { const int lane=lane_id(); const int g=lane>>4,q=(lane&15)>>2,p=lane&3;
    { const u16* bp=b1+cbk*224+128*wc+4*g; u32x2 bb[7];
      #pragma unroll
      for(int n=0;n<6;n++) bb[n]=*(const u32x2*)(bp+16*n);
      bb[6]=*(const u32x2*)(b1+cbk*224+96+16*wc+4*g);
      g1_init<0>(bb[0]); g1_init<1>(bb[1]); g1_init<2>(bb[2]); g1_init<3>(bb[3]); g1_init<4>(bb[4]); g1_init<5>(bb[5]);
      g1_zero<6>(); g1_zero<7>();
      #pragma unroll
      for(int m=0;m<4;m++) hv[m]=f32x4{bflo(bb[6][0]),bfhi(bb[6][0]),bflo(bb[6][1]),bfhi(bb[6][1])}; }
    const int lds0=(int)(unsigned)(uintptr_t)lds;
    int aA=lds0+wr*4096+lane*16; const int rowb=lds0+8192+(4*g+q)*448+8*p; const int baseE=rowb+32*(g&1), baseO=rowb-32*(g&1);
    int bE=baseE+256*wc, bO=baseO+256*wc, b8=wc?baseO+224:baseE+192;
    const int sg=(2*g+(q>>1))&3; int bQ0=lds0+22528+(4*g+q)*128+32*((2*wc)^sg)+8*p, bQ1=lds0+22528+(4*g+q)*128+32*((2*wc+1)^sg)+8*p;
    cnt=15; int slot=0, dlt;
    asm volatile(G1_ASM : [aA]"+v"(aA),[bE]"+v"(bE),[bO]"+v"(bO),[bQ0]"+v"(bQ0),[bQ1]"+v"(bQ1),[b8]"+v"(b8),[cnt]"+s"(cnt),[slot]"+s"(slot),[dlt]"=&s"(dlt),
                 "+{v[112:115]}"(hv[0]),"+{v[116:119]}"(hv[1]),"+{v[120:123]}"(hv[2]),"+{v[124:127]}"(hv[3])
                 : [cW]"s"(-(G1_NS-1)*G1_SLOT),[cS]"s"(G1_SLOT) : "memory","scc",G1_CLOB);
  }
  const int lane=lane_id();
  const int rb0=rbk*8+wr*4;
  f32x2 st[4];
  #pragma unroll
  for(int m=0;m<4;m++) st[m]=*(const f32x2*)(rowstat+2*((rb0+m)*16+(lane&15)));
  asm volatile("s_barrier":::"memory");
  { LAS char* hs=lds+HS_BASE+(wr*2+wc)*12288+lane*16; LAS char* hs2=lds+HS2_BASE+(wr*2+wc)*2048+lane*8;
    g1_epi_half(hs2,hv); g1_epi_pair<0>(hs,cnt); g1_epi_pair<2>(hs+4096,cnt); g1_epi_pair<4>(hs+8192,cnt); }
  asm volatile("s_waitcnt lgkmcnt(0)":::"memory");
  asm volatile("s_barrier":::"memory");
  {
    const LAS float* csl=(const LAS float*)(lds+CS_BASE); const int gE=lane>>4; const int col=wc*32+4*gE;
    f32x4 c0=*(const LAS f32x4*)(csl+col), c1=*(const LAS f32x4*)(csl+col+16);
    #pragma unroll
    for(int L=1;L<4;L++){ c0+=*(const LAS f32x4*)(csl+L*64+col); c1+=*(const LAS f32x4*)(csl+L*64+col+16); }
    f32x4 x[4],y[4];
    x[0]=rdacc4<G1A(0,6)>(cnt); y[0]=rdacc4<G1A(0,7)>(cnt); x[1]=rdacc4<G1A(1,6)>(cnt); y[1]=rdacc4<G1A(1,7)>(cnt);
    x[2]=rdacc4<G1A(2,6)>(cnt); y[2]=rdacc4<G1A(2,7)>(cnt); x[3]=rdacc4<G1A(3,6)>(cnt); y[3]=rdacc4<G1A(3,7)>(cnt);
    u32x4 w[4];
    #pragma unroll
    for(int m=0;m<4;m++){ const float s=st[m][0],t=st[m][1];
      w[m][0]=cvtpk(x[m][0]*s+c0[0]*t,x[m][1]*s+c0[1]*t); w[m][1]=cvtpk(x[m][2]*s+c0[2]*t,x[m][3]*s+c0[3]*t);
      w[m][2]=cvtpk(y[m][0]*s+c1[0]*t,y[m][1]*s+c1[1]*t); w[m][3]=cvtpk(y[m][2]*s+c1[2]*t,y[m][3]*s+c1[3]*t); }
    u32x4 z[2][2];
    #pragma unroll
    for(int j=0;j<2;j++)
      #pragma unroll
      for(int d=0;d<4;d++){ auto a=__builtin_amdgcn_permlane16_swap(w[2*j][d],w[2*j+1][d],false,false); auto b=__builtin_amdgcn_permlane32_swap(a[0],a[1],false,false); z[j][0][d]=b[0]; z[j][1][d]=b[1]; }
    PIN(z[0][0]); PIN(z[0][1]); PIN(z[1][0]); PIN(z[1][1]);
    wracc4<56>(z[0][0]); wracc4<60>(z[0][1]); wracc4<64>(z[1][0]); wracc4<68>(z[1][1]);
    { const unsigned on=((lane&15)==((lane>>4)&1))?0x3f803f80u:0u; wracc4<72>(u32x4{on,on,on,on}); }
  }
  int tok;
  { int a0=(int)(unsigned)(uintptr_t)lds+AT_BASE+lane*16, a1=a0+AT_SLOT; int slot=1,dlt; tok=15;
    asm volatile(ATN_ASM : [a0]"+v"(a0),[a1]"+v"(a1),[cnt]"+s"(tok),[slot]"+s"(slot),[dlt]"=&s"(dlt) : [cW]"s"(-3*AT_SLOT),[cS]"s"(AT_SLOT) : "memory","scc",ATN_CLOB); }
  { const int ln=lane_id(); LAS char* os=lds+OS_BASE+(wr*2+wc)*4096+ln*16;
    f32x16 o0=at_rd<0>(tok), o1=at_rd<1>(tok); float l0=at_rowsum(rdacc<32>(tok),rdacc<33>(tok)), l1=at_rowsum(rdacc<36>(tok),rdacc<37>(tok));
    PIN(o0); PIN(o1); PIN(l0); PIN(l1);
    if(__any((int)at_bad(o0,l0)|(int)at_bad(o1,l1))){
      const bf16x8 q00=rdq<56>(), q01=rdq<60>(), q10=rdq<64>(), q11=rdq<68>();
      { f32x16 o; float l; attn_slow(q00,q01,KV,ln,o,l); at_store(os,o,l); }
      { f32x16 o; float l; attn_slow(q10,q11,KV,ln,o,l); at_store(os+2048,o,l); }
    } else { at_store(os,o0,l0); at_store(os+2048,o1,l1); }
    asm volatile("s_waitcnt lgkmcnt(0)":::"memory"); }
}
__global__ __launch_bounds__(512,2) __attribute__((amdgpu_num_vgpr(128))) void k234(const char* __restrict__ Yt,const char* __restrict__ W1,const char* __restrict__ Wq,const u16* __restrict__ b1,
    const float* __restrict__ rowstat,const char* __restrict__ KV,u16* Ht,u16* Ot,const char* __restrict__ w2,const char* __restrict__ wo,
    const u16* __restrict__ x,const u16* __restrict__ b2,u16* __restrict__ out,char* __restrict__ part,unsigned* __restrict__ flags){
  extern __shared__ __attribute__((aligned(16))) char ldsg[];
  LAS char* lds=(LAS char*)ldsg;
  const int wid=__builtin_amdgcn_readfirstlane(threadIdx.x>>6);
  const int b=blockIdx.x,xcd=b&7,j=b>>3;
  const int rbk=(xcd&1)*8+(j&7),cbk=(xcd>>1)*4+(j>>3);
  const int R2=(xcd&1)*4+(j&3),C2=j>>2,q2=xcd>>1;
  const unsigned lds0=(unsigned)(uintptr_t)lds;
  if(wid>=4){
    if(wid==4){ k23_loader<0>(Yt,W1,Wq,KV,w2,wo,Ht,rbk,cbk,lds,flags,R2); g2_loader<0>(Ht,Ot,w2,wo,R2,C2,q2,lds,flags,rbk,cbk); }
    else if(wid==5){ k23_loader<1>(Yt,W1,Wq,KV,w2,wo,Ht,rbk,cbk,lds,flags,R2); g2_loader<1>(Ht,Ot,w2,wo,R2,C2,q2,lds,flags,rbk,cbk); }
    else if(wid==6){ k23_loader<2>(Yt,W1,Wq,KV,w2,wo,Ht,rbk,cbk,lds,flags,R2); g2_loader<2>(Ht,Ot,w2,wo,R2,C2,q2,lds,flags,rbk,cbk); }
    else { k23_loader<3>(Yt,W1,Wq,KV,w2,wo,Ht,rbk,cbk,lds,flags,R2); g2_loader<3>(Ht,Ot,w2,wo,R2,C2,q2,lds,flags,rbk,cbk); }
  } else {
    k23_compute(wid>>1,wid&1,rbk,cbk,b1,rowstat,KV,lds,Ht,Ot);
    g2_compute(wid>>1,wid&1,R2,C2,q2,lds0,part,flags,x,b2,out);
  }
}

extern "C" void kernel_launch(void* const* d_in,const int* in_sizes,int n_in,void* d_out,int out_size,void* d_ws,size_t ws_size,hipStream_t st){
  const u16* x=(const u16*)d_in[0]; const char* wq=(const char*)d_in[1]; const char* wk=(const char*)d_in[2]; const char* wv=(const char*)d_in[3]; const char* wo=(const char*)d_in[4];
  const char* w1=(const char*)d_in[6]; const u16* b1=(const u16*)d_in[7]; const char* w2=(const char*)d_in[8]; const u16* b2=(const u16*)d_in[9];
  char* ws=(char*)d_ws;
  (void)hipFuncSetAttribute((const void*)k1_ln,hipFuncAttributeMaxDynamicSharedMemorySize,K1_LDS);
  (void)hipFuncSetAttribute((const void*)k234,hipFuncAttributeMaxDynamicSharedMemorySize,163840);
  hipLaunchKernelGGL(k1_ln,dim3(256),dim3(1024),K1_LDS,st,x,wk,wv,wq,w1,ws+WS_YT,ws+WS_KV,(float*)(ws+WS_ST),(unsigned*)(ws+WS_FLAG));
  hipLaunchKernelGGL(k234,dim3(256),dim3(512),163840,st,ws+WS_YT,w1,wq,b1,(const float*)(ws+WS_ST),ws+WS_KV,(u16*)(ws+WS_HT),(u16*)(ws+WS_OT),w2,wo,x,b2,(u16*)d_out,ws+WS_PART,(unsigned*)(ws+WS_FLAG));
}
```
